```python
import torch

def _softmax(x, axis=-1):
    return torch.softmax(x, dim=axis)
def _gelu(x):
    return torch.nn.functional.gelu(x, approximate="tanh")
def _silu(x):
    return torch.nn.functional.silu(x)
def _sigmoid(x):
    return torch.sigmoid(x)
def _layernorm(x, axis=-1, eps=1e-5):
    m = x.mean(dim=axis, keepdim=True)
    v = x.var(dim=axis, keepdim=True, unbiased=False)
    return (x - m) / torch.sqrt(v + eps)
def _scatter_add(tgt, idx, src, axis=0):
    return tgt.clone().index_add_(axis, idx, src)
def _segment_sum(src, seg, n):
    out = torch.zeros((n,) + src.shape[1:], dtype=src.dtype)
    return out.index_add_(0, seg, src)
def _pad(x, pw):
    flat = tuple(v for pair in reversed(pw) for v in pair)
    return torch.nn.functional.pad(x, flat)

kernel_name = 'gated_mix__ssm_conv_s5__dense_activation_mish__prompt'

def setup_inputs():

    _w = (lambda a: (lambda t: t.to(torch.bfloat16) if t.is_floating_point() else t)(torch.as_tensor(a)))
    def _A_setup():
        B, S, D = 1, 2048, 640
        P = D
        Lambda_re = -0.5 * (lambda _x: torch.exp(torch.as_tensor(_x)))((lambda *s: torch.randn(*s, dtype=torch.bfloat16) * 0.2)(P))
        Lambda_im = (lambda *s: torch.randn(*s, dtype=torch.bfloat16) * 0.2)(P) * 1.0
        B_re = (lambda *s: torch.randn(*s, dtype=torch.bfloat16) * 0.2)(P, D) * (D ** -0.5)
        B_im = (lambda *s: torch.randn(*s, dtype=torch.bfloat16) * 0.2)(P, D) * (D ** -0.5)
        C_re = (lambda *s: torch.randn(*s, dtype=torch.bfloat16) * 0.2)(D, P) * (P ** -0.5)
        C_im = (lambda *s: torch.randn(*s, dtype=torch.bfloat16) * 0.2)(D, P) * (P ** -0.5)
        log_dt = (lambda *s: torch.randn(*s, dtype=torch.bfloat16) * 0.2)(P) * 0.3 - 2.0
        Dskip = (lambda *s: torch.randn(*s, dtype=torch.bfloat16) * 0.05)(D)
        return {
            "x": _w((lambda *s: torch.randn(*s, dtype=torch.bfloat16) * 0.05)(B, S, D)),
            "Lambda_re": _w(Lambda_re),
            "Lambda_im": _w(Lambda_im),
            "B_re": _w(B_re),
            "B_im": _w(B_im),
            "C_re": _w(C_re),
            "C_im": _w(C_im),
            "log_dt": _w(log_dt),
            "Dskip": _w(Dskip),
        }
    _A_in = _A_setup()

    def _B_setup():
        B, S, D, F = 1, 2048, 640, 1536
        return {
            "x": _w((lambda *s: torch.randn(*s, dtype=torch.bfloat16) * 0.05)(B, S, D)),
            "w1": _w((lambda *s: torch.randn(*s, dtype=torch.bfloat16) * 3.2)(D, F) * (D ** -0.5)),
            "b1": _w((lambda *s: torch.randn(*s, dtype=torch.bfloat16) * 0.05)(F)),
            "w2": _w((lambda *s: torch.randn(*s, dtype=torch.bfloat16) * 0.05)(F, D) * (F ** -0.5)),
        }
    _B_in = _B_setup()

    return {
        "A_x": _A_in["x"],
        "A_Lambda_re": _A_in["Lambda_re"],
        "A_Lambda_im": _A_in["Lambda_im"],
        "A_B_re": _A_in["B_re"],
        "A_B_im": _A_in["B_im"],
        "A_C_re": _A_in["C_re"],
        "A_C_im": _A_in["C_im"],
        "A_log_dt": _A_in["log_dt"],
        "A_Dskip": _A_in["Dskip"],
        "B_x": _B_in["x"],
        "B_w1": _B_in["w1"],
        "B_b1": _B_in["b1"],
        "B_w2": _B_in["w2"],
        "gate_w": _w((lambda *s: torch.randn(*s, dtype=torch.bfloat16) * 0.05)(640) * 16.0),
    }

def reference(A_x, A_Lambda_re, A_Lambda_im, A_B_re, A_B_im, A_C_re, A_C_im, A_log_dt, A_Dskip, B_x, B_w1, B_b1, B_w2, gate_w):
    def _A_ref(x, Lambda_re, Lambda_im, B_re, B_im, C_re, C_im, log_dt, Dskip):
        Bsz, L, H = x.shape
        Lam = Lambda_re + 1j * Lambda_im
        Bmat = B_re + 1j * B_im
        Cmat = C_re + 1j * C_im
        dt = torch.exp(log_dt)
        Lam_bar = torch.exp(dt * Lam)
        B_bar = ((Lam_bar - 1.0) / Lam)[:, None] * Bmat
        Bu = torch.einsum("bld,pd->blp", x + 0j, B_bar)
        h = torch.zeros_like(Bu[:, 0, :])
        hs = []
        for t in range(L):
            h = Lam_bar[None, :] * h + Bu[:, t, :]
            hs.append(h)
        hseq = torch.stack(hs, 1)
        y = 2.0 * torch.einsum("blp,dp->bld", hseq, Cmat).real + x * Dskip[None, None, :]
        return y

    def _B_ref(x, w1, b1, w2):
        h = torch.einsum("bsd,df->bsf", x, w1) + b1
        sp = torch.log1p(torch.exp(-torch.abs(h))) + torch.where(h > 0, h, torch.zeros_like(h))
        act = h * torch.tanh(sp)
        return torch.einsum("bsf,fd->bsd", act, w2)

    x = A_x
    g = _sigmoid((x * gate_w).sum(-1, keepdims=True))
    a = _A_ref(x, A_Lambda_re, A_Lambda_im, A_B_re, A_B_im, A_C_re, A_C_im, A_log_dt, A_Dskip)
    b = _B_ref(x, B_w1, B_b1, B_w2)
    return g * a + (1 - g) * b
```

```cpp
#include <hip/hip_runtime.h>
#include <hip/hip_bf16.h>
typedef __hip_bfloat16 bf16;
__device__ __forceinline__ float b2f(bf16 v){ return __bfloat162float(v); }
constexpr int L=2048, D=640, P=640, F=1536;

__global__ void k_gemm1(const bf16* x, const bf16* Bre, const bf16* Bim, const bf16* w1, const bf16* b1,
                        float* bu, float* act){
  int t = blockIdx.x; __shared__ float xs[D];
  for(int i=threadIdx.x;i<D;i+=blockDim.x) xs[i]=b2f(x[t*D+i]);
  __syncthreads();
  for(int n=threadIdx.x;n<2*P+F;n+=blockDim.x){
    float s=0;
    if(n<P){ for(int k=0;k<D;k++) s+=xs[k]*b2f(Bre[n*D+k]); bu[t*2*P+n]=s; }
    else if(n<2*P){ int p=n-P; for(int k=0;k<D;k++) s+=xs[k]*b2f(Bim[p*D+k]); bu[t*2*P+n]=s; }
    else { int f=n-2*P; for(int k=0;k<D;k++) s+=xs[k]*b2f(w1[k*F+f]); s+=b2f(b1[f]);
      float sp=log1pf(expf(-fabsf(s)))+fmaxf(s,0.f); act[t*F+f]=s*tanhf(sp); }
  }
}
__global__ void k_scan(const bf16* Lre,const bf16* Lim,const bf16* ldt,float* bu){
  int p=blockIdx.x*blockDim.x+threadIdx.x; if(p>=P) return;
  float dt=expf(b2f(ldt[p])); float lr=b2f(Lre[p]), li=b2f(Lim[p]);
  float e=expf(dt*lr); float br=e*cosf(dt*li), bi=e*sinf(dt*li);
  float nr=br-1.f, ni=bi; float den=lr*lr+li*li;
  float cr=(nr*lr+ni*li)/den, ci=(ni*lr-nr*li)/den;
  float hr=0,hi=0;
  for(int t=0;t<L;t++){
    float ur=bu[t*2*P+p], ui=bu[t*2*P+P+p];
    float vr=cr*ur-ci*ui, vi=cr*ui+ci*ur;
    float a=br*hr-bi*hi+vr, b=br*hi+bi*hr+vi; hr=a;hi=b;
    bu[t*2*P+p]=hr; bu[t*2*P+P+p]=hi;
  }
}
__global__ void k_gemm2(const bf16* x,const bf16* Cre,const bf16* Cim,const bf16* Dk,const bf16* w2,const bf16* gw,
                        const float* h,const float* act,float* out){
  int t=blockIdx.x; __shared__ float hs[2*P]; __shared__ float as[F]; __shared__ float red[256];
  for(int i=threadIdx.x;i<2*P;i+=blockDim.x) hs[i]=h[t*2*P+i];
  for(int i=threadIdx.x;i<F;i+=blockDim.x) as[i]=act[t*F+i];
  float gs=0; for(int i=threadIdx.x;i<D;i+=blockDim.x) gs+=b2f(x[t*D+i])*b2f(gw[i]);
  red[threadIdx.x]=gs; __syncthreads();
  for(int s=128;s>0;s>>=1){ if(threadIdx.x<s) red[threadIdx.x]+=red[threadIdx.x+s]; __syncthreads(); }
  float g=1.f/(1.f+expf(-red[0]));
  for(int d=threadIdx.x;d<D;d+=blockDim.x){
    float a=0; for(int p=0;p<P;p++) a+=hs[p]*b2f(Cre[d*P+p])-hs[P+p]*b2f(Cim[d*P+p]);
    a=2.f*a+b2f(x[t*D+d])*b2f(Dk[d]);
    float b=0; for(int f=0;f<F;f++) b+=as[f]*b2f(w2[f*D+d]);
    out[t*D+d]=g*a+(1.f-g)*b;
  }
}
extern "C" void kernel_launch(void* const* d_in, const int* in_sizes, int n_in,
                              void* d_out, int out_size, void* d_ws, size_t ws_size,
                              hipStream_t stream) {
  const bf16* x=(const bf16*)d_in[0]; const bf16* Lre=(const bf16*)d_in[1]; const bf16* Lim=(const bf16*)d_in[2];
  const bf16* Bre=(const bf16*)d_in[3]; const bf16* Bim=(const bf16*)d_in[4]; const bf16* Cre=(const bf16*)d_in[5]; const bf16* Cim=(const bf16*)d_in[6];
  const bf16* ldt=(const bf16*)d_in[7]; const bf16* Dk=(const bf16*)d_in[8]; const bf16* w1=(const bf16*)d_in[10]; const bf16* b1=(const bf16*)d_in[11];
  const bf16* w2=(const bf16*)d_in[12]; const bf16* gw=(const bf16*)d_in[13];
  float* bu=(float*)d_ws; float* act=bu+L*2*P;
  k_gemm1<<<L,256,0,stream>>>(x,Bre,Bim,w1,b1,bu,act);
  k_scan<<<P/64,64,0,stream>>>(Lre,Lim,ldt,bu);
  k_gemm2<<<L,256,0,stream>>>(x,Cre,Cim,Dk,w2,gw,bu,act,(float*)d_out);
}
```

```cpp
#include <hip/hip_runtime.h>
#include <hip/hip_bf16.h>
#include <stdint.h>

typedef __attribute__((ext_vector_type(8))) __bf16 bf16x8;
typedef __attribute__((ext_vector_type(2))) __bf16 bf16x2;
typedef __attribute__((ext_vector_type(4))) float f32x4;
typedef __attribute__((ext_vector_type(2))) float f32x2;
typedef __attribute__((ext_vector_type(4))) unsigned u32x4;
typedef __attribute__((ext_vector_type(2))) unsigned u32x2;
typedef __attribute__((ext_vector_type(4))) short s16x4;
typedef unsigned short u16;
#define LAS __attribute__((address_space(3)))
#define GAS __attribute__((address_space(1)))
#define DI __device__ __forceinline__

#define ROT1 0
#define ROT2M 0
#define ROT2S 0
constexpr int T_ = 2048, D_ = 640, P_ = 640, F_ = 1536;
constexpr int KS_MLP = 48, KS = 88;

constexpr size_t OFF_A    = 0;
constexpr size_t OFF_B    = OFF_A + (size_t)32 * KS * 4096;
constexpr size_t OFF_BP   = OFF_B + (size_t)8 * KS * 5120;
constexpr size_t OFF_E128 = OFF_BP + (size_t)32 * 16 * P_ * 16;
constexpr size_t OFF_EP   = OFF_E128 + (size_t)8 * P_ * 16;
constexpr size_t OFF_PAR  = OFF_EP + (size_t)16 * 4 * P_ * 8;
constexpr size_t OFF_G    = OFF_PAR + (size_t)P_ * 64;
constexpr size_t OFF_FLAG = OFF_G + (size_t)T_ * 4;

#define STAMP(i)
#define STAMP_DECL
#define STAMP_OUT(kid)

DI float bflo(unsigned v) { return __uint_as_float(v << 16); }
DI float bfhi(unsigned v) { return __uint_as_float(v & 0xffff0000u); }
DI unsigned pack2(float a, float b) {
  bf16x2 r; r[0] = (__bf16)a; r[1] = (__bf16)b; return __builtin_bit_cast(unsigned, r);
}
DI float dswap(float v) {
  return __int_as_float(__builtin_amdgcn_mov_dpp(__float_as_int(v), 0xB1, 0xf, 0xf, true));
}
DI float cmul(float lr, float ls, float v) { return fmaf(lr, v, ls * dswap(v)); }
DI float cmuladd(float lr, float ls, float v, float a) { return fmaf(lr, v, fmaf(ls, dswap(v), a)); }
DI f32x2 cm2(f32x2 a, f32x2 b) { return (f32x2){a[0] * b[0] - a[1] * b[1], a[0] * b[1] + a[1] * b[0]}; }
DI f32x2 cfma(f32x2 lrr, f32x2 lni, f32x2 s, f32x2 b) {
  f32x2 sy = __builtin_shufflevector(s, s, 1, 0);
  return __builtin_elementwise_fma(lrr, s, __builtin_elementwise_fma(lni, sy, b));
}

DI float sfma(float a, float b, float c) { float d; asm("v_fma_f32 %0, %1, %2, %3" : "=v"(d) : "v"(a), "v"(b), "v"(c)); return d; }
DI float smul(float a, float b) { float d; asm("v_mul_f32 %0, %1, %2" : "=v"(d) : "v"(a), "v"(b)); return d; }

DI f32x2 mish2(f32x2 h) {
  f32x2 z = h * 1.44269504088896341f;
  f32x2 w; w[0] = __builtin_amdgcn_exp2f(fminf(z[0], 43.f)); w[1] = __builtin_amdgcn_exp2f(fminf(z[1], 43.f));
  f32x2 n = w * (w + 2.f);
  f32x2 d = n + 2.f;
  f32x2 r; r[0] = __builtin_amdgcn_rcpf(d[0]); r[1] = __builtin_amdgcn_rcpf(d[1]);
  return (h * n) * r;
}

DI void glds16(const void* gsrc, unsigned lds_dst) {
  unsigned keep;
  asm volatile("s_mov_b32 %0, m0\n\ts_mov_b32 m0, %2\n\ts_nop 0\n\tglobal_load_lds_dwordx4 %1, off\n\ts_mov_b32 m0, %0"
               : "=&s"(keep) : "v"(gsrc), "s"(lds_dst) : "memory");
}
DI unsigned ld_u16_asm(const void* p) { unsigned r; asm volatile("global_load_ushort %0, %1, off" : "=v"(r) : "v"(p) : "memory"); return r; }
DI unsigned ld_u32_asm(const void* p) { unsigned r; asm volatile("global_load_dword %0, %1, off" : "=v"(r) : "v"(p) : "memory"); return r; }
DI u32x4 ld_u128_asm(const void* p) { u32x4 r; asm volatile("global_load_dwordx4 %0, %1, off" : "=v"(r) : "v"(p) : "memory"); return r; }

DI float sum16(float v) {
  auto r = __builtin_amdgcn_permlane16_swap(__float_as_uint(v), __float_as_uint(v), false, false);
  return __uint_as_float(r[0]) + __uint_as_float(r[1]);
}
DI float sum32(float v) {
  auto r = __builtin_amdgcn_permlane32_swap(__float_as_uint(v), __float_as_uint(v), false, false);
  return __uint_as_float(r[0]) + __uint_as_float(r[1]);
}

constexpr int ST_X = 0, ST_BM = 16384, ST_W1 = 26624, ST_SZ = 38912, NSTG = 4;
constexpr int SP = NSTG * ST_SZ;
constexpr int SP_PAR = SP, SP_B1 = SP + 5120, SP_EC = SP + 5504, SP_CNT = SP + 6784, SP_GW = SP + 6848, K1_LDS = SP + 6848 + 1280;

struct K1Args {
  const u16 *x, *Lre, *Lim, *Bre, *Bim, *Cre, *Cim, *ldt, *w1, *b1, *w2, *gw;
  char* ws;
};

DI void packC_store(char* ws, int id, int lane, u32x4 re, u32x4 im) {
  const int ntile = id / 20, mp = id % 20, n = lane & 15, gg = lane >> 4;
  char* dst = ws + OFF_B + ((size_t)((ntile / 5) * KS + KS_MLP + 2 * mp + (gg >> 1)) * 5 + (ntile % 5)) * 1024 + ((2 * (gg & 1)) * 16 + n) * 16;
#pragma unroll
  for (int h = 0; h < 2; ++h) {
    u32x4 o;
    o[0] = pack2(2.f * bflo(re[2 * h]), -2.f * bflo(im[2 * h]));
    o[1] = pack2(2.f * bfhi(re[2 * h]), -2.f * bfhi(im[2 * h]));
    o[2] = pack2(2.f * bflo(re[2 * h + 1]), -2.f * bflo(im[2 * h + 1]));
    o[3] = pack2(2.f * bfhi(re[2 * h + 1]), -2.f * bfhi(im[2 * h + 1]));
    *(u32x4*)(dst + h * 256) = o;
  }
}

#define SCHED __builtin_amdgcn_sched_barrier(0)
#define WAITBAR(n) do { SCHED; asm volatile("s_waitcnt vmcnt(" #n ") lgkmcnt(0)\n\ts_barrier" ::: "memory"); SCHED; } while (0)

template <bool MLP>
DI void k1_body(const K1Args& a, char* smem, const int wave) {
  const int tid = threadIdx.x, lane = tid & 63;
  const int b = blockIdx.x, grp = b & 7, idx = b >> 3;
  const int rc = 8 * (grp >> 2) + (idx >> 2), j = 4 * (grp & 3) + (idx & 3);
  const int rot = ROT1;
  const int r16 = lane & 15, g = lane >> 4;
  const int tw = wave & 3;
  const unsigned sbase = (unsigned)(size_t)(LAS char*)smem;
  char* ws = a.ws;
  STAMP_DECL
  STAMP(0);

  unsigned pv0, pv1, pv2;
  if constexpr (!MLP) {
    const int ch = 40 * j + (lane < 40 ? lane : 0);
    if (wave == 7) {
      pv0 = ld_u16_asm(a.Lre + ch); pv1 = ld_u16_asm(a.Lim + ch); pv2 = ld_u16_asm(a.ldt + ch);
    }
  }
  const char* src[5]; int inc[5]; unsigned dsto[5];
#pragma unroll
  for (int i = 0; i < 5; ++i) {
    int p = wave + 8 * i; p = p >= 38 ? p - 38 : p;
    dsto[i] = sbase + p * 1024;
    const int pr = p < 16 ? p : p - 16;
    const int row = 8 * pr + (lane >> 3), ch = (lane & 7) ^ ((row >> 1) & 7);
    const u16* px = a.x + (size_t)(128 * rc + row) * D_ + 8 * ch;
    const u16* pb = ((row & 1) ? a.Bim : a.Bre) + (size_t)(40 * j + (row >> 1)) * D_ + 8 * ch;
    const int ci = 64 * (p >= 26 ? p - 26 : 0) + lane, k = ci / 12, cp = ci % 12;
    int c = cp - 2 * ((k >> 3) & 1); c = c < 0 ? c + 12 : c;
    const u16* pw = a.w1 + (size_t)k * F_ + 96 * j + 8 * c;
    inc[i] = p < 26 ? 128 : 64 * F_ * 2;
    src[i] = (const char*)(p < 16 ? px : p < 26 ? pb : pw) + (size_t)rot * inc[i];
  }
  int kiss = rot;
  auto stage = [&](unsigned boff) {
    const bool wrap = kiss == 9;
    kiss = wrap ? 0 : kiss + 1;
#pragma unroll
    for (int i = 0; i < 5; ++i) {
      glds16(src[i], dsto[i] + boff);
      src[i] += wrap ? -9 * inc[i] : inc[i];
    }
  };
  int dl = 1;
  auto piece = [&](int i, unsigned boff) {
    if (i == 0) { dl = kiss == 9 ? -9 : 1; kiss = kiss == 9 ? 0 : kiss + 1; }
    SCHED;
    glds16(src[i], dsto[i] + boff);
    src[i] += dl * inc[i];
    SCHED;
  };
  stage(0);
  asm volatile("s_barrier" ::: "memory");
  STAMP(1);

  {
    const int idc = wave == 1 ? 3 * b : wave == 2 ? 3 * b + 1 : wave == 5 ? 3 * b + 2 : 768 + b;
    const int ic = idc < 800 ? idc : 0;
    const size_t co = (size_t)(16 * (ic / 20) + r16) * P_ + 32 * (ic % 20) + 8 * g;
    if (MLP && wave == 0) {
      const int ub = b < 240 ? b : 0, ks = ub / 5, m = ub % 5;
      const u16* p = a.w2 + (size_t)(32 * ks + 4 * g) * D_ + 128 * m + 8 * r16;
      u32x4 R[8];
#pragma unroll
      for (int jj = 0; jj < 8; ++jj) R[jj] = ld_u128_asm(p + (size_t)((jj & 3) + 16 * (jj >> 2)) * D_);
      stage(ST_SZ); stage(2 * ST_SZ);
      asm volatile("s_waitcnt vmcnt(10)" : "+v"(R[0]), "+v"(R[1]), "+v"(R[2]), "+v"(R[3]), "+v"(R[4]), "+v"(R[5]), "+v"(R[6]), "+v"(R[7]) :: "memory");
      if (b < 240) {
        const int n0 = 128 * m + 8 * r16, q = n0 / 80, nt = (n0 % 80) / 16;
        char* dst = ws + OFF_B + ((size_t)(q * KS + ks) * 5 + nt) * 1024 + (g * 16 + (n0 & 15)) * 16;
#pragma unroll
        for (int e = 0; e < 8; ++e) {
          u32x4 o;
#pragma unroll
          for (int d = 0; d < 4; ++d) o[d] = __builtin_amdgcn_perm(R[2 * d + 1][e >> 1], R[2 * d][e >> 1], (e & 1) ? 0x07060302u : 0x05040100u);
          *(u32x4*)(dst + e * 16) = o;
        }
      }
    } else if (MLP && wave == 3) {
      unsigned v = ld_u32_asm(a.b1 + 96 * j + 2 * (lane < 48 ? lane : 0));
      u32x4 g0 = ld_u128_asm(a.gw + 8 * lane), g1 = ld_u128_asm(a.gw + 8 * (64 + (lane & 15)));
      stage(ST_SZ); stage(2 * ST_SZ);
      asm volatile("s_waitcnt vmcnt(10)" : "+v"(v), "+v"(g0), "+v"(g1) :: "memory");
      if (lane < 48) *(f32x2*)(smem + SP_B1 + lane * 8) = (f32x2){bflo(v), bfhi(v)};
      *(u32x4*)(smem + SP_GW + lane * 16) = g0;
      if (lane < 16) *(u32x4*)(smem + SP_GW + 1024 + lane * 16) = g1;
      if (lane == 0) *(unsigned*)(smem + SP_CNT) = 0u;
    } else if (!MLP && wave == 7) {
      stage(ST_SZ); stage(2 * ST_SZ);
      asm volatile("s_waitcnt vmcnt(15)" : "+v"(pv0), "+v"(pv1), "+v"(pv2) :: "memory");
      const int ch = 40 * j + (lane < 40 ? lane : 0);
      const float lre = bflo(pv0), lim = bflo(pv1), dt = expf(bflo(pv2));
      float e = expf(dt * lre), sn, cs;
      sincosf(dt * lim, &sn, &cs);
      f32x2 l1 = {e * cs, e * sn};
      const float nr = l1[0] - 1.f, ni = l1[1], den = lre * lre + lim * lim;
      f32x2 cf = {(nr * lre + ni * lim) / den, (ni * lre - nr * lim) / den};
      f32x2 l2 = cm2(l1, l1), l4 = cm2(l2, l2), l8 = cm2(l4, l4), l12 = cm2(l8, l4), l16 = cm2(l8, l8);
      f32x2 l32 = cm2(l16, l16), l64 = cm2(l32, l32), l128 = cm2(l64, l64);
      if (lane < 40) {
        float* pp = (float*)(smem + SP_PAR + lane * 128);
        *(f32x4*)(pp) = (f32x4){l1[0], l1[1], l16[0], l16[1]};
        *(f32x4*)(pp + 4) = (f32x4){cf[0], cf[1], l32[0], l32[1]};
        *(f32x4*)(pp + 8) = (f32x4){l12[0], l12[1], l8[0], l8[1]};
        *(f32x4*)(pp + 12) = (f32x4){l4[0], l4[1], 1.f, 0.f};
        *(f32x2*)(pp + 16) = l64;
        if (rc == 0) {
          float* gp = (float*)(ws + OFF_PAR + (size_t)ch * 64);
          const f32x2 l96 = cm2(l64, l32);
          *(f32x4*)gp = (f32x4){l1[0], l1[1], l128[0], l128[1]};
          *(f32x4*)(gp + 4) = (f32x4){1.f, 0.f, l32[0], l32[1]};
          *(f32x4*)(gp + 8) = (f32x4){l64[0], l64[1], l96[0], l96[1]};
        }
      }
    } else if (wave == 4) {
      stage(ST_SZ); stage(2 * ST_SZ);
      if (lane == 0) *(unsigned*)(ws + OFF_FLAG + (size_t)b * 128) = 0u;
    } else {
      u32x4 re = ld_u128_asm(a.Cre + co), im = ld_u128_asm(a.Cim + co);
      stage(ST_SZ); stage(2 * ST_SZ);
      asm volatile("s_waitcnt vmcnt(10)" : "+v"(re), "+v"(im) :: "memory");
      if (idc < 800) packC_store(ws, idc, lane, re, im);
    }
  }
  STAMP(3);

  const unsigned xo0 = ST_X + (32 * tw + r16) * 128 + ((g ^ (r16 >> 1)) * 16);
  const unsigned xo1 = xo0 ^ 64;

#define K1_PIPE(LOADF, STEP, MMA, INIT)                                             \
    WAITBAR(10);                                                                    \
    STAMP(4);                                                                       \
    INIT;                                                                           \
    LOADF(F0, 0u, 0);                                                               \
    STEP(F0, F1, 0u, 1, 3, 3 * ST_SZ);                                              \
    _Pragma("unroll 1") for (int s = 0; s < 6; ++s) {                               \
      WAITBAR(10);                                                                  \
      STEP(F1, F0, ((s + 1) & 3) * ST_SZ, 0, 1, (s & 3) * ST_SZ);                   \
      STEP(F0, F1, ((s + 1) & 3) * ST_SZ, 1, 2, (s & 3) * ST_SZ);                   \
    }                                                                               \
    WAITBAR(10); STAMP(11); STEP(F1, F0, 3 * ST_SZ, 0, 0, 0u); STEP(F0, F1, 3 * ST_SZ, 1, 0, 0u); \
    WAITBAR(5);  STAMP(12); STEP(F1, F0, 0 * ST_SZ, 0, 0, 0u); STEP(F0, F1, 0 * ST_SZ, 1, 0, 0u); \
    WAITBAR(0);  STAMP(13); STEP(F1, F0, 1 * ST_SZ, 0, 0, 0u); STEP(F0, F1, 1 * ST_SZ, 1, 0, 0u); \
    MMA(F1); SCHED;

  if constexpr (MLP) {
    f32x4 acc[6][2], gac;
    const int q4 = r16 >> 2, p4 = r16 & 3;
    const unsigned wbase = ST_W1 + (8 * g + q4) * 192 + (p4 >> 1) * 16 + (p4 & 1) * 8;
    const unsigned wa = wbase + (g & 1) * 32;
    const unsigned wb = wbase + ((g & 1) ? 0 : 160);
    const bool do_gate = j < 8 && (j >> 1) == tw;
    int kc = 2 * rot;
    struct Frag { bf16x8 x[2], w[6], gx, gf; };
    Frag F0, F1;
    auto loadf = [&](Frag& F, unsigned boff, int kk) {
      const char* sb = smem + boff;
#pragma unroll
      for (int tt = 0; tt < 2; ++tt) F.x[tt] = *(const bf16x8*)(sb + (kk ? xo1 : xo0) + tt * 2048);
#pragma unroll
      for (int ft = 0; ft < 6; ++ft) {
        unsigned ad = (ft < 5 ? wa + 32 * ft : wb) + kk * 32 * 192;
        s16x4 lo = __builtin_amdgcn_ds_read_tr16_b64_v4i16((LAS s16x4*)(sb + ad));
        s16x4 hi = __builtin_amdgcn_ds_read_tr16_b64_v4i16((LAS s16x4*)(sb + ad + 4 * 192));
        F.w[ft] = __builtin_bit_cast(bf16x8, __builtin_shufflevector(lo, hi, 0, 1, 2, 3, 4, 5, 6, 7));
      }
      if (do_gate) {
        F.gf = *(const bf16x8*)(smem + SP_GW + kc * 64 + g * 16);
        F.gx = *(const bf16x8*)(sb + (kk ? xo1 : xo0) + (j & 1) * 2048);
      }
      kc = kc == 19 ? 0 : kc + 1;
    };
    auto mma = [&](Frag& F) {
#pragma unroll
      for (int ft = 0; ft < 6; ++ft)
#pragma unroll
        for (int tt = 0; tt < 2; ++tt)
          acc[ft][tt] = __builtin_amdgcn_mfma_f32_16x16x32_bf16(F.w[ft], F.x[tt], acc[ft][tt], 0, 0, 0);
      if (do_gate) gac = __builtin_amdgcn_mfma_f32_16x16x32_bf16(F.gf, F.gx, gac, 0, 0, 0);
    };
    auto step = [&](Frag& C, Frag& N, unsigned boff, const int kk, const int ph, unsigned dboff) {
      const char* sb = smem + boff;
#pragma unroll
      for (int m = 0; m < 12; ++m) {
        SCHED;
        acc[m >> 1][m & 1] = __builtin_amdgcn_mfma_f32_16x16x32_bf16(C.w[m >> 1], C.x[m & 1], acc[m >> 1][m & 1], 0, 0, 0);
        SCHED;
        if (m < 2) N.x[m] = *(const bf16x8*)(sb + (kk ? xo1 : xo0) + m * 2048);
        if (m < 6) {
          unsigned ad = (m < 5 ? wa + 32 * m : wb) + kk * 32 * 192;
          s16x4 lo = __builtin_amdgcn_ds_read_tr16_b64_v4i16((LAS s16x4*)(sb + ad));
          s16x4 hi = __builtin_amdgcn_ds_read_tr16_b64_v4i16((LAS s16x4*)(sb + ad + 4 * 192));
          N.w[m] = __builtin_bit_cast(bf16x8, __builtin_shufflevector(lo, hi, 0, 1, 2, 3, 4, 5, 6, 7));
        }
        if (m == 6 && do_gate) {
          gac = __builtin_amdgcn_mfma_f32_16x16x32_bf16(C.gf, C.gx, gac, 0, 0, 0);
          N.gf = *(const bf16x8*)(smem + SP_GW + kc * 64 + g * 16);
          N.gx = *(const bf16x8*)(sb + (kk ? xo1 : xo0) + (j & 1) * 2048);
        }
        if (ph == 1 && (m == 6 || m == 8 || m == 10)) piece((m - 6) >> 1, dboff);
        if (ph == 2 && (m == 6 || m == 8)) piece(3 + ((m - 6) >> 1), dboff);
        if (ph == 3 && m >= 6 && m < 11) piece(m - 6, dboff);
      }
      kc = kc == 19 ? 0 : kc + 1;
      SCHED;
    };
    auto init = [&]() {
#pragma unroll
      for (int ft = 0; ft < 6; ++ft) { f32x4 bv = *(const f32x4*)(smem + SP_B1 + (16 * ft + 4 * g) * 4); acc[ft][0] = bv; acc[ft][1] = bv; }
      gac = (f32x4){0.f, 0.f, 0.f, 0.f};
    };
    K1_PIPE(loadf, step, mma, init());
    STAMP(14);
    const int c = 2 * rc + (tw >> 1);
    if (do_gate && g == 0) *(float*)(ws + OFF_G + (size_t)(128 * rc + 16 * j + r16) * 4) = 1.f / (1.f + expf(-gac[0]));
#pragma unroll
    for (int tt = 0; tt < 2; ++tt) {
#pragma unroll
      for (int fp = 0; fp < 3; ++fp) {
        f32x4 a0 = acc[2 * fp][tt], a1 = acc[2 * fp + 1][tt];
        f32x2 m0 = mish2((f32x2){a0[0], a0[1]}), m1 = mish2((f32x2){a0[2], a0[3]});
        f32x2 m2 = mish2((f32x2){a1[0], a1[1]}), m3 = mish2((f32x2){a1[2], a1[3]});
        u32x4 o;
        o[0] = pack2(m0[0], m0[1]); o[1] = pack2(m1[0], m1[1]); o[2] = pack2(m2[0], m2[1]); o[3] = pack2(m3[0], m3[1]);
        int ks = 3 * j + fp, mt = 2 * (tw & 1) + tt;
        *(u32x4*)(ws + OFF_A + ((size_t)(c * KS + ks) * 4 + mt) * 1024 + lane * 16) = o;
      }
    }
  } else {
    f32x4 acc[2][5];
    const unsigned bo0 = ST_BM + r16 * 128 + ((g ^ (r16 >> 1)) * 16);
    const unsigned bo1 = bo0 ^ 64;
    struct Frag { bf16x8 x[2], w[5]; };
    Frag F0, F1;
    auto loadf = [&](Frag& F, unsigned boff, int kk) {
      const char* sb = smem + boff;
#pragma unroll
      for (int tt = 0; tt < 2; ++tt) F.x[tt] = *(const bf16x8*)(sb + (kk ? xo1 : xo0) + tt * 2048);
#pragma unroll
      for (int nt = 0; nt < 5; ++nt) F.w[nt] = *(const bf16x8*)(sb + (kk ? bo1 : bo0) + nt * 2048);
    };
    auto mma = [&](Frag& F) {
#pragma unroll
      for (int nt = 0; nt < 5; ++nt)
#pragma unroll
        for (int tt = 0; tt < 2; ++tt)
          acc[tt][nt] = __builtin_amdgcn_mfma_f32_16x16x32_bf16(F.x[tt], F.w[nt], acc[tt][nt], 0, 0, 0);
    };
    auto step = [&](Frag& C, Frag& N, unsigned boff, const int kk, const int ph, unsigned dboff) {
      const char* sb = smem + boff;
#pragma unroll
      for (int m = 0; m < 10; ++m) {
        SCHED;
        acc[m & 1][m >> 1] = __builtin_amdgcn_mfma_f32_16x16x32_bf16(C.x[m & 1], C.w[m >> 1], acc[m & 1][m >> 1], 0, 0, 0);
        SCHED;
        if (m < 2) N.x[m] = *(const bf16x8*)(sb + (kk ? xo1 : xo0) + m * 2048);
        if (m < 5) N.w[m] = *(const bf16x8*)(sb + (kk ? bo1 : bo0) + m * 2048);
        if (ph == 1 && (m == 5 || m == 7)) piece((m - 5) >> 1, dboff);
        if (ph == 2 && (m == 4 || m == 6 || m == 8)) piece(2 + ((m - 4) >> 1), dboff);
        if (ph == 3 && m >= 5) piece(m - 5, dboff);
      }
      SCHED;
    };
    float l1r[5], l1s[5], l16r[5], l16s[5], wgr[5], wgs[5], cfr[5], cfs[5];
    auto init = [&]() {
#pragma unroll
      for (int tt = 0; tt < 2; ++tt)
#pragma unroll
        for (int nt = 0; nt < 5; ++nt) acc[tt][nt] = (f32x4){0.f, 0.f, 0.f, 0.f};
      const float sgn = (r16 & 1) ? 1.f : -1.f;
#pragma unroll
      for (int nt = 0; nt < 5; ++nt) {
        const float* pp = (const float*)(smem + SP_PAR + (8 * nt + (r16 >> 1)) * 128);
        const f32x4 p0 = *(const f32x4*)pp;
        const f32x2 cf = *(const f32x2*)(pp + 4);
        const f32x2 wg = *(const f32x2*)(pp + 8 + 2 * g);
        l1r[nt] = p0[0]; l1s[nt] = sgn * p0[1]; l16r[nt] = p0[2]; l16s[nt] = sgn * p0[3];
        wgr[nt] = wg[0]; wgs[nt] = sgn * wg[1]; cfr[nt] = cf[0]; cfs[nt] = sgn * cf[1];
      }
    };
    K1_PIPE(loadf, step, mma, init());
    STAMP(14);
    const int c = 2 * rc + (tw >> 1);
    char* bp = ws + OFF_BP + ((size_t)(c * 16 + 8 * (tw & 1) + g) * 2 * P_ + 80 * j + r16) * 8;
    float bu[2][5][4];
#pragma unroll
    for (int tt = 0; tt < 2; ++tt)
#pragma unroll
      for (int nt = 0; nt < 5; ++nt) {
#pragma unroll
        for (int r = 0; r < 4; ++r) bu[tt][nt][r] = cmul(cfr[nt], cfs[nt], acc[tt][nt][r]);
        u32x2 o; o[0] = pack2(bu[tt][nt][0], bu[tt][nt][1]); o[1] = pack2(bu[tt][nt][2], bu[tt][nt][3]);
        *(u32x2*)(bp + ((size_t)4 * tt * 2 * P_ + 16 * nt) * 8) = o;
      }
    float h[2][5];
#pragma unroll
    for (int tt = 0; tt < 2; ++tt)
#pragma unroll
      for (int nt = 0; nt < 5; ++nt) h[tt][nt] = bu[tt][nt][0];
#pragma unroll
    for (int r = 1; r < 4; ++r)
#pragma unroll
      for (int tt = 0; tt < 2; ++tt)
#pragma unroll
        for (int nt = 0; nt < 5; ++nt) h[tt][nt] = cmuladd(l1r[nt], l1s[nt], h[tt][nt], bu[tt][nt][r]);
    float ee[5];
#pragma unroll
    for (int nt = 0; nt < 5; ++nt) ee[nt] = cmuladd(l16r[nt], l16s[nt], h[0][nt], h[1][nt]);
#pragma unroll
    for (int nt = 0; nt < 5; ++nt) ee[nt] = cmul(wgr[nt], wgs[nt], ee[nt]);
#pragma unroll
    for (int nt = 0; nt < 5; ++nt) ee[nt] = sum16(ee[nt]);
#pragma unroll
    for (int nt = 0; nt < 5; ++nt) ee[nt] = sum32(ee[nt]);
    if (g == 0) {
#pragma unroll
      for (int nt = 0; nt < 5; ++nt) *(float*)(smem + SP_EC + (tw * 80 + 16 * nt + r16) * 4) = ee[nt];
    }
    asm volatile("s_waitcnt lgkmcnt(0)" ::: "memory");
    unsigned old = 0;
    if (lane == 0) old = __hip_atomic_fetch_add((LAS unsigned*)(smem + SP_CNT), 1u, __ATOMIC_RELAXED, __HIP_MEMORY_SCOPE_WORKGROUP);
    old = __builtin_amdgcn_readfirstlane(old);
    asm volatile("" ::: "memory");
    if (old == 3) {
#pragma unroll
      for (int rnd = 0; rnd < 2; ++rnd) {
        const int col = rnd == 0 ? lane : 64 + (lane & 15);
        const int chl = col >> 1;
        const float sg = (col & 1) ? 1.f : -1.f;
        const float* pp = (const float*)(smem + SP_PAR + chl * 128);
        const f32x4 p1 = *(const f32x4*)(pp + 4);
        const float* ec = (const float*)(smem + SP_EC) + col;
        const float e1 = ec[0];
        const float e2 = cmuladd(p1[2], sg * p1[3], e1, ec[80]);
        const float e3 = cmuladd(p1[2], sg * p1[3], e2, ec[160]);
        const float e4 = cmuladd(p1[2], sg * p1[3], e3, ec[240]);
        const float o1 = e1, o2 = e2, o3 = e3, o4 = e4;
        if (rnd == 0 || lane < 16) {
          const int ch = 40 * j + chl;
          float* ep = (float*)(ws + OFF_EP + ((size_t)rc * 4 * P_ + ch) * 8) + (col & 1);
          ep[0] = 0.f; ep[2 * P_] = o1; ep[4 * P_] = o2; ep[6 * P_] = o3;
          *(float*)(ws + OFF_E128 + ((size_t)(rc >> 1) * P_ + ch) * 16 + (rc & 1) * 8 + (col & 1) * 4) = o4;
        }
      }
    }
  }
  STAMP(15);
  STAMP(16);
  STAMP_OUT(0);
}

__global__ __launch_bounds__(512, 2) void k1(K1Args a) {
  __shared__ __attribute__((aligned(16))) char smem[K1_LDS];
  const int wave = __builtin_amdgcn_readfirstlane(threadIdx.x >> 6);
  if (wave < 4) k1_body<true>(a, smem, wave);
  else k1_body<false>(a, smem, wave);
}

constexpr int RED_S = 84;
constexpr int RED_BYTES = 64 * RED_S * 4;
constexpr int HR = 352;
constexpr int K2_H = 4 * RED_BYTES;
constexpr int K2_FLG = K2_H + 64 * HR;
constexpr int K2_LDS = K2_FLG + 64;
constexpr int NB = 4;

__global__ __launch_bounds__(512, 2) void k2(const u16* __restrict__ x, const u16* __restrict__ Dsk,
    char* ws, float* __restrict__ out) {
  __shared__ __attribute__((aligned(16))) char smem[K2_LDS];
  const int tid = threadIdx.x, lane = tid & 63;
  const int wave = __builtin_amdgcn_readfirstlane(tid >> 6);
  const int b = blockIdx.x, grp = b & 7, idx = b >> 3;
  const int c = 8 * (grp >> 1) + (idx >> 2), q = 4 * (grp & 1) + (idx & 3);
  const int rotm = ROT2M, rots = ROT2S;
  const float* gvec = (const float*)(ws + OFF_G) + 64 * c;
  volatile LAS unsigned* flg = (volatile LAS unsigned*)(smem + K2_FLG);
  GAS unsigned* gflags = (GAS unsigned*)(ws + OFF_FLAG + (size_t)c * 8 * 128);
  const __amdgpu_buffer_rsrc_t rA = __builtin_amdgcn_make_buffer_rsrc((void*)(ws + OFF_A + (size_t)c * KS * 4096), 0, KS * 4096, 0x00020000);
  STAMP_DECL
  STAMP(0);
  if (wave < 4) {
    asm volatile("s_barrier" ::: "memory");
    const __amdgpu_buffer_rsrc_t rB = __builtin_amdgcn_make_buffer_rsrc((void*)(ws + OFF_B + (size_t)q * KS * 5120), 0, KS * 5120, 0x00020000);
    const unsigned va = lane * 16 + wave * 4096, vb = lane * 16 + wave * 5120;
    u32x4 Ab[NB][4], Bb[NB][5];
    f32x4 acc[4][5], gq[4];
    unsigned fl = 0;
#pragma unroll
    for (int mt = 0; mt < 4; ++mt)
#pragma unroll
      for (int nt = 0; nt < 5; ++nt) acc[mt][nt] = (f32x4){0.f, 0.f, 0.f, 0.f};
    auto kq_of = [&](int i) {
      int kq;
      if (i < 12) { kq = i + rotm; kq = kq >= 12 ? kq - 12 : kq; }
      else { kq = i - 12 + rots; kq = 12 + (kq >= 10 ? kq - 10 : kq); }
      return kq;
    };
#define LOAD1(i, kq, e)                                                                                        \
    do {                                                                                                       \
      if ((e) < 5) Bb[(i) % NB][(e)] = __builtin_amdgcn_raw_buffer_load_b128(rB, vb + (e) * 1024, (kq) * 20480, 0); \
      else if ((i) < 12) Ab[(i) % NB][(e) - 5] = __builtin_amdgcn_raw_buffer_load_b128(rA, va + ((e) - 5) * 1024, (kq) * 16384, 0); \
      else Ab[(i) % NB][(e) - 5] = __builtin_amdgcn_raw_buffer_load_b128(rA, va + ((e) - 5) * 1024, (kq) * 16384, 16); \
    } while (0)
#pragma unroll
    for (int i = 0; i < NB - 1; ++i) {
      const int kq = kq_of(i);
#pragma unroll
      for (int e = 0; e < 9; ++e) LOAD1(i, kq, e);
    }
#pragma unroll
    for (int i = 0; i < 22; ++i) {
      const int in = i + NB - 1;
      const int kq = kq_of(in < 22 ? in : 0);
      if (in == 10) {
        int tz = threadIdx.x & 7; asm volatile("" : "+v"(tz));
        fl = __hip_atomic_load(gflags + tz * 32, __ATOMIC_RELAXED, __HIP_MEMORY_SCOPE_AGENT);
      }
      if (in == 12) {
        for (int sp = 0; !__all(fl == 1u) && sp < (1 << 20); ++sp) {
          __builtin_amdgcn_s_sleep(1);
          int tz = threadIdx.x & 7; asm volatile("" : "+v"(tz));
          fl = __hip_atomic_load(gflags + tz * 32, __ATOMIC_RELAXED, __HIP_MEMORY_SCOPE_AGENT);
        }
        __builtin_amdgcn_fence(__ATOMIC_ACQUIRE, "wavefront");
        asm volatile("" ::: "memory");
        STAMP(10);
      }
#pragma unroll
      for (int m = 0; m < 20; ++m) {
        SCHED;
        acc[m / 5][m % 5] = __builtin_amdgcn_mfma_f32_16x16x32_bf16(__builtin_bit_cast(bf16x8, Ab[i % NB][m / 5]),
                                                                    __builtin_bit_cast(bf16x8, Bb[i % NB][m % 5]), acc[m / 5][m % 5], 0, 0, 0);
        SCHED;
        if (in < 22 && (m & 1) == 1 && m < 18) LOAD1(in, kq, m >> 1);
      }
      SCHED;
      if (i == 0) STAMP(1);
      if (i == 4) STAMP(2);
      if (i == 8) STAMP(3);
      if (i == 11) STAMP(4);
      if (i == 12) STAMP(5);
      if (i == 16) STAMP(6);
      if (i == 21) STAMP(7);
      if (i == 9) {
        int tz = threadIdx.x; asm volatile("" : "+v"(tz));
#pragma unroll
        for (int mt = 0; mt < 4; ++mt) gq[mt] = *(const f32x4*)(gvec + 16 * mt + 4 * ((tz >> 4) & 3));
      }
      if (i == 11) {
#pragma unroll
        for (int mt = 0; mt < 4; ++mt)
#pragma unroll
          for (int r = 0; r < 4; ++r) {
            const float og = 1.f - gq[mt][r];
#pragma unroll
            for (int nt = 0; nt < 5; ++nt) acc[mt][nt][r] *= og;
          }
      }
    }
#undef LOAD1
    int tz = threadIdx.x; asm volatile("" : "+v"(tz));
    float* red = (float*)(smem + wave * RED_BYTES) + 4 * ((tz >> 4) & 3) * RED_S + (tz & 15);
#pragma unroll
    for (int mt = 0; mt < 4; ++mt)
#pragma unroll
      for (int nt = 0; nt < 5; ++nt)
#pragma unroll
        for (int r = 0; r < 4; ++r) red[(16 * mt + r) * RED_S + 16 * nt] = acc[mt][nt][r];
    STAMP(23);
    __syncthreads();
    STAMP(24);
  } else {
    const int u = wave - 4;
    if (u < 3) {
      __builtin_amdgcn_s_setprio(3);
      int L = 64 * u + lane; L = L < 160 ? L : 159;
      const int hf = L >= 80 ? 1 : 0, chl = L - 80 * hf, ch = 80 * q + chl;
      const int rc = c >> 1, si_ = 2 * (c & 1) + hf;
      const f32x4 pa = *(const f32x4*)(ws + OFF_PAR + (size_t)ch * 64);
      const f32x2 pw = *(const f32x2*)(ws + OFF_PAR + (size_t)ch * 64 + 16 + si_ * 8);
      const f32x2 ep = *(const f32x2*)(ws + OFF_EP + ((size_t)(rc * 4 + si_) * P_ + ch) * 8);
      f32x4 ev[8];
#pragma unroll
      for (int rp = 0; rp < 8; ++rp) {
        ev[rp] = (f32x4){0.f, 0.f, 0.f, 0.f};
        if (2 * rp < rc) ev[rp] = *(const f32x4*)(ws + OFF_E128 + ((size_t)rp * P_ + ch) * 16);
      }
      u32x4 bq[8]; f32x4 gv[8];
#pragma unroll
      for (int i = 0; i < 8; ++i) bq[i] = *(const u32x4*)(ws + OFF_BP + ((size_t)(c * 16 + 8 * hf + i) * P_ + ch) * 16);
#pragma unroll
      for (int i = 0; i < 8; ++i) gv[i] = *(const f32x4*)(gvec + 32 * hf + 4 * i);
      if (tid == 256) { flg[0] = 0u; flg[1] = 0u; }
      __builtin_amdgcn_s_setprio(0);
      asm volatile("s_waitcnt lgkmcnt(0)\n\ts_barrier" ::: "memory");
      float sr = 0.f, si = 0.f;
      {
        const float lr = pa[2], li = pa[3], nli = -pa[3];
#pragma unroll
        for (int rp = 0; rp < 8; ++rp) {
          if (2 * rp < rc) { const float tr = sfma(nli, si, ev[rp][0]), ti = sfma(li, sr, ev[rp][1]); sr = sfma(lr, sr, tr); si = sfma(lr, si, ti); }
          if (2 * rp + 1 < rc) { const float tr = sfma(nli, si, ev[rp][2]), ti = sfma(li, sr, ev[rp][3]); sr = sfma(lr, sr, tr); si = sfma(lr, si, ti); }
        }
        const float tr = sfma(-pw[1], si, ep[0]), ti = sfma(pw[1], sr, ep[1]);
        sr = sfma(pw[0], sr, tr); si = sfma(pw[0], si, ti);
      }
      STAMP(1);
      const float lr = pa[0], li = pa[1], nli = -pa[1];
      char* wp = smem + K2_H + 32 * hf * HR + chl * 4;
#pragma unroll
      for (int t = 0; t < 32; ++t) {
        const unsigned wr = bq[t >> 2][(t >> 1) & 1], wi = bq[t >> 2][2 + ((t >> 1) & 1)];
        const float tr = sfma(nli, si, (t & 1) ? bfhi(wr) : bflo(wr)), ti = sfma(li, sr, (t & 1) ? bfhi(wi) : bflo(wi));
        sr = sfma(lr, sr, tr); si = sfma(lr, si, ti);
        const float gt = gv[t >> 2][t & 3];
        *(unsigned*)(wp + t * HR) = pack2(smul(sr, gt), smul(si, gt));
      }
      asm volatile("s_waitcnt lgkmcnt(0)" ::: "memory");
      if (lane == 0) __hip_atomic_fetch_add((LAS unsigned*)(smem + K2_FLG), 1u, __ATOMIC_RELAXED, __HIP_MEMORY_SCOPE_WORKGROUP);
      STAMP(2);
    } else {
      asm volatile("s_barrier" ::: "memory");
    }
    for (int sp = 0; flg[0] < 3u && sp < (1 << 22); ++sp) __builtin_amdgcn_s_sleep(1);
    asm volatile("" ::: "memory");
    __builtin_amdgcn_s_setprio(3);
    {
      const char* hp = smem + K2_H + (16 * u + (lane & 15)) * HR + (lane >> 4) * 16;
#pragma unroll
      for (int kl = 0; kl < 5; ++kl) {
        const u32x4 fr = *(const u32x4*)(hp + kl * 64);
        __builtin_amdgcn_raw_buffer_store_b128(fr, rA, lane * 16 + u * 1024, (KS_MLP + 5 * q + kl) * 4096, 16);
      }
    }
    STAMP(3);
    asm volatile("s_waitcnt vmcnt(0)" ::: "memory");
    STAMP(5);
    unsigned old = 0;
    if (lane == 0) old = __hip_atomic_fetch_add((LAS unsigned*)(smem + K2_FLG) + 1, 1u, __ATOMIC_RELAXED, __HIP_MEMORY_SCOPE_WORKGROUP);
    if (lane == 0 && old == 3u) __hip_atomic_store(gflags + q * 32, 1u, __ATOMIC_RELAXED, __HIP_MEMORY_SCOPE_AGENT);
    __builtin_amdgcn_s_setprio(0);
    STAMP(4);
    const int e0 = tid - 256;
    f32x4 sk[5];
#pragma unroll
    for (int i = 0; i < 5; ++i) {
      const int e = e0 + 256 * i, t = e / 20, cq = e % 20;
      u32x2 xv = *(const u32x2*)(x + (size_t)(64 * c + t) * D_ + 80 * q + 4 * cq);
      u32x2 dv = *(const u32x2*)(Dsk + 80 * q + 4 * cq);
      const float gt = gvec[t];
      sk[i][0] = gt * bflo(xv[0]) * bflo(dv[0]); sk[i][1] = gt * bfhi(xv[0]) * bfhi(dv[0]);
      sk[i][2] = gt * bflo(xv[1]) * bflo(dv[1]); sk[i][3] = gt * bfhi(xv[1]) * bfhi(dv[1]);
    }
    STAMP(23);
    __syncthreads();
    STAMP(24);
#pragma unroll
    for (int i = 0; i < 5; ++i) {
      const int e = e0 + 256 * i, t = e / 20, cq = e % 20;
      const char* rp = smem + (t * RED_S + 4 * cq) * 4;
      f32x4 v = (*(const f32x4*)(rp) + *(const f32x4*)(rp + RED_BYTES)) + (*(const f32x4*)(rp + 2 * RED_BYTES) + *(const f32x4*)(rp + 3 * RED_BYTES)) + sk[i];
      *(f32x4*)(out + (size_t)(64 * c + t) * D_ + 80 * q + 4 * cq) = v;
    }
  }
  STAMP(25);
  STAMP(26);
  STAMP_OUT(1);
}

extern "C" void kernel_launch(void* const* d_in, const int* in_sizes, int n_in,
                              void* d_out, int out_size, void* d_ws, size_t ws_size,
                              hipStream_t stream) {
  const u16* x   = (const u16*)d_in[0];
  const u16* Lre = (const u16*)d_in[1];
  const u16* Lim = (const u16*)d_in[2];
  const u16* Bre = (const u16*)d_in[3];
  const u16* Bim = (const u16*)d_in[4];
  const u16* Cre = (const u16*)d_in[5];
  const u16* Cim = (const u16*)d_in[6];
  const u16* ldt = (const u16*)d_in[7];
  const u16* Dsk = (const u16*)d_in[8];
  const u16* w1  = (const u16*)d_in[10];
  const u16* b1  = (const u16*)d_in[11];
  const u16* w2  = (const u16*)d_in[12];
  const u16* gw  = (const u16*)d_in[13];
  char* ws = (char*)d_ws;
  K1Args a{x, Lre, Lim, Bre, Bim, Cre, Cim, ldt, w1, b1, w2, gw, ws};
  k1<<<256, 512, 0, stream>>>(a);
  k2<<<256, 512, 0, stream>>>(x, Dsk, ws, (float*)d_out);
}
```

```cpp
#include <hip/hip_runtime.h>
#include <hip/hip_bf16.h>
#include <stdint.h>

typedef __attribute__((ext_vector_type(8))) __bf16 bf16x8;
typedef __attribute__((ext_vector_type(2))) __bf16 bf16x2;
typedef __attribute__((ext_vector_type(4))) float f32x4;
typedef __attribute__((ext_vector_type(2))) float f32x2;
typedef __attribute__((ext_vector_type(4))) unsigned u32x4;
typedef __attribute__((ext_vector_type(2))) unsigned u32x2;
typedef __attribute__((ext_vector_type(4))) short s16x4;
typedef unsigned short u16;
#define LAS __attribute__((address_space(3)))
#define GAS __attribute__((address_space(1)))
#define DI __device__ __forceinline__

constexpr int T_ = 2048, D_ = 640, P_ = 640, F_ = 1536;
constexpr int KS_MLP = 48, KS = 88;

constexpr unsigned OFF_A    = 0;
constexpr unsigned OFF_B    = OFF_A + 32u * KS * 4096;
constexpr unsigned OFF_BP   = OFF_B + 8u * KS * 5120;
constexpr unsigned OFF_E128 = OFF_BP + 32u * 16 * P_ * 16;
constexpr unsigned OFF_EP   = OFF_E128 + 8u * P_ * 16;
constexpr unsigned OFF_PAR  = OFF_EP + 16u * 4 * P_ * 8;
constexpr unsigned OFF_G    = OFF_PAR + P_ * 64u;
constexpr unsigned OFF_PF   = OFF_G + T_ * 4u;
constexpr unsigned OFF_DF   = OFF_PF + 2048;
constexpr unsigned OFF_HF   = OFF_DF + 2048;
constexpr unsigned OFF_EPOCH = OFF_HF + 32 * 128;
constexpr unsigned WS_BYTES = OFF_EPOCH + 256;

#define STAMP(i)
#define STAMP_DECL
#define STAMP_OUT(kid)

DI float bflo(unsigned v) { return __uint_as_float(v << 16); }
DI float bfhi(unsigned v) { return __uint_as_float(v & 0xffff0000u); }
DI unsigned pack2(float a, float b) {
  bf16x2 r; r[0] = (__bf16)a; r[1] = (__bf16)b; return __builtin_bit_cast(unsigned, r);
}
DI float dswap(float v) {
  return __int_as_float(__builtin_amdgcn_mov_dpp(__float_as_int(v), 0xB1, 0xf, 0xf, true));
}
DI float cmul(float lr, float ls, float v) { return fmaf(lr, v, ls * dswap(v)); }
DI float cmuladd(float lr, float ls, float v, float a) { return fmaf(lr, v, fmaf(ls, dswap(v), a)); }
DI f32x2 cm2(f32x2 a, f32x2 b) { return (f32x2){a[0] * b[0] - a[1] * b[1], a[0] * b[1] + a[1] * b[0]}; }
DI f32x2 cfma(f32x2 lrr, f32x2 lni, f32x2 s, f32x2 b) {
  f32x2 sy = __builtin_shufflevector(s, s, 1, 0);
  return __builtin_elementwise_fma(lrr, s, __builtin_elementwise_fma(lni, sy, b));
}

DI float sfma(float a, float b, float c) { float d; asm("v_fma_f32 %0, %1, %2, %3" : "=v"(d) : "v"(a), "v"(b), "v"(c)); return d; }
DI float smul(float a, float b) { float d; asm("v_mul_f32 %0, %1, %2" : "=v"(d) : "v"(a), "v"(b)); return d; }

DI f32x2 mish2(f32x2 h) {
  f32x2 z = h * 1.44269504088896341f;
  f32x2 w; w[0] = __builtin_amdgcn_exp2f(fminf(z[0], 43.f)); w[1] = __builtin_amdgcn_exp2f(fminf(z[1], 43.f));
  f32x2 n = w * (w + 2.f);
  f32x2 d = n + 2.f;
  f32x2 r; r[0] = __builtin_amdgcn_rcpf(d[0]); r[1] = __builtin_amdgcn_rcpf(d[1]);
  return (h * n) * r;
}

DI void glds16(const void* gsrc, unsigned lds_dst) {
  unsigned keep;
  asm volatile("s_mov_b32 %0, m0\n\ts_mov_b32 m0, %2\n\ts_nop 0\n\tglobal_load_lds_dwordx4 %1, off\n\ts_mov_b32 m0, %0"
               : "=&s"(keep) : "v"(gsrc), "s"(lds_dst) : "memory");
}
DI void touch(const void* gsrc, unsigned lds_dst) {
  unsigned keep;
  asm volatile("s_mov_b32 %0, m0\n\ts_mov_b32 m0, %2\n\ts_nop 0\n\tglobal_load_lds_dword %1, off\n\ts_mov_b32 m0, %0"
               : "=&s"(keep) : "v"(gsrc), "s"(lds_dst) : "memory");
}
DI unsigned ld_u16_asm(const void* p) { unsigned r; asm volatile("global_load_ushort %0, %1, off" : "=v"(r) : "v"(p) : "memory"); return r; }
DI unsigned ld_u32_asm(const void* p) { unsigned r; asm volatile("global_load_dword %0, %1, off" : "=v"(r) : "v"(p) : "memory"); return r; }
DI u32x4 ld_u128_asm(const void* p) { u32x4 r; asm volatile("global_load_dwordx4 %0, %1, off" : "=v"(r) : "v"(p) : "memory"); return r; }

DI float sum16(float v) {
  auto r = __builtin_amdgcn_permlane16_swap(__float_as_uint(v), __float_as_uint(v), false, false);
  return __uint_as_float(r[0]) + __uint_as_float(r[1]);
}
DI float sum32(float v) {
  auto r = __builtin_amdgcn_permlane32_swap(__float_as_uint(v), __float_as_uint(v), false, false);
  return __uint_as_float(r[0]) + __uint_as_float(r[1]);
}

typedef unsigned long long u64;
#define SC1 16
#define SCHED __builtin_amdgcn_sched_barrier(0)
#define RSRC __amdgpu_buffer_rsrc_t
DI void st128(RSRC r, unsigned off, u32x4 v) { __builtin_amdgcn_raw_buffer_store_b128(v, r, off, 0, SC1); }
DI void st64(RSRC r, unsigned off, u32x2 v) { __builtin_amdgcn_raw_buffer_store_b64(v, r, off, 0, SC1); }
DI void st32f(RSRC r, unsigned off, float v) { __builtin_amdgcn_raw_buffer_store_b32(__float_as_uint(v), r, off, 0, SC1); }
DI u32x4 ld128(RSRC r, unsigned off) { return __builtin_amdgcn_raw_buffer_load_b128(r, off, 0, SC1); }
DI f32x4 ld128f(RSRC r, unsigned off) { return __builtin_bit_cast(f32x4, __builtin_amdgcn_raw_buffer_load_b128(r, off, 0, SC1)); }
DI f32x2 ld64f(RSRC r, unsigned off) { return __builtin_bit_cast(f32x2, __builtin_amdgcn_raw_buffer_load_b64(r, off, 0, SC1)); }
DI float ld32f(RSRC r, unsigned off) { return __uint_as_float(__builtin_amdgcn_raw_buffer_load_b32(r, off, 0, SC1)); }

constexpr int ST_X = 0, ST_BM = 16384, ST_W1 = 26624, ST_SZ = 38912, NSTG = 4;
constexpr int SP = NSTG * ST_SZ;
constexpr int PARS = 96;
constexpr int SP_PAR = SP, SP_B1 = SP + 3840, SP_EC = SP + 4224, SP_GW = SP + 5504, SP_MISC = SP + 6784, LDS_BYTES = SP + 6848;
constexpr int RED_S = 84, RED_BYTES = 64 * RED_S * 4, HR = 352, K2_H = 4 * RED_BYTES;
constexpr int NB = 4;
enum { M_CNTE = 0, M_DONE = 1, M_GO = 2, M_PACK = 3, M_SCAN = 4, M_PUB = 5, M_TAG = 8 };
constexpr int SPIN_LDS = 1 << 22, SPIN_G = 1 << 19;

struct Args {
  const u16 *x, *Lre, *Lim, *Bre, *Bim, *Cre, *Cim, *ldt, *Dsk, *w1, *b1, *w2, *gw;
  char* ws;
  float* out;
};

DI void packC(const Args& a, RSRC rW, int id, int lane) {
  const int ntile = id / 20, mp = id % 20, n = lane & 15, gg = lane >> 4;
  const size_t co = (size_t)(16 * ntile + n) * P_ + 32 * mp + 8 * gg;
  const u32x4 re = *(const u32x4*)(a.Cre + co), im = *(const u32x4*)(a.Cim + co);
  const unsigned dst = OFF_B + (((ntile / 5) * KS + KS_MLP + 2 * mp + (gg >> 1)) * 5 + (ntile % 5)) * 1024 + ((2 * (gg & 1)) * 16 + n) * 16;
#pragma unroll
  for (int h = 0; h < 2; ++h) {
    u32x4 o;
    o[0] = pack2(2.f * bflo(re[2 * h]), -2.f * bflo(im[2 * h]));
    o[1] = pack2(2.f * bfhi(re[2 * h]), -2.f * bfhi(im[2 * h]));
    o[2] = pack2(2.f * bflo(re[2 * h + 1]), -2.f * bflo(im[2 * h + 1]));
    o[3] = pack2(2.f * bfhi(re[2 * h + 1]), -2.f * bfhi(im[2 * h + 1]));
    st128(rW, dst + h * 256, o);
  }
}

#define CBAR0 do { SCHED; asm volatile("s_waitcnt lgkmcnt(0)\n\ts_barrier" ::: "memory"); SCHED; } while (0)
#define CBAR do { SCHED; asm volatile("s_waitcnt vmcnt(0) lgkmcnt(0)\n\ts_barrier" ::: "memory"); SCHED; } while (0)
#define LBAR(n) asm volatile("s_waitcnt vmcnt(" #n ")\n\ts_barrier" ::: "memory")

DI void lds_wait_ge(volatile LAS unsigned* w, unsigned v) {
  for (int sp = 0; *w < v && sp < SPIN_LDS; ++sp) __builtin_amdgcn_s_sleep(1);
  asm volatile("" ::: "memory");
}
DI unsigned lds_inc(char* smem, int word, int lane) {
  unsigned old = 0;
  if (lane == 0) old = __hip_atomic_fetch_add((LAS unsigned*)(smem + SP_MISC) + word, 1u, __ATOMIC_RELAXED, __HIP_MEMORY_SCOPE_WORKGROUP);
  return __builtin_amdgcn_readfirstlane(old);
}
DI void poll256(RSRC rW, unsigned off, unsigned tlo, unsigned thi, int lane) {
  for (int sp = 0; sp < SPIN_G; ++sp) {
    const u32x4 f0 = ld128(rW, off + lane * 32), f1 = ld128(rW, off + lane * 32 + 16);
    const bool ok = f0[0] == tlo && f0[1] == thi && f0[2] == tlo && f0[3] == thi && f1[0] == tlo && f1[1] == thi && f1[2] == tlo && f1[3] == thi;
    if (__all(ok)) break;
    __builtin_amdgcn_s_sleep(2);
    asm volatile("" ::: "memory");
  }
  asm volatile("" ::: "memory");
}

__global__ __launch_bounds__(512, 2) void fused(Args a) {
  __shared__ __attribute__((aligned(16))) char smem[LDS_BYTES];
  const int tid = threadIdx.x, lane = tid & 63;
  const int wave = __builtin_amdgcn_readfirstlane(tid >> 6);
  const int b = blockIdx.x, grp = b & 7, idx = b >> 3;
  const int rc = 8 * (grp >> 2) + (idx >> 2), j = 4 * (grp & 3) + (idx & 3);
  const int c = 8 * (grp >> 1) + (idx >> 2), q = 4 * (grp & 1) + (idx & 3);
  const int r16 = lane & 15, g = lane >> 4;
  char* ws = a.ws;
  const RSRC rW = __builtin_amdgcn_make_buffer_rsrc((void*)ws, 0, WS_BYTES, 0x00020000);
  volatile LAS unsigned* misc = (volatile LAS unsigned*)(smem + SP_MISC);
  STAMP_DECL
  STAMP(0);

  if (wave >= 4) {
    const int lw = wave - 4;
    const unsigned sbase = (unsigned)(size_t)(LAS char*)smem;
    const char* src[10]; int inc[10]; unsigned dsto[10];
#pragma unroll
    for (int i = 0; i < 10; ++i) {
      int p = lw + 4 * i; p = p >= 38 ? p - 38 : p;
      dsto[i] = sbase + p * 1024;
      const int pr = p < 16 ? p : p - 16;
      const int row = 8 * pr + (lane >> 3), ch = (lane & 7) ^ ((row >> 1) & 7);
      const u16* px = a.x + (size_t)(128 * rc + row) * D_ + 8 * ch;
      const u16* pb = ((row & 1) ? a.Bim : a.Bre) + (size_t)(40 * j + (row >> 1)) * D_ + 8 * ch;
      const int ci = 64 * (p >= 26 ? p - 26 : 0) + lane, k = ci / 12, cp = ci % 12;
      int cc = cp - 2 * ((k >> 3) & 1); cc = cc < 0 ? cc + 12 : cc;
      const u16* pw = a.w1 + (size_t)k * F_ + 96 * j + 8 * cc;
      inc[i] = p < 26 ? 128 : 64 * F_ * 2;
      src[i] = (const char*)(p < 16 ? px : p < 26 ? pb : pw);
    }
    auto stage = [&](unsigned boff) {
#pragma unroll
      for (int i = 0; i < 10; ++i) { glds16(src[i], dsto[i] + boff); src[i] += inc[i]; }
    };
    stage(0); stage(ST_SZ);
    STAMP(1);
    LBAR(10);
    STAMP(2);
    stage(2 * ST_SZ); stage(3 * ST_SZ);
#pragma unroll 1
    for (int s = 1; s <= 6; ++s) {
      LBAR(20);
      if (s == 2 && tid == 256) {
        u32x2 t; t[0] = misc[M_TAG]; t[1] = misc[M_TAG + 1];
        st64(rW, OFF_PF + b * 8, t);
      }
      stage(((s + 3) & 3) * ST_SZ);
    }
    LBAR(20); LBAR(10); LBAR(0);
    STAMP(3);
    const unsigned tlo = misc[M_TAG], thi = misc[M_TAG + 1];

    const int u = lw;
    if (u == 3) {
      poll256(rW, OFF_PF, tlo, thi, lane);
      if (lane == 0) misc[M_PACK] = 1u;
      STAMP(4);
      poll256(rW, OFF_DF, tlo, thi, lane);
      if (lane == 0) misc[M_GO] = 1u;
      if (b == 0 && lane == 0) { u32x2 t; t[0] = tlo; t[1] = thi; st64(rW, OFF_EPOCH, t); }
      STAMP(5);
    } else {
      int L = 64 * u + lane; L = L < 160 ? L : 159;
      const int hf = L >= 80 ? 1 : 0, chl = L - 80 * hf, ch = 80 * q + chl;
      const int rc2 = c >> 1, si_ = 2 * (c & 1) + hf;
      lds_wait_ge(misc + M_PACK, 1u);
      const f32x4 pa = ld128f(rW, OFF_PAR + ch * 64);
      const f32x2 pw = ld64f(rW, OFF_PAR + ch * 64 + 16 + si_ * 8);
      lds_wait_ge(misc + M_GO, 1u);
      STAMP(5);
      __builtin_amdgcn_s_setprio(3);
      const f32x2 ep = ld64f(rW, OFF_EP + ((rc2 * 4 + si_) * P_ + ch) * 8);
      f32x4 ev[8];
#pragma unroll
      for (int rp = 0; rp < 8; ++rp) {
        ev[rp] = (f32x4){0.f, 0.f, 0.f, 0.f};
        if (2 * rp < rc2) ev[rp] = ld128f(rW, OFF_E128 + (rp * P_ + ch) * 16);
      }
      u32x4 bq[8]; f32x4 gv[8];
#pragma unroll
      for (int i = 0; i < 8; ++i) bq[i] = ld128(rW, OFF_BP + ((c * 16 + 8 * hf + i) * P_ + ch) * 16);
#pragma unroll
      for (int i = 0; i < 8; ++i) gv[i] = ld128f(rW, OFF_G + (64 * c + 32 * hf + 4 * i) * 4);
      __builtin_amdgcn_s_setprio(0);
      SCHED;
      float sr = 0.f, si = 0.f;
      {
        const float lr = pa[2], li = pa[3], nli = -pa[3];
#pragma unroll
        for (int rp = 0; rp < 8; ++rp) {
          if (2 * rp < rc2) { const float tr = sfma(nli, si, ev[rp][0]), ti = sfma(li, sr, ev[rp][1]); sr = sfma(lr, sr, tr); si = sfma(lr, si, ti); }
          if (2 * rp + 1 < rc2) { const float tr = sfma(nli, si, ev[rp][2]), ti = sfma(li, sr, ev[rp][3]); sr = sfma(lr, sr, tr); si = sfma(lr, si, ti); }
        }
        const float tr = sfma(-pw[1], si, ep[0]), ti = sfma(pw[1], sr, ep[1]);
        sr = sfma(pw[0], sr, tr); si = sfma(pw[0], si, ti);
      }
      STAMP(6);
      const float lr = pa[0], li = pa[1], nli = -pa[1];
      char* wp = smem + K2_H + 32 * hf * HR + chl * 4;
#pragma unroll
      for (int t = 0; t < 32; ++t) {
        const unsigned wr = bq[t >> 2][(t >> 1) & 1], wi = bq[t >> 2][2 + ((t >> 1) & 1)];
        const float tr = sfma(nli, si, (t & 1) ? bfhi(wr) : bflo(wr)), ti = sfma(li, sr, (t & 1) ? bfhi(wi) : bflo(wi));
        sr = sfma(lr, sr, tr); si = sfma(lr, si, ti);
        const float gt = gv[t >> 2][t & 3];
        *(unsigned*)(wp + t * HR) = pack2(smul(sr, gt), smul(si, gt));
      }
      asm volatile("s_waitcnt lgkmcnt(0)" ::: "memory");
      lds_inc(smem, M_SCAN, lane);
      STAMP(7);
      lds_wait_ge(misc + M_SCAN, 3u);
      __builtin_amdgcn_s_setprio(3);
      {
        const char* hp = smem + K2_H + r16 * HR + g * 16;
#pragma unroll
        for (int k = 0; k < 7; ++k) {
          const int f = u + 3 * k, kl = f >> 2, mt = f & 3;
          if (f < 20) st128(rW, OFF_A + (c * KS + KS_MLP + 5 * q) * 4096 + f * 1024 + lane * 16, *(const u32x4*)(hp + mt * 16 * HR + kl * 64));
        }
      }
      asm volatile("s_waitcnt vmcnt(0)" ::: "memory");
      if (lds_inc(smem, M_PUB, lane) == 2u && lane == 0) { u32x2 t; t[0] = tlo; t[1] = thi; st64(rW, OFF_HF + c * 128 + q * 8, t); }
      __builtin_amdgcn_s_setprio(0);
      STAMP(8);
    }
    const int e0 = tid - 256;
    f32x4 sk[5];
#pragma unroll
    for (int i = 0; i < 5; ++i) {
      const int e = e0 + 256 * i, t = e / 20, cq = e % 20;
      u32x2 xv = *(const u32x2*)(a.x + (size_t)(64 * c + t) * D_ + 80 * q + 4 * cq);
      u32x2 dv = *(const u32x2*)(a.Dsk + 80 * q + 4 * cq);
      const float gt = ld32f(rW, OFF_G + (64 * c + t) * 4);
      sk[i][0] = gt * bflo(xv[0]) * bflo(dv[0]); sk[i][1] = gt * bfhi(xv[0]) * bfhi(dv[0]);
      sk[i][2] = gt * bflo(xv[1]) * bflo(dv[1]); sk[i][3] = gt * bfhi(xv[1]) * bfhi(dv[1]);
    }
    STAMP(23);
    __syncthreads();
    STAMP(24);
#pragma unroll
    for (int i = 0; i < 5; ++i) {
      const int e = e0 + 256 * i, t = e / 20, cq = e % 20;
      const char* rp = smem + (t * RED_S + 4 * cq) * 4;
      f32x4 v = (*(const f32x4*)(rp) + *(const f32x4*)(rp + RED_BYTES)) + (*(const f32x4*)(rp + 2 * RED_BYTES) + *(const f32x4*)(rp + 3 * RED_BYTES)) + sk[i];
      *(f32x4*)(a.out + (size_t)(64 * c + t) * D_ + 80 * q + 4 * cq) = v;
    }
  } else {
    if (wave == 0) {
      if (b < 240) {
        const int ks = b / 5, m = b % 5;
        const u16* p = a.w2 + (size_t)(32 * ks + 4 * g) * D_ + 128 * m + 8 * r16;
        u32x4 R[8];
#pragma unroll
        for (int jj = 0; jj < 8; ++jj) R[jj] = *(const u32x4*)(p + (size_t)((jj & 3) + 16 * (jj >> 2)) * D_);
        const int n0 = 128 * m + 8 * r16, qq = n0 / 80, nt = (n0 % 80) / 16;
        const unsigned dst = OFF_B + ((qq * KS + ks) * 5 + nt) * 1024 + (g * 16 + (n0 & 15)) * 16;
#pragma unroll
        for (int e = 0; e < 8; ++e) {
          u32x4 o;
#pragma unroll
          for (int d = 0; d < 4; ++d) o[d] = __builtin_amdgcn_perm(R[2 * d + 1][e >> 1], R[2 * d][e >> 1], (e & 1) ? 0x07060302u : 0x05040100u);
          st128(rW, dst + e * 16, o);
        }
      }
    } else if (wave == 1) {
      const u64 ep = __hip_atomic_load((GAS u64*)(ws + OFF_EPOCH), __ATOMIC_RELAXED, __HIP_MEMORY_SCOPE_AGENT);
      const unsigned v = *(const unsigned*)(a.b1 + 96 * j + 2 * (lane < 48 ? lane : 0));
      const u32x4 g0 = *(const u32x4*)(a.gw + 8 * lane), g1 = *(const u32x4*)(a.gw + 8 * (64 + r16));
      packC(a, rW, 3 * b, lane);
      if (lane < 48) *(f32x2*)(smem + SP_B1 + lane * 8) = (f32x2){bflo(v), bfhi(v)};
      *(u32x4*)(smem + SP_GW + lane * 16) = g0;
      if (lane < 16) *(u32x4*)(smem + SP_GW + 1024 + lane * 16) = g1;
      if (lane < 8) misc[lane] = 0u;
      if (lane == 0) { const u64 tg = ep + 1; misc[M_TAG] = (unsigned)tg; misc[M_TAG + 1] = (unsigned)(tg >> 32); }
    } else if (wave == 2) {
      const int ch = 40 * j + (lane < 40 ? lane : 0);
      const float lre = bflo(a.Lre[ch]), lim = bflo(a.Lim[ch]), dt = expf(bflo(a.ldt[ch]));
      float e = expf(dt * lre), sn, cs;
      sincosf(dt * lim, &sn, &cs);
      f32x2 l1 = {e * cs, e * sn};
      const float nr = l1[0] - 1.f, ni = l1[1], den = lre * lre + lim * lim;
      f32x2 cf = {(nr * lre + ni * lim) / den, (ni * lre - nr * lim) / den};
      f32x2 l2 = cm2(l1, l1), l4 = cm2(l2, l2), l8 = cm2(l4, l4), l12 = cm2(l8, l4), l16 = cm2(l8, l8);
      f32x2 l32 = cm2(l16, l16), l64 = cm2(l32, l32), l128 = cm2(l64, l64);
      if (lane < 40) {
        float* pp = (float*)(smem + SP_PAR + lane * PARS);
        *(f32x4*)(pp) = (f32x4){l1[0], l1[1], l16[0], l16[1]};
        *(f32x4*)(pp + 4) = (f32x4){cf[0], cf[1], l32[0], l32[1]};
        *(f32x4*)(pp + 8) = (f32x4){l12[0], l12[1], l8[0], l8[1]};
        *(f32x4*)(pp + 12) = (f32x4){l4[0], l4[1], 1.f, 0.f};
        if (rc == 0) {
          const f32x2 l96 = cm2(l64, l32);
          st128(rW, OFF_PAR + ch * 64, __builtin_bit_cast(u32x4, (f32x4){l1[0], l1[1], l128[0], l128[1]}));
          st128(rW, OFF_PAR + ch * 64 + 16, __builtin_bit_cast(u32x4, (f32x4){1.f, 0.f, l32[0], l32[1]}));
          st128(rW, OFF_PAR + ch * 64 + 32, __builtin_bit_cast(u32x4, (f32x4){l64[0], l64[1], l96[0], l96[1]}));
        }
      }
    } else {
      packC(a, rW, 3 * b + 1, lane);
      packC(a, rW, 3 * b + 2, lane);
      if (b < 32) packC(a, rW, 768 + b, lane);
    }
    STAMP(1);
    const int th = wave & 1;
    const unsigned xo0 = ST_X + (64 * th + r16) * 128 + ((g ^ (r16 >> 1)) * 16);
    const unsigned xo1 = xo0 ^ 64;
    const int c1 = 2 * rc + th;

#define P1_PIPE(LOADF, STEP, MMA, INIT)                                             \
    CBAR0;                                                                          \
    STAMP(2);                                                                       \
    INIT;                                                                           \
    LOADF(F0, 0u, 0);                                                               \
    STEP(F0, F1, 0u, 1);                                                            \
    _Pragma("unroll 1") for (int s = 1; s < 10; ++s) {                              \
      CBAR;                                                                         \
      STEP(F1, F0, (s & 3) * ST_SZ, 0);                                             \
      STEP(F0, F1, (s & 3) * ST_SZ, 1);                                             \
    }                                                                               \
    MMA(F1); SCHED;

    if (wave < 2) {
      f32x4 acc[6][4], gac;
      const int q4 = r16 >> 2, p4 = r16 & 3;
      const unsigned wbase = ST_W1 + (8 * g + q4) * 192 + (p4 >> 1) * 16 + (p4 & 1) * 8;
      const unsigned wa = wbase + (g & 1) * 32;
      const unsigned wb = wbase + ((g & 1) ? 0 : 160);
      const bool do_gate = j < 8 && (j >> 2) == th;
      int kc = 0;
      struct Frag { bf16x8 x[4], w[6], gx, gf; };
      Frag F0, F1;
      auto rdw = [&](const char* sb, int ft, int kk) {
        unsigned ad = (ft < 5 ? wa + 32 * ft : wb) + kk * 32 * 192;
        s16x4 lo = __builtin_amdgcn_ds_read_tr16_b64_v4i16((LAS s16x4*)(sb + ad));
        s16x4 hi = __builtin_amdgcn_ds_read_tr16_b64_v4i16((LAS s16x4*)(sb + ad + 4 * 192));
        return __builtin_bit_cast(bf16x8, __builtin_shufflevector(lo, hi, 0, 1, 2, 3, 4, 5, 6, 7));
      };
      auto loadf = [&](Frag& F, unsigned boff, int kk) {
        const char* sb = smem + boff;
#pragma unroll
        for (int tt = 0; tt < 4; ++tt) F.x[tt] = *(const bf16x8*)(sb + (kk ? xo1 : xo0) + tt * 2048);
#pragma unroll
        for (int ft = 0; ft < 6; ++ft) F.w[ft] = rdw(sb, ft, kk);
        if (do_gate) {
          F.gf = *(const bf16x8*)(smem + SP_GW + kc * 64 + g * 16);
          F.gx = *(const bf16x8*)(sb + (kk ? xo1 : xo0) + (j & 3) * 2048);
        }
        ++kc;
      };
      auto mma = [&](Frag& F) {
#pragma unroll
        for (int m = 0; m < 24; ++m)
          acc[m >> 2][m & 3] = __builtin_amdgcn_mfma_f32_16x16x32_bf16(F.w[m >> 2], F.x[m & 3], acc[m >> 2][m & 3], 0, 0, 0);
        if (do_gate) gac = __builtin_amdgcn_mfma_f32_16x16x32_bf16(F.gf, F.gx, gac, 0, 0, 0);
      };
      auto step = [&](Frag& C, Frag& N, unsigned boff, const int kk) {
        const char* sb = smem + boff;
#pragma unroll
        for (int m = 0; m < 24; ++m) {
          SCHED;
          acc[m >> 2][m & 3] = __builtin_amdgcn_mfma_f32_16x16x32_bf16(C.w[m >> 2], C.x[m & 3], acc[m >> 2][m & 3], 0, 0, 0);
          SCHED;
          if (m < 4) N.x[m] = *(const bf16x8*)(sb + (kk ? xo1 : xo0) + m * 2048);
          if (m >= 4 && m < 10) N.w[m - 4] = rdw(sb, m - 4, kk);
          if (m == 10 && do_gate) {
            gac = __builtin_amdgcn_mfma_f32_16x16x32_bf16(C.gf, C.gx, gac, 0, 0, 0);
            N.gf = *(const bf16x8*)(smem + SP_GW + kc * 64 + g * 16);
            N.gx = *(const bf16x8*)(sb + (kk ? xo1 : xo0) + (j & 3) * 2048);
          }
        }
        ++kc;
        SCHED;
      };
      auto init = [&]() {
#pragma unroll
        for (int ft = 0; ft < 6; ++ft) {
          f32x4 bv = *(const f32x4*)(smem + SP_B1 + (16 * ft + 4 * g) * 4);
#pragma unroll
          for (int tt = 0; tt < 4; ++tt) acc[ft][tt] = bv;
        }
        gac = (f32x4){0.f, 0.f, 0.f, 0.f};
      };
      P1_PIPE(loadf, step, mma, init());
      STAMP(3);
      if (do_gate && g == 0) st32f(rW, OFF_G + (128 * rc + 16 * j + r16) * 4, 1.f / (1.f + expf(-gac[0])));
#pragma unroll
      for (int fp = 0; fp < 3; ++fp)
#pragma unroll
        for (int tt = 0; tt < 4; ++tt) {
          f32x4 a0 = acc[2 * fp][tt], a1 = acc[2 * fp + 1][tt];
          f32x2 m0 = mish2((f32x2){a0[0], a0[1]}), m1 = mish2((f32x2){a0[2], a0[3]});
          f32x2 m2 = mish2((f32x2){a1[0], a1[1]}), m3 = mish2((f32x2){a1[2], a1[3]});
          u32x4 o;
          o[0] = pack2(m0[0], m0[1]); o[1] = pack2(m1[0], m1[1]); o[2] = pack2(m2[0], m2[1]); o[3] = pack2(m3[0], m3[1]);
          st128(rW, OFF_A + ((c1 * KS + 3 * j + fp) * 4 + tt) * 1024 + lane * 16, o);
        }
    } else {
      f32x4 acc[4][5];
      const unsigned bo0 = ST_BM + r16 * 128 + ((g ^ (r16 >> 1)) * 16);
      const unsigned bo1 = bo0 ^ 64;
      struct Frag { bf16x8 x[4], w[5]; };
      Frag F0, F1;
      auto loadf = [&](Frag& F, unsigned boff, int kk) {
        const char* sb = smem + boff;
#pragma unroll
        for (int tt = 0; tt < 4; ++tt) F.x[tt] = *(const bf16x8*)(sb + (kk ? xo1 : xo0) + tt * 2048);
#pragma unroll
        for (int nt = 0; nt < 5; ++nt) F.w[nt] = *(const bf16x8*)(sb + (kk ? bo1 : bo0) + nt * 2048);
      };
      auto mma = [&](Frag& F) {
#pragma unroll
        for (int m = 0; m < 20; ++m)
          acc[m & 3][m >> 2] = __builtin_amdgcn_mfma_f32_16x16x32_bf16(F.x[m & 3], F.w[m >> 2], acc[m & 3][m >> 2], 0, 0, 0);
      };
      auto step = [&](Frag& C, Frag& N, unsigned boff, const int kk) {
        const char* sb = smem + boff;
#pragma unroll
        for (int m = 0; m < 20; ++m) {
          SCHED;
          acc[m & 3][m >> 2] = __builtin_amdgcn_mfma_f32_16x16x32_bf16(C.x[m & 3], C.w[m >> 2], acc[m & 3][m >> 2], 0, 0, 0);
          SCHED;
          if (m < 4) N.x[m] = *(const bf16x8*)(sb + (kk ? xo1 : xo0) + m * 2048);
          if (m >= 4 && m < 9) N.w[m - 4] = *(const bf16x8*)(sb + (kk ? bo1 : bo0) + (m - 4) * 2048);
        }
        SCHED;
      };
      float l1r[5], l1s[5], l16r[5], l16s[5], wgr[5], wgs[5], cfr[5], cfs[5];
      auto init = [&]() {
#pragma unroll
        for (int tt = 0; tt < 4; ++tt)
#pragma unroll
          for (int nt = 0; nt < 5; ++nt) acc[tt][nt] = (f32x4){0.f, 0.f, 0.f, 0.f};
        const float sgn = (r16 & 1) ? 1.f : -1.f;
#pragma unroll
        for (int nt = 0; nt < 5; ++nt) {
          const float* pp = (const float*)(smem + SP_PAR + (8 * nt + (r16 >> 1)) * PARS);
          const f32x4 p0 = *(const f32x4*)pp;
          const f32x2 cf = *(const f32x2*)(pp + 4);
          const f32x2 wg = *(const f32x2*)(pp + 8 + 2 * g);
          l1r[nt] = p0[0]; l1s[nt] = sgn * p0[1]; l16r[nt] = p0[2]; l16s[nt] = sgn * p0[3];
          wgr[nt] = wg[0]; wgs[nt] = sgn * wg[1]; cfr[nt] = cf[0]; cfs[nt] = sgn * cf[1];
        }
      };
      P1_PIPE(loadf, step, mma, init());
      STAMP(3);
      const unsigned bp = OFF_BP + ((c1 * 16 + g) * 2 * P_ + 80 * j + r16) * 8;
#pragma unroll
      for (int hv = 0; hv < 2; ++hv) {
        float bu[2][5][4];
#pragma unroll
        for (int t2 = 0; t2 < 2; ++t2)
#pragma unroll
          for (int nt = 0; nt < 5; ++nt) {
#pragma unroll
            for (int r = 0; r < 4; ++r) bu[t2][nt][r] = cmul(cfr[nt], cfs[nt], acc[2 * hv + t2][nt][r]);
            u32x2 o; o[0] = pack2(bu[t2][nt][0], bu[t2][nt][1]); o[1] = pack2(bu[t2][nt][2], bu[t2][nt][3]);
            st64(rW, bp + (4 * (2 * hv + t2) * 2 * P_ + 16 * nt) * 8, o);
          }
        float h[2][5];
#pragma unroll
        for (int t2 = 0; t2 < 2; ++t2)
#pragma unroll
          for (int nt = 0; nt < 5; ++nt) h[t2][nt] = bu[t2][nt][0];
#pragma unroll
        for (int r = 1; r < 4; ++r)
#pragma unroll
          for (int t2 = 0; t2 < 2; ++t2)
#pragma unroll
            for (int nt = 0; nt < 5; ++nt) h[t2][nt] = cmuladd(l1r[nt], l1s[nt], h[t2][nt], bu[t2][nt][r]);
        float ee[5];
#pragma unroll
        for (int nt = 0; nt < 5; ++nt) ee[nt] = cmuladd(l16r[nt], l16s[nt], h[0][nt], h[1][nt]);
#pragma unroll
        for (int nt = 0; nt < 5; ++nt) ee[nt] = cmul(wgr[nt], wgs[nt], ee[nt]);
#pragma unroll
        for (int nt = 0; nt < 5; ++nt) ee[nt] = sum16(ee[nt]);
#pragma unroll
        for (int nt = 0; nt < 5; ++nt) ee[nt] = sum32(ee[nt]);
        if (g == 0) {
#pragma unroll
          for (int nt = 0; nt < 5; ++nt) *(float*)(smem + SP_EC + ((2 * th + hv) * 80 + 16 * nt + r16) * 4) = ee[nt];
        }
      }
      asm volatile("s_waitcnt lgkmcnt(0)" ::: "memory");
      if (lds_inc(smem, M_CNTE, lane) == 1u) {
        asm volatile("" ::: "memory");
#pragma unroll
        for (int rnd = 0; rnd < 2; ++rnd) {
          const int col = rnd == 0 ? lane : 64 + r16;
          const int chl = col >> 1;
          const float sg = (col & 1) ? 1.f : -1.f;
          const f32x2 l32 = *(const f32x2*)(smem + SP_PAR + chl * PARS + 24);
          const float* ec = (const float*)(smem + SP_EC) + col;
          const float e1 = ec[0];
          const float e2 = cmuladd(l32[0], sg * l32[1], e1, ec[80]);
          const float e3 = cmuladd(l32[0], sg * l32[1], e2, ec[160]);
          const float e4 = cmuladd(l32[0], sg * l32[1], e3, ec[240]);
          if (rnd == 0 || lane < 16) {
            const int ch = 40 * j + chl;
            const unsigned eo = OFF_EP + (rc * 4 * P_ + ch) * 8 + (col & 1) * 4;
            st32f(rW, eo, 0.f); st32f(rW, eo + P_ * 8, e1); st32f(rW, eo + 2 * P_ * 8, e2); st32f(rW, eo + 3 * P_ * 8, e3);
            st32f(rW, OFF_E128 + ((rc >> 1) * P_ + ch) * 16 + (rc & 1) * 8 + (col & 1) * 4, e4);
          }
        }
      }
    }
    STAMP(4);
    asm volatile("s_waitcnt vmcnt(0)" ::: "memory");
    const unsigned tlo = misc[M_TAG], thi = misc[M_TAG + 1];
    if (lds_inc(smem, M_DONE, lane) == 3u && lane == 0) { u32x2 t; t[0] = tlo; t[1] = thi; st64(rW, OFF_DF + b * 8, t); }
    STAMP(5);

    const unsigned va = OFF_A + c * KS * 4096 + lane * 16 + wave * 4096, vb = OFF_B + q * KS * 5120 + lane * 16 + wave * 5120;
    u32x4 Ab[NB][4], Bb[NB][5];
    f32x4 acc[4][5], gq[4];
    u32x2 fl = {0u, 0u};
#pragma unroll
    for (int mt = 0; mt < 4; ++mt)
#pragma unroll
      for (int nt = 0; nt < 5; ++nt) acc[mt][nt] = (f32x4){0.f, 0.f, 0.f, 0.f};
#define LOAD1(i, e)                                                                                            \
    do {                                                                                                       \
      if ((e) < 5) Bb[(i) % NB][(e)] = __builtin_amdgcn_raw_buffer_load_b128(rW, vb + (e) * 1024, (i) * 20480, SC1); \
      else Ab[(i) % NB][(e) - 5] = __builtin_amdgcn_raw_buffer_load_b128(rW, va + ((e) - 5) * 1024, (i) * 16384, SC1); \
    } while (0)
    lds_wait_ge(misc + M_PACK, 1u);
#pragma unroll
    for (int i = 0; i < NB - 1; ++i)
#pragma unroll
      for (int e = 0; e < 5; ++e) LOAD1(i, e);
    lds_wait_ge(misc + M_GO, 1u);
    STAMP(6);
#pragma unroll
    for (int i = 0; i < NB - 1; ++i)
#pragma unroll
      for (int e = 5; e < 9; ++e) LOAD1(i, e);
#pragma unroll
    for (int i = 0; i < 22; ++i) {
      const int in = i + NB - 1;
      if (in == 10) {
        int tz = threadIdx.x & 7; asm volatile("" : "+v"(tz));
        fl = __builtin_amdgcn_raw_buffer_load_b64(rW, OFF_HF + c * 128 + tz * 8, 0, SC1);
      }
      if (in == 12) {
        for (int sp = 0; !__all(fl[0] == tlo && fl[1] == thi) && sp < SPIN_G; ++sp) {
          __builtin_amdgcn_s_sleep(1);
          int tz = threadIdx.x & 7; asm volatile("" : "+v"(tz) :: "memory");
          fl = __builtin_amdgcn_raw_buffer_load_b64(rW, OFF_HF + c * 128 + tz * 8, 0, SC1);
        }
        asm volatile("" ::: "memory");
        STAMP(10);
      }
#pragma unroll
      for (int m = 0; m < 20; ++m) {
        SCHED;
        acc[m / 5][m % 5] = __builtin_amdgcn_mfma_f32_16x16x32_bf16(__builtin_bit_cast(bf16x8, Ab[i % NB][m / 5]),
                                                                    __builtin_bit_cast(bf16x8, Bb[i % NB][m % 5]), acc[m / 5][m % 5], 0, 0, 0);
        SCHED;
        if (in < 22 && (m & 1) == 1 && m < 18) LOAD1(in, m >> 1);
      }
      SCHED;
      if (i == 0) STAMP(7);
      if (i == 4) STAMP(8);
      if (i == 8) STAMP(9);
      if (i == 11) STAMP(11);
      if (i == 16) STAMP(12);
      if (i == 21) STAMP(13);
      if (i == 9) {
        int tz = threadIdx.x; asm volatile("" : "+v"(tz));
#pragma unroll
        for (int mt = 0; mt < 4; ++mt) gq[mt] = ld128f(rW, OFF_G + (64 * c + 16 * mt + 4 * ((tz >> 4) & 3)) * 4);
      }
      if (i == 11) {
#pragma unroll
        for (int mt = 0; mt < 4; ++mt)
#pragma unroll
          for (int r = 0; r < 4; ++r) {
            const float og = 1.f - gq[mt][r];
#pragma unroll
            for (int nt = 0; nt < 5; ++nt) acc[mt][nt][r] *= og;
          }
      }
    }
#undef LOAD1
    int tz = threadIdx.x; asm volatile("" : "+v"(tz));
    float* red = (float*)(smem + wave * RED_BYTES) + 4 * ((tz >> 4) & 3) * RED_S + (tz & 15);
#pragma unroll
    for (int mt = 0; mt < 4; ++mt)
#pragma unroll
      for (int nt = 0; nt < 5; ++nt)
#pragma unroll
        for (int r = 0; r < 4; ++r) red[(16 * mt + r) * RED_S + 16 * nt] = acc[mt][nt][r];
    STAMP(23);
    __syncthreads();
    STAMP(24);
  }
  STAMP(25);
  STAMP(26);
  STAMP_OUT(0);
}

extern "C" void kernel_launch(void* const* d_in, const int* in_sizes, int n_in,
                              void* d_out, int out_size, void* d_ws, size_t ws_size,
                              hipStream_t stream) {
  Args a;
  a.x   = (const u16*)d_in[0];
  a.Lre = (const u16*)d_in[1];
  a.Lim = (const u16*)d_in[2];
  a.Bre = (const u16*)d_in[3];
  a.Bim = (const u16*)d_in[4];
  a.Cre = (const u16*)d_in[5];
  a.Cim = (const u16*)d_in[6];
  a.ldt = (const u16*)d_in[7];
  a.Dsk = (const u16*)d_in[8];
  a.w1  = (const u16*)d_in[10];
  a.b1  = (const u16*)d_in[11];
  a.w2  = (const u16*)d_in[12];
  a.gw  = (const u16*)d_in[13];
  a.ws  = (char*)d_ws;
  a.out = (float*)d_out;
  fused<<<256, 512, 0, stream>>>(a);
}
```

```cpp
#include <hip/hip_runtime.h>
#include <hip/hip_bf16.h>
#include <stdint.h>

typedef __attribute__((ext_vector_type(8))) __bf16 bf16x8;
typedef __attribute__((ext_vector_type(2))) __bf16 bf16x2;
typedef __attribute__((ext_vector_type(4))) float f32x4;
typedef __attribute__((ext_vector_type(2))) float f32x2;
typedef __attribute__((ext_vector_type(4))) unsigned u32x4;
typedef __attribute__((ext_vector_type(2))) unsigned u32x2;
typedef __attribute__((ext_vector_type(4))) short s16x4;
typedef unsigned short u16;
#define LAS __attribute__((address_space(3)))
#define GAS __attribute__((address_space(1)))
#define DI __device__ __forceinline__

constexpr int T_ = 2048, D_ = 640, P_ = 640, F_ = 1536;

constexpr unsigned OFF_AF = 0;
constexpr unsigned OFF_PB = OFF_AF + 64u * 22 * 8192;
constexpr unsigned OFF_E  = OFF_PB + 64u * 16 * 10240;
constexpr unsigned OFF_FA = OFF_E + 256u * 640;
constexpr unsigned OFF_FE = OFF_FA + 64 * 128;
constexpr unsigned OFF_FR = OFF_FE + 16 * 128;
constexpr unsigned OFF_EPOCH = OFF_FR + 64 * 128;
constexpr unsigned WS_BYTES = OFF_EPOCH + 256;

#define STAMP(i)
#define STAMP_DECL
#define STAMP_OUT(kid)

DI float bflo(unsigned v) { return __uint_as_float(v << 16); }
DI float bfhi(unsigned v) { return __uint_as_float(v & 0xffff0000u); }
DI unsigned pack2(float a, float b) {
  bf16x2 r; r[0] = (__bf16)a; r[1] = (__bf16)b; return __builtin_bit_cast(unsigned, r);
}
DI float dswap(float v) {
  return __int_as_float(__builtin_amdgcn_mov_dpp(__float_as_int(v), 0xB1, 0xf, 0xf, true));
}
DI float cmul(float lr, float ls, float v) { return fmaf(lr, v, ls * dswap(v)); }
DI float cmuladd(float lr, float ls, float v, float a) { return fmaf(lr, v, fmaf(ls, dswap(v), a)); }
DI f32x2 cm2(f32x2 a, f32x2 b) { return (f32x2){a[0] * b[0] - a[1] * b[1], a[0] * b[1] + a[1] * b[0]}; }
DI float sfma(float a, float b, float c) { float d; asm("v_fma_f32 %0, %1, %2, %3" : "=v"(d) : "v"(a), "v"(b), "v"(c)); return d; }
DI float smul(float a, float b) { float d; asm("v_mul_f32 %0, %1, %2" : "=v"(d) : "v"(a), "v"(b)); return d; }

DI f32x2 mish2(f32x2 h) {
  f32x2 z = h * 1.44269504088896341f;
  f32x2 w; w[0] = __builtin_amdgcn_exp2f(fminf(z[0], 43.f)); w[1] = __builtin_amdgcn_exp2f(fminf(z[1], 43.f));
  f32x2 n = w * (w + 2.f);
  f32x2 d = n + 2.f;
  f32x2 r; r[0] = __builtin_amdgcn_rcpf(d[0]); r[1] = __builtin_amdgcn_rcpf(d[1]);
  return (h * n) * r;
}

DI void glds16(const void* gsrc, unsigned lds_dst) {
  unsigned keep;
  asm volatile("s_mov_b32 %0, m0\n\ts_mov_b32 m0, %2\n\ts_nop 0\n\tglobal_load_lds_dwordx4 %1, off\n\ts_mov_b32 m0, %0"
               : "=&s"(keep) : "v"(gsrc), "s"(lds_dst) : "memory");
}
DI void touch(const void* gsrc, unsigned lds_dst) {
  unsigned keep;
  asm volatile("s_mov_b32 %0, m0\n\ts_mov_b32 m0, %2\n\ts_nop 0\n\tglobal_load_lds_dword %1, off\n\ts_mov_b32 m0, %0"
               : "=&s"(keep) : "v"(gsrc), "s"(lds_dst) : "memory");
}
DI float sum16(float v) {
  auto r = __builtin_amdgcn_permlane16_swap(__float_as_uint(v), __float_as_uint(v), false, false);
  return __uint_as_float(r[0]) + __uint_as_float(r[1]);
}
DI float sum32(float v) {
  auto r = __builtin_amdgcn_permlane32_swap(__float_as_uint(v), __float_as_uint(v), false, false);
  return __uint_as_float(r[0]) + __uint_as_float(r[1]);
}

typedef unsigned long long u64;
#define SC1 16
#define SCHED __builtin_amdgcn_sched_barrier(0)
#define RSRC __amdgpu_buffer_rsrc_t
DI void st128(RSRC r, unsigned off, u32x4 v) { __builtin_amdgcn_raw_buffer_store_b128(v, r, off, 0, SC1); }
DI void st128f(RSRC r, unsigned off, f32x4 v) { __builtin_amdgcn_raw_buffer_store_b128(__builtin_bit_cast(u32x4, v), r, off, 0, SC1); }
DI void st64(RSRC r, unsigned off, u32x2 v) { __builtin_amdgcn_raw_buffer_store_b64(v, r, off, 0, SC1); }
DI f32x4 ld128f(RSRC r, unsigned off) { return __builtin_bit_cast(f32x4, __builtin_amdgcn_raw_buffer_load_b128(r, off, 0, SC1)); }
DI f32x2 ld64f(RSRC r, unsigned off) { return __builtin_bit_cast(f32x2, __builtin_amdgcn_raw_buffer_load_b64(r, off, 0, SC1)); }
DI void glds16c(const void* gsrc, unsigned lds_dst) {
  unsigned keep;
  asm volatile("s_mov_b32 %0, m0\n\ts_mov_b32 m0, %2\n\ts_nop 0\n\tglobal_load_lds_dwordx4 %1, off sc1\n\ts_mov_b32 m0, %0"
               : "=&s"(keep) : "v"(gsrc), "s"(lds_dst) : "memory");
}

DI void gldss(unsigned voff, const void* sbase_, unsigned lds_dst) {
  asm volatile("s_mov_b32 m0, %2\n\ts_nop 0\n\tglobal_load_lds_dwordx4 %0, %1" :: "v"(voff), "s"(sbase_), "s"(lds_dst) : "memory", "m0");
}
DI void gldssc(unsigned voff, const void* sbase_, unsigned lds_dst) {
  asm volatile("s_mov_b32 m0, %2\n\ts_nop 0\n\tglobal_load_lds_dwordx4 %0, %1 sc1" :: "v"(voff), "s"(sbase_), "s"(lds_dst) : "memory", "m0");
}

constexpr int ST_X = 0, ST_BM = 16384, ST_W1 = 26624, ST_SZ = 38912, NSTG = 4;
constexpr int SP = NSTG * ST_SZ;
constexpr int PARS = 96;
constexpr int SP_PAR = SP, SP_B1 = SP + 3840, SP_EC = SP + 4224, SP_EL = SP + 5504, SP_GW = SP + 5824, SP_G = SP + 7104,
              SP_MISC = SP + 7616, LDS_BYTES = SP + 7872;
constexpr int BUS = 528, L_BU = SP - 80 * BUS;
constexpr int HIS = 176, L_HI = SP - 128 * HIS;
constexpr int SLOT = 18432, NSLOT = 7;
enum { M_CNTE = 0, M_GRDY, M_P1D, M_ACT, M_BURDY, M_SRD, M_SWR, M_PUB, M_RED, M_NEXT = 10, M_CONS = 12, M_OFF = 16, M_HOK = 18, M_BUW = 19, M_TAG = 24, M_FULL = 32 };
constexpr int SPIN_LDS = 1 << 22, SPIN_G = 1 << 19;

struct Args {
  const u16 *x, *Lre, *Lim, *Bre, *Bim, *Cre, *Cim, *ldt, *Dsk, *w1, *b1, *w2, *gw;
  char* ws;
  float* out;
};

#define CBAR do { SCHED; asm volatile("s_waitcnt lgkmcnt(0)\n\ts_barrier" ::: "memory"); SCHED; } while (0)
#define LBAR(n) asm volatile("s_waitcnt vmcnt(" #n ")\n\ts_barrier" ::: "memory")
#define WAITV(n) asm volatile("s_waitcnt vmcnt(" #n ")" ::: "memory")

DI void lds_wait_ge(volatile LAS unsigned* w, unsigned v) {
  for (int sp = 0; *w < v && sp < SPIN_LDS; ++sp) __builtin_amdgcn_s_sleep(1);
  asm volatile("" ::: "memory");
}
DI unsigned lds_inc(char* smem, int word, int lane) {
  unsigned old = 0;
  if (lane == 0) old = __hip_atomic_fetch_add((LAS unsigned*)(smem + SP_MISC) + word, 1u, __ATOMIC_RELAXED, __HIP_MEMORY_SCOPE_WORKGROUP);
  return __builtin_amdgcn_readfirstlane(old);
}
DI void poll_flags(RSRC rW, unsigned off, int n, unsigned tlo, unsigned thi, int lane) {
  const unsigned o = off + (lane < n ? lane : 0) * 8;
  for (int sp = 0; n > 0 && sp < SPIN_G; ++sp) {
    const u32x2 f = __builtin_amdgcn_raw_buffer_load_b64(rW, o, 0, SC1);
    if (__all(f[0] == tlo && f[1] == thi)) break;
    __builtin_amdgcn_s_sleep(1);
    asm volatile("" ::: "memory");
  }
  asm volatile("" ::: "memory");
}
DI void set_flag(RSRC rW, unsigned off, unsigned tlo, unsigned thi) { u32x2 t; t[0] = tlo; t[1] = thi; st64(rW, off, t); }

DI u32x4 mish_unit(f32x4 a0, f32x4 a1, float og) {
  f32x2 m0 = mish2((f32x2){a0[0], a0[1]}) * og, m1 = mish2((f32x2){a0[2], a0[3]}) * og;
  f32x2 m2 = mish2((f32x2){a1[0], a1[1]}) * og, m3 = mish2((f32x2){a1[2], a1[3]}) * og;
  u32x4 o;
  o[0] = pack2(m0[0], m0[1]); o[1] = pack2(m1[0], m1[1]); o[2] = pack2(m2[0], m2[1]); o[3] = pack2(m3[0], m3[1]);
  return o;
}

DI void scan_task(const Args& a, RSRC rW, char* smem, volatile LAS unsigned* misc, int u, int lane, int rc, int cg, int jl,
                  unsigned tlo, unsigned thi) {
  const int j = 4 * cg + jl;
  int L = 64 * u + lane; L = L < 160 ? L : 159;
  const int seg = L / 40, chl = L - 40 * seg;
  const float* pp = (const float*)(smem + SP_PAR + chl * PARS);
  const f32x2 l1 = *(const f32x2*)pp, l128 = *(const f32x2*)(pp + 20);
  f32x2 pw = seg == 1 ? *(const f32x2*)(pp + 6) : *(const f32x2*)(pp + 16 + (seg == 3 ? 2 : 0));
  if (seg == 0) pw = (f32x2){1.f, 0.f};
  float sr = 0.f, si = 0.f;
  {
    u32x4 ev[15];
    for (int sp = 0; sp < SPIN_G; ++sp) {
      bool ok = true;
#pragma unroll
      for (int r = 0; r < 15; ++r) {
        ev[r] = (u32x4){0u, 0u, tlo, thi};
        if (r < rc) ev[r] = __builtin_amdgcn_raw_buffer_load_b128(rW, OFF_E + ((j * 16 + r) * 40 + chl) * 16, 0, SC1);
      }
#pragma unroll
      for (int r = 0; r < 15; ++r) ok = ok && ev[r][2] == tlo && ev[r][3] == thi;
      if (__all(ok)) break;
      __builtin_amdgcn_s_sleep(2);
      asm volatile("" ::: "memory");
    }
    __builtin_amdgcn_s_setprio(3);
    const float lr = l128[0], li = l128[1], nli = -l128[1];
#pragma unroll
    for (int r = 0; r < 15; ++r)
      if (r < rc) {
        const float tr = sfma(nli, si, __uint_as_float(ev[r][0])), ti = sfma(li, sr, __uint_as_float(ev[r][1]));
        sr = sfma(lr, sr, tr); si = sfma(lr, si, ti);
      }
  }
  lds_wait_ge(misc + M_BURDY, 1u);
  lds_wait_ge(misc + M_BUW, 2u);
  f32x4 br[8], bi[8], gv[8];
  const char* bp = smem + L_BU + 2 * chl * BUS + seg * 128;
#pragma unroll
  for (int i = 0; i < 8; ++i) {
    br[i] = *(const f32x4*)(bp + i * 16); bi[i] = *(const f32x4*)(bp + BUS + i * 16);
    gv[i] = *(const f32x4*)(smem + SP_G + seg * 128 + i * 16);
  }
  const f32x2 ep = *(const f32x2*)(smem + SP_EC + (seg * 80 + 2 * chl) * 4);
  asm volatile("s_waitcnt lgkmcnt(0)" ::: "memory");
  lds_inc(smem, M_SRD, lane);
  {
    const float tr = sfma(-pw[1], si, ep[0]), ti = sfma(pw[1], sr, ep[1]);
    sr = sfma(pw[0], sr, tr); si = sfma(pw[0], si, ti);
  }
  lds_wait_ge(misc + M_SRD, 3u);
  const float lr = l1[0], li = l1[1], nli = -l1[1];
  char* wp = smem + L_HI + seg * 32 * HIS + chl * 2;
#pragma unroll
  for (int t = 0; t < 32; ++t) {
    const float tr = sfma(nli, si, br[t >> 2][t & 3]), ti = sfma(li, sr, bi[t >> 2][t & 3]);
    sr = sfma(lr, sr, tr); si = sfma(lr, si, ti);
    const float g2 = gv[t >> 2][t & 3];
    const unsigned pk = pack2(smul(sr, g2), smul(-si, g2));
    *(u16*)(wp + t * HIS) = (u16)pk;
    *(u16*)(wp + t * HIS + 80) = (u16)(pk >> 16);
  }
  asm volatile("s_waitcnt lgkmcnt(0)" ::: "memory");
  lds_inc(smem, M_SWR, lane);
  if (u == 2) { __builtin_amdgcn_s_setprio(0); return; }
  lds_wait_ge(misc + M_SWR, 3u);
#pragma unroll
  for (int pe = 0; pe < 10; ++pe) {
    const int part = pe / 5, e = pe % 5;
    const int t = 64 * u + lane, pi = 5 * jl + e, step = 12 + 2 * (pi >> 2) + part;
    const u32x4 v = *(const u32x4*)(smem + L_HI + t * HIS + part * 80 + e * 16);
    st128(rW, OFF_AF + (((rc * 4 + cg) * 22 + step) * 8 + (t >> 4)) * 1024 + ((pi & 3) * 16 + (t & 15)) * 16, v);
  }
  WAITV(0);
  if (lds_inc(smem, M_PUB, lane) == 1u) {
    if (lane == 0) set_flag(rW, OFF_FA + (rc * 4 + cg) * 128 + 32 + jl * 8, tlo, thi);
    poll_flags(rW, OFF_FA + (rc * 4 + cg) * 128 + 32, 4, tlo, thi, lane);
    if (lane == 0) misc[M_HOK] = 1u;
  }
  __builtin_amdgcn_s_setprio(0);
}

__global__ __launch_bounds__(512, 2) void fused(Args a) {
  __shared__ __attribute__((aligned(16))) char smem[LDS_BYTES];
  const int tid = threadIdx.x, lane = tid & 63;
  const int wave = __builtin_amdgcn_readfirstlane(tid >> 6);
  const int b = blockIdx.x, grp = b & 7, idx = b >> 3;
  const int cg = grp & 3, jl = idx & 3;
  const int rc = 8 * (grp >> 2) + (idx >> 2), j = 4 * cg + jl;
  const int r16 = lane & 15, g = lane >> 4;
  char* ws = a.ws;
  const RSRC rW = __builtin_amdgcn_make_buffer_rsrc((void*)ws, 0, WS_BYTES, 0x00020000);
  volatile LAS unsigned* misc = (volatile LAS unsigned*)(smem + SP_MISC);
  const unsigned sbase = (unsigned)(size_t)(LAS char*)smem;
  STAMP_DECL
  STAMP(0);

  if (wave >= 4) {
    const int lw = wave - 4;
    {
      unsigned voff[10]; const char* sb_[10]; int inc[10]; unsigned dsto[10];
#pragma unroll
      for (int i = 0; i < 10; ++i) {
        int p = lw + 4 * i; p = p >= 38 ? p - 38 : p;
        dsto[i] = sbase + p * 1024;
        const int rw = lane >> 3;
        const unsigned ox = (8 * p + rw) * (D_ * 2) + 16 * ((lane & 7) ^ (((8 * p + rw) >> 1) & 7));
        const int pb = p < 21 ? p - 16 : p - 21;
        const unsigned ob = (8 * pb + rw) * (D_ * 2) + 16 * ((lane & 7) ^ ((((rw >> 1) & 3) << 1) | (p >= 21 ? 1 : 0)));
        const int ci = 64 * (p >= 26 ? p - 26 : 0) + lane, k = ci / 12, cp = ci % 12;
        int cc = cp - 2 * ((k >> 3) & 1); cc = cc < 0 ? cc + 12 : cc;
        const unsigned ow = k * (F_ * 2) + 16 * cc;
        voff[i] = p < 16 ? ox : p < 26 ? ob : ow;
        inc[i] = p < 26 ? 128 : 64 * F_ * 2;
        sb_[i] = (const char*)(p < 16 ? a.x + (size_t)128 * rc * D_ : p < 21 ? a.Bre + (size_t)40 * j * D_ : p < 26 ? a.Bim + (size_t)40 * j * D_ : a.w1 + 96 * j);
      }
      auto stage = [&](unsigned boff) {
#pragma unroll
        for (int i = 0; i < 10; ++i) { gldss(voff[i], sb_[i], dsto[i] + boff); sb_[i] += inc[i]; }
      };
      stage(0); stage(ST_SZ);
      STAMP(1);
      LBAR(10);
      STAMP(2);
      stage(2 * ST_SZ);
      LBAR(10);
      stage(3 * ST_SZ); stage(0);
#pragma unroll 1
      for (int s = 2; s <= 6; ++s) {
        LBAR(20);
        stage(((s + 3) & 3) * ST_SZ);
      }
      LBAR(20); LBAR(10); LBAR(0);
    }
    STAMP(3);
    const unsigned tlo = misc[M_TAG], thi = misc[M_TAG + 1];

    unsigned woff[10], coff[10];
#pragma unroll
    for (int i = 0; i < 10; ++i) {
      const int ci = 64 * i + lane;
      const int k = ci / 20, cp = ci % 20;
      int cc = cp - 2 * ((k >> 2) & 1); cc = cc < 0 ? cc + 20 : cc;
      woff[i] = k * (D_ * 2) + 16 * cc;
      const int row = ci >> 2, f4 = (row >> 2) & 3, fx = (f4 == 0 ? 0 : f4 == 1 ? 2 : f4 == 2 ? 3 : 1);
      coff[i] = row * (P_ * 2) + 16 * ((ci & 3) ^ fx);
    }
    const char* wbase2 = (const char*)(a.w2 + (size_t)384 * cg * D_ + 160 * jl);
    const char* abase = ws + OFF_AF + (size_t)(rc * 4 + cg) * 22 * 8192;
    const unsigned aoff = lane * 16;
    auto issueB = [&](int n, unsigned sl) {
      if (n < 12) {
        const char* p = wbase2 + (size_t)n * (32 * D_ * 2);
#pragma unroll
        for (int i = 0; i < 10; ++i) gldss(woff[i], p, sl + 8192 + i * 1024);
      } else {
        const char* p = (const char*)(((n & 1) ? a.Cim : a.Cre) + (size_t)160 * jl * P_ + 160 * cg) + ((n - 12) >> 1) * 64;
#pragma unroll
        for (int i = 0; i < 10; ++i) gldss(coff[i], p, sl + 8192 + i * 1024);
      }
    };
    auto issueA = [&](int n, unsigned sl) {
#pragma unroll
      for (int i = 0; i < 8; ++i) gldssc(aoff, abase + (size_t)n * 8192 + i * 1024, sl + i * 1024);
    };
    int p1 = -1, p2 = -1;
    if (lw < 2) {
      scan_task(a, rW, smem, misc, lw, lane, rc, cg, jl, tlo, thi);
      lds_wait_ge(misc + M_FULL, 1u);
      STAMP(6);
    } else {
      const int h = lw - 2;
      lds_wait_ge(misc + M_P1D, 4u);
#pragma unroll 1
      for (int k = 0; k < 3; ++k) issueB(2 * k + h, sbase + (2 * k + h) * SLOT);
      STAMP(4);
      poll_flags(rW, OFF_FA + (rc * 4 + cg) * 128, 4, tlo, thi, lane);
      STAMP(5);
#pragma unroll 1
      for (int k = 0; k < 3; ++k) issueA(2 * k + h, sbase + (2 * k + h) * SLOT);
      WAITV(16); if (lane == 0) misc[M_FULL + h] = 1u;
      WAITV(8);  if (lane == 0) misc[M_FULL + 2 + h] = 1u;
      p2 = 4 + h;
      lds_wait_ge(misc + M_SRD, 3u);
    }
#pragma unroll 1
    for (;;) {
      const int n = (int)lds_inc(smem, M_NEXT, lane);
      if (n >= 22) break;
      const unsigned need = n - NSLOT + 1;
      bool ok = n < NSLOT || (misc[M_CONS] >= need && misc[M_CONS + 1] >= need && misc[M_CONS + 2] >= need && misc[M_CONS + 3] >= need);
      ok = ok && (n < 12 || misc[M_HOK] != 0u);
      if (!ok) {
        WAITV(0);
        if (lane == 0) { if (p1 >= 0) misc[M_FULL + p1] = 1u; if (p2 >= 0) misc[M_FULL + p2] = 1u; }
        p1 = p2 = -1;
        if (n >= NSLOT) {
          lds_wait_ge(misc + M_CONS, need); lds_wait_ge(misc + M_CONS + 1, need); lds_wait_ge(misc + M_CONS + 2, need); lds_wait_ge(misc + M_CONS + 3, need);
        }
        if (n >= 12) lds_wait_ge(misc + M_HOK, 1u);
      }
      asm volatile("" ::: "memory");
      const unsigned sl = sbase + (n % NSLOT) * SLOT;
      issueB(n, sl); issueA(n, sl);
      WAITV(36);
      if (lane == 0 && p1 >= 0) misc[M_FULL + p1] = 1u;
      p1 = p2; p2 = n;
    }
    WAITV(18); if (lane == 0 && p1 >= 0) misc[M_FULL + p1] = 1u;
    WAITV(0);  if (lane == 0 && p2 >= 0) misc[M_FULL + p2] = 1u;
    STAMP(8);
    const int tl = 16 * (lw & 1) + r16, n0 = 160 * jl + 80 * (lw >> 1) + 4 * g;
    const size_t orow = (size_t)(128 * rc + 32 * cg + tl) * D_ + n0;
    f32x4 sk[5];
    {
      const float gt = 0.5f * *(const float*)(smem + SP_G + (32 * cg + tl) * 4);
#pragma unroll
      for (int i = 0; i < 5; ++i) {
        u32x2 xv = *(const u32x2*)(a.x + orow + 16 * i);
        u32x2 dv = *(const u32x2*)(a.Dsk + n0 + 16 * i);
        sk[i][0] = gt * bflo(xv[0]) * bflo(dv[0]); sk[i][1] = gt * bfhi(xv[0]) * bfhi(dv[0]);
        sk[i][2] = gt * bflo(xv[1]) * bflo(dv[1]); sk[i][3] = gt * bfhi(xv[1]) * bfhi(dv[1]);
      }
    }
    STAMP(22);
    for (int sp = 0; misc[M_RED] < 4u && sp < SPIN_LDS; ++sp) __builtin_amdgcn_s_sleep(4);
    asm volatile("" ::: "memory");
    STAMP(23);
    poll_flags(rW, OFF_FR + (rc * 4 + jl) * 128, 4, tlo, thi, lane);
    STAMP(24);
    if (rc == 15 && j == 0 && tid == 256) set_flag(rW, OFF_EPOCH, tlo, thi);
    u32x2 pv[3][5];
#pragma unroll
    for (int s = 0; s < 3; ++s) {
      const int sc = (cg + 1 + s) & 3;
#pragma unroll
      for (int i = 0; i < 5; ++i)
        pv[s][i] = __builtin_amdgcn_raw_buffer_load_b64(rW, OFF_PB + (((rc * 4 + jl) * 4 + cg) * 4 + sc) * 10240 + (lw >> 1) * 5120 + i * 1024 + lane * 16 + (lw & 1) * 8, 0, SC1);
    }
#pragma unroll
    for (int i = 0; i < 5; ++i) {
      f32x4 v = *(const f32x4*)(smem + (5 * lw + i) * 1024 + lane * 16) + sk[i];
#pragma unroll
      for (int s = 0; s < 3; ++s) v += (f32x4){bflo(pv[s][i][0]), bfhi(pv[s][i][0]), bflo(pv[s][i][1]), bfhi(pv[s][i][1])};
      *(f32x4*)(a.out + orow + 16 * i) = v;
    }
  } else {
    if (wave == 1) {
      const u64 ep = __hip_atomic_load((GAS u64*)(ws + OFF_EPOCH), __ATOMIC_RELAXED, __HIP_MEMORY_SCOPE_AGENT);
      const unsigned v = *(const unsigned*)(a.b1 + 96 * j + 2 * (lane < 48 ? lane : 0));
      const u32x4 g0 = *(const u32x4*)(a.gw + 8 * lane), g1 = *(const u32x4*)(a.gw + 8 * (64 + r16));
      if (lane < 48) *(f32x2*)(smem + SP_B1 + lane * 8) = (f32x2){bflo(v), bfhi(v)};
      *(u32x4*)(smem + SP_GW + lane * 16) = g0;
      if (lane < 16) *(u32x4*)(smem + SP_GW + 1024 + lane * 16) = g1;
      if (lane < 24 || lane >= 32) misc[lane] = lane == M_NEXT ? 6u : 0u;
      if (lane == 0) { const u64 tg = ep + 1; misc[M_TAG] = (unsigned)tg; misc[M_TAG + 1] = (unsigned)(tg >> 32); }
    } else if (wave == 2) {
      const int ch = 40 * j + (lane < 40 ? lane : 0);
      const float lre = bflo(a.Lre[ch]), lim = bflo(a.Lim[ch]), dt = expf(bflo(a.ldt[ch]));
      float e = expf(dt * lre), sn, cs;
      sincosf(dt * lim, &sn, &cs);
      f32x2 l1 = {e * cs, e * sn};
      const float nr = l1[0] - 1.f, ni = l1[1], den = lre * lre + lim * lim;
      f32x2 cf = {(nr * lre + ni * lim) / den, (ni * lre - nr * lim) / den};
      f32x2 l2 = cm2(l1, l1), l4 = cm2(l2, l2), l8 = cm2(l4, l4), l12 = cm2(l8, l4), l16 = cm2(l8, l8);
      f32x2 l32 = cm2(l16, l16), l64 = cm2(l32, l32), l128 = cm2(l64, l64), l96 = cm2(l64, l32);
      if (lane < 40) {
        float* pp = (float*)(smem + SP_PAR + lane * PARS);
        *(f32x4*)(pp) = (f32x4){l1[0], l1[1], l16[0], l16[1]};
        *(f32x4*)(pp + 4) = (f32x4){cf[0], cf[1], l32[0], l32[1]};
        *(f32x4*)(pp + 8) = (f32x4){l12[0], l12[1], l8[0], l8[1]};
        *(f32x4*)(pp + 12) = (f32x4){l4[0], l4[1], 1.f, 0.f};
        *(f32x4*)(pp + 16) = (f32x4){l64[0], l64[1], l96[0], l96[1]};
        *(f32x2*)(pp + 20) = l128;
      }
    }
    STAMP(1);
    const int th = wave & 1;
    const unsigned xo0 = ST_X + (64 * th + r16) * 128 + ((g ^ (r16 >> 1)) * 16);
    const unsigned xo1 = xo0 ^ 64;
    const int q4 = r16 >> 2, p4 = r16 & 3;

#define P1_PIPE(LOADF, STEP, MMA, INIT)                                             \
    CBAR;                                                                           \
    __builtin_amdgcn_s_setprio(3);                                                  \
    STAMP(2);                                                                       \
    INIT;                                                                           \
    LOADF(F0, 0u, 0);                                                               \
    STEP(F0, F1, 0u, 1);                                                            \
    _Pragma("unroll 1") for (int s = 1; s < 10; ++s) {                              \
      CBAR;                                                                         \
      STEP(F1, F0, (s & 3) * ST_SZ, 0);                                             \
      STEP(F0, F1, (s & 3) * ST_SZ, 1);                                             \
    }                                                                               \
    asm volatile("s_waitcnt lgkmcnt(0)" ::: "memory");                              \
    lds_inc(smem, M_P1D, lane);                                                     \
    MMA(F1); SCHED; __builtin_amdgcn_s_setprio(0);

    unsigned tlo, thi;
    if (wave < 2) {
      f32x4 acc[6][4];
      const unsigned wbase = ST_W1 + (8 * g + q4) * 192 + (p4 >> 1) * 16 + (p4 & 1) * 8;
      const unsigned wa = wbase + (g & 1) * 32;
      const unsigned wb = wbase + ((g & 1) ? 0 : 160);
      struct Frag { bf16x8 x[4], w[6]; };
      Frag F0, F1;
      auto rdw = [&](const char* sb, int ft, int kk) {
        unsigned ad = (ft < 5 ? wa + 32 * ft : wb) + kk * 32 * 192;
        s16x4 lo = __builtin_amdgcn_ds_read_tr16_b64_v4i16((LAS s16x4*)(sb + ad));
        s16x4 hi = __builtin_amdgcn_ds_read_tr16_b64_v4i16((LAS s16x4*)(sb + ad + 4 * 192));
        return __builtin_bit_cast(bf16x8, __builtin_shufflevector(lo, hi, 0, 1, 2, 3, 4, 5, 6, 7));
      };
      auto loadf = [&](Frag& F, unsigned boff, int kk) {
        const char* sb = smem + boff;
#pragma unroll
        for (int tt = 0; tt < 4; ++tt) F.x[tt] = *(const bf16x8*)(sb + (kk ? xo1 : xo0) + tt * 2048);
#pragma unroll
        for (int ft = 0; ft < 6; ++ft) F.w[ft] = rdw(sb, ft, kk);
      };
      auto mma = [&](Frag& F) {
#pragma unroll
        for (int m = 0; m < 24; ++m)
          acc[m >> 2][m & 3] = __builtin_amdgcn_mfma_f32_16x16x32_bf16(F.w[m >> 2], F.x[m & 3], acc[m >> 2][m & 3], 0, 0, 0);
      };
      auto step = [&](Frag& C, Frag& N, unsigned boff, const int kk) {
        const char* sb = smem + boff;
#pragma unroll
        for (int m = 0; m < 24; ++m) {
          SCHED;
          acc[m >> 2][m & 3] = __builtin_amdgcn_mfma_f32_16x16x32_bf16(C.w[m >> 2], C.x[m & 3], acc[m >> 2][m & 3], 0, 0, 0);
          SCHED;
          if (m < 4) N.x[m] = *(const bf16x8*)(sb + (kk ? xo1 : xo0) + m * 2048);
          if (m >= 4 && m < 10) N.w[m - 4] = rdw(sb, m - 4, kk);
        }
        SCHED;
      };
      auto init = [&]() {
#pragma unroll
        for (int ft = 0; ft < 6; ++ft) {
          f32x4 bv = *(const f32x4*)(smem + SP_B1 + (16 * ft + 4 * g) * 4);
#pragma unroll
          for (int tt = 0; tt < 4; ++tt) acc[ft][tt] = bv;
        }
      };
      P1_PIPE(loadf, step, mma, init());
      STAMP(3);
      tlo = misc[M_TAG]; thi = misc[M_TAG + 1];
      lds_wait_ge(misc + M_GRDY, 2u);
#pragma unroll
      for (int tt = 0; tt < 4; ++tt) {
        const float og = 1.f - 0.5f * *(const float*)(smem + SP_G + (64 * th + 16 * tt + r16) * 4);
#pragma unroll
        for (int fp = 0; fp < 3; ++fp)
          st128(rW, OFF_AF + (((rc * 4 + cg) * 22 + 3 * jl + fp) * 8 + 4 * th + tt) * 1024 + lane * 16,
                mish_unit(acc[2 * fp][tt], acc[2 * fp + 1][tt], og));
      }
      STAMP(4);
      WAITV(0);
      if (lds_inc(smem, M_ACT, lane) == 1u && lane == 0) set_flag(rW, OFF_FA + (rc * 4 + cg) * 128 + jl * 8, tlo, thi);
      STAMP(5);
    } else {
      f32x4 acc[4][5], gac[4];
      const unsigned bo0 = ST_BM + (40 * (r16 & 1) + (r16 >> 1)) * 128 + ((g ^ ((((r16 >> 2) & 3) << 1) | (r16 & 1))) * 16);
      const unsigned bo1 = bo0 ^ 64;
      struct Frag { bf16x8 x[4], w[5], gf; };
      Frag F0, F1;
      int kc = 0;
      auto loadf = [&](Frag& F, unsigned boff, int kk) {
        const char* sb = smem + boff;
#pragma unroll
        for (int tt = 0; tt < 4; ++tt) F.x[tt] = *(const bf16x8*)(sb + (kk ? xo1 : xo0) + tt * 2048);
#pragma unroll
        for (int nt = 0; nt < 5; ++nt) F.w[nt] = *(const bf16x8*)(sb + (kk ? bo1 : bo0) + nt * 1024);
        F.gf = *(const bf16x8*)(smem + SP_GW + kc * 64 + g * 16);
        ++kc;
      };
      auto mma = [&](Frag& F) {
#pragma unroll
        for (int tt = 0; tt < 4; ++tt) gac[tt] = __builtin_amdgcn_mfma_f32_16x16x32_bf16(F.x[tt], F.gf, gac[tt], 0, 0, 0);
#pragma unroll
        for (int m = 0; m < 20; ++m)
          acc[m & 3][m >> 2] = __builtin_amdgcn_mfma_f32_16x16x32_bf16(F.x[m & 3], F.w[m >> 2], acc[m & 3][m >> 2], 0, 0, 0);
      };
      auto step = [&](Frag& C, Frag& N, unsigned boff, const int kk) {
        const char* sb = smem + boff;
#pragma unroll
        for (int m = 0; m < 24; ++m) {
          SCHED;
          if (m < 20) acc[m & 3][m >> 2] = __builtin_amdgcn_mfma_f32_16x16x32_bf16(C.x[m & 3], C.w[m >> 2], acc[m & 3][m >> 2], 0, 0, 0);
          else gac[m & 3] = __builtin_amdgcn_mfma_f32_16x16x32_bf16(C.x[m & 3], C.gf, gac[m & 3], 0, 0, 0);
          SCHED;
          if (m < 4) N.x[m] = *(const bf16x8*)(sb + (kk ? xo1 : xo0) + m * 2048);
          if (m >= 4 && m < 9) N.w[m - 4] = *(const bf16x8*)(sb + (kk ? bo1 : bo0) + (m - 4) * 1024);
          if (m == 9) N.gf = *(const bf16x8*)(smem + SP_GW + kc * 64 + g * 16);
        }
        ++kc;
        SCHED;
      };
      float l1r[5], l1s[5], l16r[5], l16s[5], wgr[5], wgs[5], cfr[5], cfs[5];
      auto init = [&]() {
#pragma unroll
        for (int tt = 0; tt < 4; ++tt) {
          gac[tt] = (f32x4){0.f, 0.f, 0.f, 0.f};
#pragma unroll
          for (int nt = 0; nt < 5; ++nt) acc[tt][nt] = (f32x4){0.f, 0.f, 0.f, 0.f};
        }
        const float sgn = (r16 & 1) ? 1.f : -1.f;
#pragma unroll
        for (int nt = 0; nt < 5; ++nt) {
          const float* pp = (const float*)(smem + SP_PAR + (8 * nt + (r16 >> 1)) * PARS);
          const f32x4 p0 = *(const f32x4*)pp;
          const f32x2 cf = *(const f32x2*)(pp + 4);
          const f32x2 wg = *(const f32x2*)(pp + 8 + 2 * g);
          l1r[nt] = p0[0]; l1s[nt] = sgn * p0[1]; l16r[nt] = p0[2]; l16s[nt] = sgn * p0[3];
          wgr[nt] = wg[0]; wgs[nt] = sgn * wg[1]; cfr[nt] = cf[0]; cfs[nt] = sgn * cf[1];
        }
      };
      P1_PIPE(loadf, step, mma, init());
      STAMP(3);
      tlo = misc[M_TAG]; thi = misc[M_TAG + 1];
      {
        f32x4 lg = r16 == 0 ? gac[0] : r16 == 1 ? gac[1] : r16 == 2 ? gac[2] : gac[3];
        f32x4 gg;
#pragma unroll
        for (int r = 0; r < 4; ++r) gg[r] = 2.f * __builtin_amdgcn_rcpf(1.f + __expf(-lg[r]));
        if (r16 < 4) *(f32x4*)(smem + SP_G + (64 * th + 16 * r16 + 4 * g) * 4) = gg;
        asm volatile("s_waitcnt lgkmcnt(0)" ::: "memory");
        lds_inc(smem, M_GRDY, lane);
      }
      {
        float hh[4][5];
#pragma unroll
        for (int tt = 0; tt < 4; ++tt)
#pragma unroll
          for (int nt = 0; nt < 5; ++nt) hh[tt][nt] = acc[tt][nt][0];
#pragma unroll
        for (int r = 1; r < 4; ++r)
#pragma unroll
          for (int tt = 0; tt < 4; ++tt)
#pragma unroll
            for (int nt = 0; nt < 5; ++nt) hh[tt][nt] = cmuladd(l1r[nt], l1s[nt], hh[tt][nt], acc[tt][nt][r]);
        float ee[2][5];
#pragma unroll
        for (int hv = 0; hv < 2; ++hv)
#pragma unroll
          for (int nt = 0; nt < 5; ++nt) ee[hv][nt] = cmuladd(l16r[nt], l16s[nt], hh[2 * hv][nt], hh[2 * hv + 1][nt]);
#pragma unroll
        for (int hv = 0; hv < 2; ++hv)
#pragma unroll
          for (int nt = 0; nt < 5; ++nt) ee[hv][nt] = cmul(wgr[nt], wgs[nt], ee[hv][nt]);
#pragma unroll
        for (int hv = 0; hv < 2; ++hv)
#pragma unroll
          for (int nt = 0; nt < 5; ++nt) ee[hv][nt] = sum16(ee[hv][nt]);
#pragma unroll
        for (int hv = 0; hv < 2; ++hv)
#pragma unroll
          for (int nt = 0; nt < 5; ++nt) ee[hv][nt] = sum32(ee[hv][nt]);
        if (g == 0) {
#pragma unroll
          for (int hv = 0; hv < 2; ++hv)
#pragma unroll
            for (int nt = 0; nt < 5; ++nt) *(float*)(smem + SP_EC + ((2 * th + hv) * 80 + 16 * nt + r16) * 4) = ee[hv][nt];
        }
      }
      asm volatile("s_waitcnt lgkmcnt(0)" ::: "memory");
      const bool chain = lds_inc(smem, M_CNTE, lane) == 1u;
      if (chain) {
        asm volatile("" ::: "memory");
#pragma unroll
        for (int rnd = 0; rnd < 2; ++rnd) {
          const int col = rnd == 0 ? lane : 64 + r16;
          const float sg = (col & 1) ? 1.f : -1.f;
          const f32x4 p1_ = *(const f32x4*)(smem + SP_PAR + (col >> 1) * PARS + 16);
          float* ec = (float*)(smem + SP_EC) + col;
          const float e1 = ec[0];
          const float e2 = cmuladd(p1_[2], sg * p1_[3], e1, ec[80]);
          const float e3 = cmuladd(p1_[2], sg * p1_[3], e2, ec[160]);
          const float e4 = cmul(p1_[0], sg * p1_[1], cmuladd(p1_[2], sg * p1_[3], e3, ec[240]));
          const float e4o = dswap(e4);
          if ((rnd == 0 || lane < 16) && !(col & 1))
            st128(rW, OFF_E + ((j * 16 + rc) * 40 + (col >> 1)) * 16, (u32x4){__float_as_uint(e4), __float_as_uint(e4o), tlo, thi});
          const float o1 = cmul(p1_[0], sg * p1_[1], e1), o2 = cmul(p1_[0], sg * p1_[1], e2), o3 = cmul(p1_[0], sg * p1_[1], e3);
          if (rnd == 0 || lane < 16) { ec[0] = 0.f; ec[80] = o1; ec[160] = o2; ec[240] = o3; }
        }
        asm volatile("s_waitcnt lgkmcnt(0)" ::: "memory");
        if (lane == 0) misc[M_BURDY] = 1u;
      }
      {
        char* bw = smem + L_BU + r16 * BUS + (64 * th + 4 * g) * 4;
#pragma unroll
        for (int tt = 0; tt < 4; ++tt)
#pragma unroll
          for (int nt = 0; nt < 5; ++nt) {
            f32x4 bu;
#pragma unroll
            for (int r = 0; r < 4; ++r) bu[r] = cmul(cfr[nt], cfs[nt], acc[tt][nt][r]);
            *(f32x4*)(bw + nt * 16 * BUS + tt * 64) = bu;
          }
        asm volatile("s_waitcnt lgkmcnt(0)" ::: "memory");
        lds_inc(smem, M_BUW, lane);
      }
      STAMP(4);
      if (wave == 3) scan_task(a, rW, smem, misc, 2, lane, rc, cg, jl, tlo, thi);
      STAMP(5);
    }

    const int wn = wave & 1, wt = wave >> 1;
    f32x4 acc[5][4];
#pragma unroll
    for (int nt = 0; nt < 5; ++nt)
#pragma unroll
      for (int tt = 0; tt < 4; ++tt) acc[nt][tt] = (f32x4){0.f, 0.f, 0.f, 0.f};
    struct Frag2 { bf16x8 x[4], w[5]; };
    Frag2 G0, G1;
    const unsigned ao = 4 * wt * 1024 + lane * 16;
    const unsigned twb = 8192 + (4 * g + q4) * 320 + (p4 >> 1) * 16 + (p4 & 1) * 8;
    const unsigned twa = twb + (5 * wn + (g & 1)) * 32;
    const unsigned twl = (wn == 1 && (g & 1)) ? twb : twa + 128;
    const int f4 = (r16 >> 2) & 3, fx = (f4 == 0 ? 0 : f4 == 1 ? 2 : f4 == 2 ? 3 : 1);
    const unsigned cwo = 8192 + (80 * wn + r16) * 64 + ((g ^ fx) * 16);
    auto rd2 = [&](Frag2& N, const char* sb, int e, const bool ssm) {
      if (e < 4) N.x[e] = *(const bf16x8*)(sb + ao + e * 1024);
      else if (ssm) N.w[e - 4] = *(const bf16x8*)(sb + cwo + (e - 4) * 1024);
      else {
        const unsigned ad = e - 4 < 4 ? twa + 32 * (e - 4) : twl;
        s16x4 lo = __builtin_amdgcn_ds_read_tr16_b64_v4i16((LAS s16x4*)(sb + ad));
        s16x4 hi = __builtin_amdgcn_ds_read_tr16_b64_v4i16((LAS s16x4*)(sb + ad + 16 * 320));
        N.w[e - 4] = __builtin_bit_cast(bf16x8, __builtin_shufflevector(lo, hi, 0, 1, 2, 3, 4, 5, 6, 7));
      }
    };
    auto wait_full = [&](int st) { lds_wait_ge(misc + M_FULL + st, 1u); };
    int slot = 0;
    unsigned fpre = 0;
    auto step2 = [&](Frag2& C, Frag2& N, int n, const bool next_ssm, const bool last) {
      if (!last && fpre == 0u) wait_full(n + 1);
      asm volatile("s_waitcnt lgkmcnt(0)" ::: "memory");
      const char* sb = smem + slot * SLOT;
      slot = slot == NSLOT - 1 ? 0 : slot + 1;
#pragma unroll
      for (int m = 0; m < 20; ++m) {
        SCHED;
        acc[m >> 2][m & 3] = __builtin_amdgcn_mfma_f32_16x16x32_bf16(C.w[m >> 2], C.x[m & 3], acc[m >> 2][m & 3], 0, 0, 0);
        SCHED;
        if (!last && m < 9) rd2(N, sb, m, next_ssm);
        if (m == 9) { if (lane == 0) misc[M_CONS + wave] = n + 1; }
        if (m == 12) fpre = misc[M_FULL + n + 2];
      }
      SCHED;
    };
    wait_full(0);
    __builtin_amdgcn_s_setprio(1);
    STAMP(6);
#pragma unroll
    for (int e = 0; e < 9; ++e) rd2(G0, smem, e, false);
    slot = 1;
#pragma unroll 1
    for (int n = 0; n < 10; n += 2) { step2(G0, G1, n, false, false); step2(G1, G0, n + 1, false, false); }
    STAMP(7);
    step2(G0, G1, 10, false, false); step2(G1, G0, 11, true, false);
    STAMP(8);
#pragma unroll 1
    for (int n = 12; n < 20; n += 2) { step2(G0, G1, n, true, false); step2(G1, G0, n + 1, true, false); }
    step2(G0, G1, 20, true, false); step2(G1, G0, 21, true, true);
    __builtin_amdgcn_s_setprio(0);
    STAMP(9);
#pragma unroll
    for (int t2 = 0; t2 < 2; ++t2) {
      const int owner = 2 * wt + t2;
      if (owner != cg) {
        const unsigned pb = OFF_PB + (((rc * 4 + jl) * 4 + owner) * 4 + cg) * 10240 + wn * 5120 + lane * 16;
#pragma unroll
        for (int nt = 0; nt < 5; ++nt) {
          const f32x4 v0 = acc[nt][2 * t2], v1 = acc[nt][2 * t2 + 1];
          u32x4 o; o[0] = pack2(v0[0], v0[1]); o[1] = pack2(v0[2], v0[3]); o[2] = pack2(v1[0], v1[1]); o[3] = pack2(v1[2], v1[3]);
          st128(rW, pb + nt * 1024, o);
        }
      }
    }
    lds_wait_ge(misc + M_CONS, 22u); lds_wait_ge(misc + M_CONS + 1, 22u); lds_wait_ge(misc + M_CONS + 2, 22u); lds_wait_ge(misc + M_CONS + 3, 22u);
#pragma unroll
    for (int tt = 0; tt < 4; ++tt) {
      if (2 * wt + (tt >> 1) == cg) {
#pragma unroll
        for (int nt = 0; nt < 5; ++nt) *(f32x4*)(smem + (5 * (2 * wn + (tt & 1)) + nt) * 1024 + lane * 16) = acc[nt][tt];
      }
    }
    STAMP(10);
    asm volatile("s_waitcnt vmcnt(0) lgkmcnt(0)" ::: "memory");
    if (lds_inc(smem, M_RED + 1, lane) == 3u) {
      if (lane == 0) { set_flag(rW, OFF_FR + (rc * 4 + jl) * 128 + cg * 8, tlo, thi); misc[M_RED] = 4u; }
    }
    STAMP(11);
  }
  STAMP(25);
  STAMP(26);
  STAMP_OUT(0);
}

extern "C" void kernel_launch(void* const* d_in, const int* in_sizes, int n_in,
                              void* d_out, int out_size, void* d_ws, size_t ws_size,
                              hipStream_t stream) {
  Args a;
  a.x   = (const u16*)d_in[0];
  a.Lre = (const u16*)d_in[1];
  a.Lim = (const u16*)d_in[2];
  a.Bre = (const u16*)d_in[3];
  a.Bim = (const u16*)d_in[4];
  a.Cre = (const u16*)d_in[5];
  a.Cim = (const u16*)d_in[6];
  a.ldt = (const u16*)d_in[7];
  a.Dsk = (const u16*)d_in[8];
  a.w1  = (const u16*)d_in[10];
  a.b1  = (const u16*)d_in[11];
  a.w2  = (const u16*)d_in[12];
  a.gw  = (const u16*)d_in[13];
  a.ws  = (char*)d_ws;
  a.out = (float*)d_out;
  fused<<<256, 512, 0, stream>>>(a);
}
```

```cpp
#include <hip/hip_runtime.h>
#include <hip/hip_bf16.h>
#include <stdint.h>

typedef __attribute__((ext_vector_type(8))) __bf16 bf16x8;
typedef __attribute__((ext_vector_type(2))) __bf16 bf16x2;
typedef __attribute__((ext_vector_type(4))) float f32x4;
typedef __attribute__((ext_vector_type(2))) float f32x2;
typedef __attribute__((ext_vector_type(4))) unsigned u32x4;
typedef __attribute__((ext_vector_type(2))) unsigned u32x2;
typedef __attribute__((ext_vector_type(4))) short s16x4;
typedef unsigned short u16;
#define LAS __attribute__((address_space(3)))
#define GAS __attribute__((address_space(1)))
#define DI __device__ __forceinline__

constexpr int T_ = 2048, D_ = 640, P_ = 640, F_ = 1536;

constexpr unsigned OFF_AF = 0;
constexpr unsigned OFF_PB = OFF_AF + 64u * 22 * 8192;
constexpr unsigned OFF_E  = OFF_PB + 64u * 16 * 10240;
constexpr unsigned OFF_FA = OFF_E + 256u * 640;
constexpr unsigned OFF_FE = OFF_FA + 64 * 128;
constexpr unsigned OFF_FR = OFF_FE + 16 * 128;
constexpr unsigned OFF_EPOCH = OFF_FR + 64 * 128;
constexpr unsigned WS_BYTES = OFF_EPOCH + 256;

#define STAMP(i)
#define STAMP_DECL
#define STAMP_OUT(kid)

DI float bflo(unsigned v) { return __uint_as_float(v << 16); }
DI float bfhi(unsigned v) { return __uint_as_float(v & 0xffff0000u); }
DI unsigned pack2(float a, float b) {
  bf16x2 r; r[0] = (__bf16)a; r[1] = (__bf16)b; return __builtin_bit_cast(unsigned, r);
}
DI float dswap(float v) {
  return __int_as_float(__builtin_amdgcn_mov_dpp(__float_as_int(v), 0xB1, 0xf, 0xf, true));
}
DI float cmul(float lr, float ls, float v) { return fmaf(lr, v, ls * dswap(v)); }
DI float cmuladd(float lr, float ls, float v, float a) { return fmaf(lr, v, fmaf(ls, dswap(v), a)); }
DI f32x2 cm2(f32x2 a, f32x2 b) { return (f32x2){a[0] * b[0] - a[1] * b[1], a[0] * b[1] + a[1] * b[0]}; }
DI float sfma(float a, float b, float c) { float d; asm("v_fma_f32 %0, %1, %2, %3" : "=v"(d) : "v"(a), "v"(b), "v"(c)); return d; }
DI float smul(float a, float b) { float d; asm("v_mul_f32 %0, %1, %2" : "=v"(d) : "v"(a), "v"(b)); return d; }

DI f32x2 mish2(f32x2 h) {
  f32x2 z = h * 1.44269504088896341f;
  f32x2 w; w[0] = __builtin_amdgcn_exp2f(fminf(z[0], 43.f)); w[1] = __builtin_amdgcn_exp2f(fminf(z[1], 43.f));
  f32x2 n = w * (w + 2.f);
  f32x2 d = n + 2.f;
  f32x2 r; r[0] = __builtin_amdgcn_rcpf(d[0]); r[1] = __builtin_amdgcn_rcpf(d[1]);
  return (h * n) * r;
}

DI void glds16(const void* gsrc, unsigned lds_dst) {
  unsigned keep;
  asm volatile("s_mov_b32 %0, m0\n\ts_mov_b32 m0, %2\n\ts_nop 0\n\tglobal_load_lds_dwordx4 %1, off\n\ts_mov_b32 m0, %0"
               : "=&s"(keep) : "v"(gsrc), "s"(lds_dst) : "memory");
}
DI void touch(const void* gsrc, unsigned lds_dst) {
  unsigned keep;
  asm volatile("s_mov_b32 %0, m0\n\ts_mov_b32 m0, %2\n\ts_nop 0\n\tglobal_load_lds_dword %1, off\n\ts_mov_b32 m0, %0"
               : "=&s"(keep) : "v"(gsrc), "s"(lds_dst) : "memory");
}
DI float sum16(float v) {
  auto r = __builtin_amdgcn_permlane16_swap(__float_as_uint(v), __float_as_uint(v), false, false);
  return __uint_as_float(r[0]) + __uint_as_float(r[1]);
}
DI float sum32(float v) {
  auto r = __builtin_amdgcn_permlane32_swap(__float_as_uint(v), __float_as_uint(v), false, false);
  return __uint_as_float(r[0]) + __uint_as_float(r[1]);
}

typedef unsigned long long u64;
#define SC1 16
#define SCHED __builtin_amdgcn_sched_barrier(0)
#define RSRC __amdgpu_buffer_rsrc_t
DI void st128(RSRC r, unsigned off, u32x4 v) { __builtin_amdgcn_raw_buffer_store_b128(v, r, off, 0, SC1); }
DI void st128f(RSRC r, unsigned off, f32x4 v) { __builtin_amdgcn_raw_buffer_store_b128(__builtin_bit_cast(u32x4, v), r, off, 0, SC1); }
DI void st64(RSRC r, unsigned off, u32x2 v) { __builtin_amdgcn_raw_buffer_store_b64(v, r, off, 0, SC1); }
DI f32x4 ld128f(RSRC r, unsigned off) { return __builtin_bit_cast(f32x4, __builtin_amdgcn_raw_buffer_load_b128(r, off, 0, SC1)); }
DI f32x2 ld64f(RSRC r, unsigned off) { return __builtin_bit_cast(f32x2, __builtin_amdgcn_raw_buffer_load_b64(r, off, 0, SC1)); }
DI void glds16c(const void* gsrc, unsigned lds_dst) {
  unsigned keep;
  asm volatile("s_mov_b32 %0, m0\n\ts_mov_b32 m0, %2\n\ts_nop 0\n\tglobal_load_lds_dwordx4 %1, off sc1\n\ts_mov_b32 m0, %0"
               : "=&s"(keep) : "v"(gsrc), "s"(lds_dst) : "memory");
}

DI void gldss(unsigned voff, const void* sbase_, unsigned lds_dst) {
  asm volatile("s_mov_b32 m0, %2\n\ts_nop 0\n\tglobal_load_lds_dwordx4 %0, %1" :: "v"(voff), "s"(sbase_), "s"(lds_dst) : "memory", "m0");
}
DI void gldssc(unsigned voff, const void* sbase_, unsigned lds_dst) {
  asm volatile("s_mov_b32 m0, %2\n\ts_nop 0\n\tglobal_load_lds_dwordx4 %0, %1 sc1" :: "v"(voff), "s"(sbase_), "s"(lds_dst) : "memory", "m0");
}

constexpr int ST_X = 0, ST_BM = 16384, ST_W1 = 26624, ST_SZ = 38912, NSTG = 4;
constexpr int SP = NSTG * ST_SZ;
constexpr int PARS = 96;
constexpr int SP_PAR = SP, SP_B1 = SP + 3840, SP_EC = SP + 4224, SP_EL = SP + 5504, SP_GW = SP + 5824, SP_G = SP + 7104,
              SP_MISC = SP + 7616, LDS_BYTES = SP + 7872;
constexpr int BUS = 528, L_BU = SP - 80 * BUS;
constexpr int HIS = 176, L_HI = SP - 128 * HIS;
constexpr int SLOT = 18432, NSLOT = 7;
enum { M_CNTE = 0, M_GRDY, M_P1D, M_ACT, M_BURDY, M_SRD, M_SWR, M_PUB, M_RED, M_NEXT = 10, M_CONS = 12, M_OFF = 16, M_HOK = 18, M_BUW = 19, M_TAG = 24, M_FULL = 32 };
constexpr int SPIN_LDS = 1 << 22, SPIN_G = 1 << 19;

struct Args {
  const u16 *x, *Lre, *Lim, *Bre, *Bim, *Cre, *Cim, *ldt, *Dsk, *w1, *b1, *w2, *gw;
  char* ws;
  float* out;
};

#define CBAR do { SCHED; asm volatile("s_waitcnt lgkmcnt(0)\n\ts_barrier" ::: "memory"); SCHED; } while (0)
#define LBAR(n) asm volatile("s_waitcnt vmcnt(" #n ")\n\ts_barrier" ::: "memory")
#define WAITV(n) asm volatile("s_waitcnt vmcnt(" #n ")" ::: "memory")

DI void lds_wait_ge(volatile LAS unsigned* w, unsigned v) {
  for (int sp = 0; *w < v && sp < SPIN_LDS; ++sp) __builtin_amdgcn_s_sleep(1);
  asm volatile("" ::: "memory");
}
DI unsigned lds_inc(char* smem, int word, int lane) {
  unsigned old = 0;
  if (lane == 0) old = __hip_atomic_fetch_add((LAS unsigned*)(smem + SP_MISC) + word, 1u, __ATOMIC_RELAXED, __HIP_MEMORY_SCOPE_WORKGROUP);
  return __builtin_amdgcn_readfirstlane(old);
}
DI void poll_flags(RSRC rW, unsigned off, int n, unsigned tlo, unsigned thi, int lane) {
  const unsigned o = off + (lane < n ? lane : 0) * 8;
  for (int sp = 0; n > 0 && sp < SPIN_G; ++sp) {
    const u32x2 f = __builtin_amdgcn_raw_buffer_load_b64(rW, o, 0, SC1);
    if (__all(f[0] == tlo && f[1] == thi)) break;
    __builtin_amdgcn_s_sleep(1);
    asm volatile("" ::: "memory");
  }
  asm volatile("" ::: "memory");
}
DI void set_flag(RSRC rW, unsigned off, unsigned tlo, unsigned thi) { u32x2 t; t[0] = tlo; t[1] = thi; st64(rW, off, t); }

DI u32x4 mish_unit(f32x4 a0, f32x4 a1, float og) {
  f32x2 m0 = mish2((f32x2){a0[0], a0[1]}) * og, m1 = mish2((f32x2){a0[2], a0[3]}) * og;
  f32x2 m2 = mish2((f32x2){a1[0], a1[1]}) * og, m3 = mish2((f32x2){a1[2], a1[3]}) * og;
  u32x4 o;
  o[0] = pack2(m0[0], m0[1]); o[1] = pack2(m1[0], m1[1]); o[2] = pack2(m2[0], m2[1]); o[3] = pack2(m3[0], m3[1]);
  return o;
}

DI void scan_task(RSRC rW, char* smem, volatile LAS unsigned* misc, int u, int lane, int rc, int j, unsigned tlo, unsigned thi) {
  int L = 64 * u + lane; const bool act = L < 160; L = act ? L : 159;
  const int seg = L / 40, chl = L - 40 * seg;
  const float* pp = (const float*)(smem + SP_PAR + chl * PARS);
  const f32x2 l1 = *(const f32x2*)pp, l128 = *(const f32x2*)(pp + 20);
  const f32x4 cl = *(const f32x4*)(pp + 4);
  f32x2 pw = seg == 1 ? (f32x2){cl[2], cl[3]} : *(const f32x2*)(pp + 16 + (seg == 3 ? 2 : 0));
  if (seg == 0) pw = (f32x2){1.f, 0.f};
  const float lr = l1[0], li = l1[1], nli = -l1[1];
  lds_wait_ge(misc + M_BUW, 2u);
  f32x4 ar[8], ai[8], wr[8], wi[8];
  {
    const char* bp = smem + L_BU + 2 * chl * BUS + seg * 128;
#pragma unroll
    for (int i = 0; i < 8; ++i) { ar[i] = *(const f32x4*)(bp + i * 16); ai[i] = *(const f32x4*)(bp + BUS + i * 16); }
  }
  float sr = 0.f, si = 0.f;
#pragma unroll
  for (int t = 0; t < 32; ++t) {
    const float tr = fmaf(nli, si, ar[t >> 2][t & 3]), ti = fmaf(li, sr, ai[t >> 2][t & 3]);
    sr = fmaf(lr, sr, tr); si = fmaf(lr, si, ti);
    ar[t >> 2][t & 3] = sr; ai[t >> 2][t & 3] = si;
  }
  if (act) *(f32x2*)(smem + SP_EC + (seg * 40 + chl) * 8) = (f32x2){sr, si};
  asm volatile("s_waitcnt lgkmcnt(0)" ::: "memory");
  lds_inc(smem, M_SRD, lane);
  lds_wait_ge(misc + M_SRD, 3u);
  float qr = 0.f, qi = 0.f;
#pragma unroll
  for (int i = 0; i < 3; ++i) {
    const f32x2 e = *(const f32x2*)(smem + SP_EC + (i * 40 + chl) * 8);
    const float tr = fmaf(-cl[3], qi, e[0]), ti = fmaf(cl[3], qr, e[1]);
    const float nr = fmaf(cl[2], qr, tr), ni = fmaf(cl[2], qi, ti);
    qr = i < seg ? nr : qr; qi = i < seg ? ni : qi;
  }
  if (act && seg == 3) {
    const float er = fmaf(cl[2], qr, fmaf(-cl[3], qi, sr)), ei = fmaf(cl[2], qi, fmaf(cl[3], qr, si));
    st128(rW, OFF_E + ((j * 16 + rc) * 40 + chl) * 16, (u32x4){__float_as_uint(er), __float_as_uint(ei), tlo, thi});
  }
  lds_wait_ge(misc + M_GRDY, 2u);
  {
    float pr = cl[0], pi = cl[1];
    const float nci_ = -cl[1];
#pragma unroll
    for (int i = 0; i < 8; ++i) {
      const f32x4 gv = *(const f32x4*)(smem + SP_G + seg * 128 + i * 16);
#pragma unroll
      for (int r = 0; r < 4; ++r) {
        const float xr = fmaf(cl[0], ar[i][r], (nci_ * ai[i][r])), xi = fmaf(cl[0], ai[i][r], (cl[1] * ar[i][r]));
        ar[i][r] = (xr * gv[r]); ai[i][r] = (xi * gv[r]);
        const float tr = (nli * pi), ti = (li * pr);
        pr = fmaf(lr, pr, tr); pi = fmaf(lr, pi, ti);
        wr[i][r] = (pr * gv[r]); wi[i][r] = (pi * gv[r]);
      }
    }
  }
  float cr = 0.f, ci = 0.f;
  {
    u32x4 ev[15];
    for (int sp = 0; sp < SPIN_G; ++sp) {
      bool ok = true;
#pragma unroll
      for (int r = 0; r < 15; ++r) {
        ev[r] = (u32x4){0u, 0u, tlo, thi};
        if (r < rc) ev[r] = __builtin_amdgcn_raw_buffer_load_b128(rW, OFF_E + ((j * 16 + r) * 40 + chl) * 16, 0, SC1);
      }
#pragma unroll
      for (int r = 0; r < 15; ++r) ok = ok && ev[r][2] == tlo && ev[r][3] == thi;
      if (__all(ok)) break;
      __builtin_amdgcn_s_sleep(1);
      asm volatile("" ::: "memory");
    }
    const float er = l128[0], ei = l128[1], nei = -l128[1];
#pragma unroll
    for (int r = 0; r < 15; ++r)
      if (r < rc) {
        const float tr = fmaf(nei, ci, __uint_as_float(ev[r][0])), ti = fmaf(ei, cr, __uint_as_float(ev[r][1]));
        cr = fmaf(er, cr, tr); ci = fmaf(er, ci, ti);
      }
    const float tr = fmaf(-pw[1], ci, qr), ti = fmaf(pw[1], cr, qi);
    cr = fmaf(pw[0], cr, tr); ci = fmaf(pw[0], ci, ti);
  }
  char* wp = smem + L_HI + seg * 32 * HIS + chl * 2;
  {
    const float nci = -ci, ncr = -cr;
#pragma unroll
    for (int t = 0; t < 32; ++t) {
      const float orr = fmaf(wr[t >> 2][t & 3], cr, fmaf(wi[t >> 2][t & 3], nci, ar[t >> 2][t & 3]));
      const float oii = fmaf(wr[t >> 2][t & 3], nci, fmaf(wi[t >> 2][t & 3], ncr, -ai[t >> 2][t & 3]));
      const unsigned pk = pack2(orr, oii);
      *(u16*)(wp + t * HIS) = (u16)pk;
      *(u16*)(wp + t * HIS + 80) = (u16)(pk >> 16);
    }
  }
  asm volatile("s_waitcnt lgkmcnt(0)" ::: "memory");
  lds_inc(smem, M_SWR, lane);
}
DI void publish_h(RSRC rW, char* smem, volatile LAS unsigned* misc, int hv, int lane, int rc, int cg, int jl, unsigned tlo, unsigned thi) {
  lds_wait_ge(misc + M_SWR, 3u);
  __builtin_amdgcn_s_setprio(3);
#pragma unroll
  for (int pe = 0; pe < 10; ++pe) {
    const int part = pe / 5, e = pe % 5;
    const int t = 64 * hv + lane, pi = 5 * jl + e, step = 12 + 2 * (pi >> 2) + part;
    const u32x4 v = *(const u32x4*)(smem + L_HI + t * HIS + part * 80 + e * 16);
    st128(rW, OFF_AF + (((rc * 4 + cg) * 22 + step) * 8 + (t >> 4)) * 1024 + ((pi & 3) * 16 + (t & 15)) * 16, v);
  }
  WAITV(0);
  if (lds_inc(smem, M_PUB, lane) == 1u) {
    if (lane == 0) set_flag(rW, OFF_FA + (rc * 4 + cg) * 128 + 32 + jl * 8, tlo, thi);
    poll_flags(rW, OFF_FA + (rc * 4 + cg) * 128 + 32, 4, tlo, thi, lane);
    if (lane == 0) misc[M_HOK] = 1u;
  }
  __builtin_amdgcn_s_setprio(0);
}

__global__ __launch_bounds__(512, 2) void fused(Args a) {
  __shared__ __attribute__((aligned(16))) char smem[LDS_BYTES];
  const int tid = threadIdx.x, lane = tid & 63;
  const int wave = __builtin_amdgcn_readfirstlane(tid >> 6);
  const int b = blockIdx.x, grp = b & 7, idx = b >> 3;
  const int cg = grp & 3, jl = idx & 3;
  const int rc = 8 * (grp >> 2) + (idx >> 2), j = 4 * cg + jl;
  const int r16 = lane & 15, g = lane >> 4;
  char* ws = a.ws;
  const RSRC rW = __builtin_amdgcn_make_buffer_rsrc((void*)ws, 0, WS_BYTES, 0x00020000);
  volatile LAS unsigned* misc = (volatile LAS unsigned*)(smem + SP_MISC);
  const unsigned sbase = (unsigned)(size_t)(LAS char*)smem;
  STAMP_DECL
  STAMP(0);

  if (wave >= 4) {
    const int lw = wave - 4;
    {
      unsigned voff[10]; const char* sb_[10]; int inc[10]; unsigned dsto[10];
#pragma unroll
      for (int i = 0; i < 10; ++i) {
        int p = lw + 4 * i; p = p >= 38 ? p - 38 : p;
        dsto[i] = sbase + p * 1024;
        const int rw = lane >> 3;
        const unsigned ox = (8 * p + rw) * (D_ * 2) + 16 * ((lane & 7) ^ (((8 * p + rw) >> 1) & 7));
        const int pb = p < 21 ? p - 16 : p - 21;
        const unsigned ob = (8 * pb + rw) * (D_ * 2) + 16 * ((lane & 7) ^ ((((rw >> 1) & 3) << 1) | (p >= 21 ? 1 : 0)));
        const int ci = 64 * (p >= 26 ? p - 26 : 0) + lane, k = ci / 12, cp = ci % 12;
        int cc = cp - 2 * ((k >> 3) & 1); cc = cc < 0 ? cc + 12 : cc;
        const unsigned ow = k * (F_ * 2) + 16 * cc;
        voff[i] = p < 16 ? ox : p < 26 ? ob : ow;
        inc[i] = p < 26 ? 128 : 64 * F_ * 2;
        sb_[i] = (const char*)(p < 16 ? a.x + (size_t)128 * rc * D_ : p < 21 ? a.Bre + (size_t)40 * j * D_ : p < 26 ? a.Bim + (size_t)40 * j * D_ : a.w1 + 96 * j);
      }
      auto stage = [&](unsigned boff) {
#pragma unroll
        for (int i = 0; i < 10; ++i) { gldss(voff[i], sb_[i], dsto[i] + boff); sb_[i] += inc[i]; }
      };
      stage(0); stage(ST_SZ);
      STAMP(1);
      LBAR(10);
      STAMP(2);
      stage(2 * ST_SZ);
      LBAR(10);
      stage(3 * ST_SZ); stage(0);
#pragma unroll 1
      for (int s = 2; s <= 6; ++s) {
        LBAR(20);
        stage(((s + 3) & 3) * ST_SZ);
      }
      LBAR(20); LBAR(10); LBAR(0);
    }
    STAMP(3);
    const unsigned tlo = misc[M_TAG], thi = misc[M_TAG + 1];

    unsigned woff[10], coff[10];
#pragma unroll
    for (int i = 0; i < 10; ++i) {
      const int ci = 64 * i + lane;
      const int k = ci / 20, cp = ci % 20;
      int cc = cp - 2 * ((k >> 2) & 1); cc = cc < 0 ? cc + 20 : cc;
      woff[i] = k * (D_ * 2) + 16 * cc;
      const int row = ci >> 2, f4 = (row >> 2) & 3, fx = (f4 == 0 ? 0 : f4 == 1 ? 2 : f4 == 2 ? 3 : 1);
      coff[i] = row * (P_ * 2) + 16 * ((ci & 3) ^ fx);
    }
    const char* wbase2 = (const char*)(a.w2 + (size_t)384 * cg * D_ + 160 * jl);
    const char* abase = ws + OFF_AF + (size_t)(rc * 4 + cg) * 22 * 8192;
    const unsigned aoff = lane * 16;
    auto issueB = [&](int n, unsigned sl) {
      if (n < 12) {
        const char* p = wbase2 + (size_t)n * (32 * D_ * 2);
#pragma unroll
        for (int i = 0; i < 10; ++i) gldss(woff[i], p, sl + 8192 + i * 1024);
      } else {
        const char* p = (const char*)(((n & 1) ? a.Cim : a.Cre) + (size_t)160 * jl * P_ + 160 * cg) + ((n - 12) >> 1) * 64;
#pragma unroll
        for (int i = 0; i < 10; ++i) gldss(coff[i], p, sl + 8192 + i * 1024);
      }
    };
    auto issueA = [&](int n, unsigned sl) {
#pragma unroll
      for (int i = 0; i < 8; ++i) gldssc(aoff, abase + (size_t)n * 8192 + i * 1024, sl + i * 1024);
    };
    int p1 = -1, p2 = -1;
    if (lw >= 2) {
      if (lw == 2) scan_task(rW, smem, misc, 2, lane, rc, j, tlo, thi);
      else {
        lds_wait_ge(misc + M_GRDY, 2u);
#pragma unroll
        for (int hh = 0; hh < 2; ++hh) {
          lds_wait_ge(misc + M_OFF + hh, 1u);
          const float og = 1.f - 0.5f * *(const float*)(smem + SP_G + (64 * hh + 48 + r16) * 4);
#pragma unroll
          for (int fp = 0; fp < 3; ++fp) {
            const char* sp = smem + hh * SLOT + fp * 2048 + lane * 16;
            st128(rW, OFF_AF + (((rc * 4 + cg) * 22 + 3 * jl + fp) * 8 + 4 * hh + 3) * 1024 + lane * 16,
                  mish_unit(*(const f32x4*)sp, *(const f32x4*)(sp + 1024), og));
          }
        }
        WAITV(0);
        if (lds_inc(smem, M_ACT, lane) == 2u && lane == 0) set_flag(rW, OFF_FA + (rc * 4 + cg) * 128 + jl * 8, tlo, thi);
      }
      STAMP(4);
      publish_h(rW, smem, misc, lw - 2, lane, rc, cg, jl, tlo, thi);
      lds_wait_ge(misc + M_FULL, 1u);
      STAMP(6);
    } else {
      const int h = lw;
      lds_wait_ge(misc + M_P1D, 4u);
#pragma unroll 1
      for (int k = 0; k < 3; ++k) issueB(2 * k + h, sbase + (2 * k + h) * SLOT);
      STAMP(4);
      poll_flags(rW, OFF_FA + (rc * 4 + cg) * 128, 4, tlo, thi, lane);
      STAMP(5);
#pragma unroll 1
      for (int k = 0; k < 3; ++k) issueA(2 * k + h, sbase + (2 * k + h) * SLOT);
      WAITV(16); if (lane == 0) misc[M_FULL + h] = 1u;
      WAITV(8);  if (lane == 0) misc[M_FULL + 2 + h] = 1u;
      p2 = 4 + h;
      lds_wait_ge(misc + M_SRD, 3u);
    }
#pragma unroll 1
    for (;;) {
      const int n = (int)lds_inc(smem, M_NEXT, lane);
      if (n >= 22) break;
      const unsigned need = n - NSLOT + 1;
      bool ok = n < NSLOT || (misc[M_CONS] >= need && misc[M_CONS + 1] >= need && misc[M_CONS + 2] >= need && misc[M_CONS + 3] >= need);
      ok = ok && (n < 12 || misc[M_HOK] != 0u);
      if (!ok) {
        WAITV(0);
        if (lane == 0) { if (p1 >= 0) misc[M_FULL + p1] = 1u; if (p2 >= 0) misc[M_FULL + p2] = 1u; }
        p1 = p2 = -1;
        if (n >= NSLOT) {
          lds_wait_ge(misc + M_CONS, need); lds_wait_ge(misc + M_CONS + 1, need); lds_wait_ge(misc + M_CONS + 2, need); lds_wait_ge(misc + M_CONS + 3, need);
        }
        if (n >= 12) lds_wait_ge(misc + M_HOK, 1u);
      }
      asm volatile("" ::: "memory");
      const unsigned sl = sbase + (n % NSLOT) * SLOT;
      issueB(n, sl); issueA(n, sl);
      WAITV(36);
      if (lane == 0 && p1 >= 0) misc[M_FULL + p1] = 1u;
      p1 = p2; p2 = n;
    }
    WAITV(18); if (lane == 0 && p1 >= 0) misc[M_FULL + p1] = 1u;
    WAITV(0);  if (lane == 0 && p2 >= 0) misc[M_FULL + p2] = 1u;
    STAMP(8);
    const int tl = 16 * (lw & 1) + r16, n0 = 160 * jl + 80 * (lw >> 1) + 4 * g;
    const size_t orow = (size_t)(128 * rc + 32 * cg + tl) * D_ + n0;
    f32x4 sk[5];
    {
      const float gt = 0.5f * *(const float*)(smem + SP_G + (32 * cg + tl) * 4);
#pragma unroll
      for (int i = 0; i < 5; ++i) {
        u32x2 xv = *(const u32x2*)(a.x + orow + 16 * i);
        u32x2 dv = *(const u32x2*)(a.Dsk + n0 + 16 * i);
        sk[i][0] = gt * bflo(xv[0]) * bflo(dv[0]); sk[i][1] = gt * bfhi(xv[0]) * bfhi(dv[0]);
        sk[i][2] = gt * bflo(xv[1]) * bflo(dv[1]); sk[i][3] = gt * bfhi(xv[1]) * bfhi(dv[1]);
      }
    }
    STAMP(22);
    for (int sp = 0; misc[M_RED] < 4u && sp < SPIN_LDS; ++sp) __builtin_amdgcn_s_sleep(4);
    asm volatile("" ::: "memory");
    STAMP(23);
    poll_flags(rW, OFF_FR + (rc * 4 + jl) * 128, 4, tlo, thi, lane);
    STAMP(24);
    if (rc == 15 && j == 0 && tid == 256) set_flag(rW, OFF_EPOCH, tlo, thi);
    u32x2 pv[3][5];
#pragma unroll
    for (int s = 0; s < 3; ++s) {
      const int sc = (cg + 1 + s) & 3;
#pragma unroll
      for (int i = 0; i < 5; ++i)
        pv[s][i] = __builtin_amdgcn_raw_buffer_load_b64(rW, OFF_PB + (((rc * 4 + jl) * 4 + cg) * 4 + sc) * 10240 + (lw >> 1) * 5120 + i * 1024 + lane * 16 + (lw & 1) * 8, 0, SC1);
    }
#pragma unroll
    for (int i = 0; i < 5; ++i) {
      f32x4 v = *(const f32x4*)(smem + (5 * lw + i) * 1024 + lane * 16) + sk[i];
#pragma unroll
      for (int s = 0; s < 3; ++s) v += (f32x4){bflo(pv[s][i][0]), bfhi(pv[s][i][0]), bflo(pv[s][i][1]), bfhi(pv[s][i][1])};
      *(f32x4*)(a.out + orow + 16 * i) = v;
    }
  } else {
    if (wave == 1) {
      const u64 ep = __hip_atomic_load((GAS u64*)(ws + OFF_EPOCH), __ATOMIC_RELAXED, __HIP_MEMORY_SCOPE_AGENT);
      const unsigned v = *(const unsigned*)(a.b1 + 96 * j + 2 * (lane < 48 ? lane : 0));
      const u32x4 g0 = *(const u32x4*)(a.gw + 8 * lane), g1 = *(const u32x4*)(a.gw + 8 * (64 + r16));
      if (lane < 48) *(f32x2*)(smem + SP_B1 + lane * 8) = (f32x2){bflo(v), bfhi(v)};
      *(u32x4*)(smem + SP_GW + lane * 16) = g0;
      if (lane < 16) *(u32x4*)(smem + SP_GW + 1024 + lane * 16) = g1;
      if (lane < 24 || lane >= 32) misc[lane] = lane == M_NEXT ? 6u : 0u;
      if (lane == 0) { const u64 tg = ep + 1; misc[M_TAG] = (unsigned)tg; misc[M_TAG + 1] = (unsigned)(tg >> 32); }
    } else if (wave == 2) {
      const int ch = 40 * j + (lane < 40 ? lane : 0);
      const float lre = bflo(a.Lre[ch]), lim = bflo(a.Lim[ch]), dt = expf(bflo(a.ldt[ch]));
      float e = expf(dt * lre), sn, cs;
      sincosf(dt * lim, &sn, &cs);
      f32x2 l1 = {e * cs, e * sn};
      const float nr = l1[0] - 1.f, ni = l1[1], den = lre * lre + lim * lim;
      f32x2 cf = {(nr * lre + ni * lim) / den, (ni * lre - nr * lim) / den};
      f32x2 l2 = cm2(l1, l1), l4 = cm2(l2, l2), l8 = cm2(l4, l4), l12 = cm2(l8, l4), l16 = cm2(l8, l8);
      f32x2 l32 = cm2(l16, l16), l64 = cm2(l32, l32), l128 = cm2(l64, l64), l96 = cm2(l64, l32);
      if (lane < 40) {
        float* pp = (float*)(smem + SP_PAR + lane * PARS);
        *(f32x4*)(pp) = (f32x4){l1[0], l1[1], l16[0], l16[1]};
        *(f32x4*)(pp + 4) = (f32x4){cf[0], cf[1], l32[0], l32[1]};
        *(f32x4*)(pp + 8) = (f32x4){l12[0], l12[1], l8[0], l8[1]};
        *(f32x4*)(pp + 12) = (f32x4){l4[0], l4[1], 1.f, 0.f};
        *(f32x4*)(pp + 16) = (f32x4){l64[0], l64[1], l96[0], l96[1]};
        *(f32x2*)(pp + 20) = l128;
      }
    }
    STAMP(1);
    const int th = wave & 1;
    const unsigned xo0 = ST_X + (64 * th + r16) * 128 + ((g ^ (r16 >> 1)) * 16);
    const unsigned xo1 = xo0 ^ 64;
    const int q4 = r16 >> 2, p4 = r16 & 3;

#define P1_PIPE(LOADF, STEP, MMA, INIT)                                             \
    CBAR;                                                                           \
    __builtin_amdgcn_s_setprio(3);                                                  \
    STAMP(2);                                                                       \
    INIT;                                                                           \
    LOADF(F0, 0u, 0);                                                               \
    STEP(F0, F1, 0u, 1);                                                            \
    _Pragma("unroll 1") for (int s = 1; s < 10; ++s) {                              \
      CBAR;                                                                         \
      STEP(F1, F0, (s & 3) * ST_SZ, 0);                                             \
      STEP(F0, F1, (s & 3) * ST_SZ, 1);                                             \
    }                                                                               \
    asm volatile("s_waitcnt lgkmcnt(0)" ::: "memory");                              \
    lds_inc(smem, M_P1D, lane);                                                     \
    MMA(F1); SCHED; __builtin_amdgcn_s_setprio(0);

    unsigned tlo, thi;
    if (wave < 2) {
      f32x4 acc[6][4];
      const unsigned wbase = ST_W1 + (8 * g + q4) * 192 + (p4 >> 1) * 16 + (p4 & 1) * 8;
      const unsigned wa = wbase + (g & 1) * 32;
      const unsigned wb = wbase + ((g & 1) ? 0 : 160);
      struct Frag { bf16x8 x[4], w[6]; };
      Frag F0, F1;
      auto rdw = [&](const char* sb, int ft, int kk) {
        unsigned ad = (ft < 5 ? wa + 32 * ft : wb) + kk * 32 * 192;
        s16x4 lo = __builtin_amdgcn_ds_read_tr16_b64_v4i16((LAS s16x4*)(sb + ad));
        s16x4 hi = __builtin_amdgcn_ds_read_tr16_b64_v4i16((LAS s16x4*)(sb + ad + 4 * 192));
        return __builtin_bit_cast(bf16x8, __builtin_shufflevector(lo, hi, 0, 1, 2, 3, 4, 5, 6, 7));
      };
      auto loadf = [&](Frag& F, unsigned boff, int kk) {
        const char* sb = smem + boff;
#pragma unroll
        for (int tt = 0; tt < 4; ++tt) F.x[tt] = *(const bf16x8*)(sb + (kk ? xo1 : xo0) + tt * 2048);
#pragma unroll
        for (int ft = 0; ft < 6; ++ft) F.w[ft] = rdw(sb, ft, kk);
      };
      auto mma = [&](Frag& F) {
#pragma unroll
        for (int m = 0; m < 24; ++m)
          acc[m >> 2][m & 3] = __builtin_amdgcn_mfma_f32_16x16x32_bf16(F.w[m >> 2], F.x[m & 3], acc[m >> 2][m & 3], 0, 0, 0);
      };
      auto step = [&](Frag& C, Frag& N, unsigned boff, const int kk) {
        const char* sb = smem + boff;
#pragma unroll
        for (int m = 0; m < 24; ++m) {
          SCHED;
          acc[m >> 2][m & 3] = __builtin_amdgcn_mfma_f32_16x16x32_bf16(C.w[m >> 2], C.x[m & 3], acc[m >> 2][m & 3], 0, 0, 0);
          SCHED;
          if (m < 4) N.x[m] = *(const bf16x8*)(sb + (kk ? xo1 : xo0) + m * 2048);
          if (m >= 4 && m < 10) N.w[m - 4] = rdw(sb, m - 4, kk);
        }
        SCHED;
      };
      auto init = [&]() {
#pragma unroll
        for (int ft = 0; ft < 6; ++ft) {
          f32x4 bv = *(const f32x4*)(smem + SP_B1 + (16 * ft + 4 * g) * 4);
#pragma unroll
          for (int tt = 0; tt < 4; ++tt) acc[ft][tt] = bv;
        }
      };
      P1_PIPE(loadf, step, mma, init());
      STAMP(3);
      tlo = misc[M_TAG]; thi = misc[M_TAG + 1];
      lds_wait_ge(misc + M_P1D, 4u);
#pragma unroll
      for (int fp = 0; fp < 3; ++fp) {
        char* sp = smem + th * SLOT + fp * 2048 + lane * 16;
        *(f32x4*)sp = acc[2 * fp][3]; *(f32x4*)(sp + 1024) = acc[2 * fp + 1][3];
      }
      asm volatile("s_waitcnt lgkmcnt(0)" ::: "memory");
      if (lane == 0) misc[M_OFF + th] = 1u;
      lds_wait_ge(misc + M_GRDY, 2u);
#pragma unroll
      for (int tt = 0; tt < 3; ++tt) {
        const float og = 1.f - 0.5f * *(const float*)(smem + SP_G + (64 * th + 16 * tt + r16) * 4);
#pragma unroll
        for (int fp = 0; fp < 3; ++fp)
          st128(rW, OFF_AF + (((rc * 4 + cg) * 22 + 3 * jl + fp) * 8 + 4 * th + tt) * 1024 + lane * 16,
                mish_unit(acc[2 * fp][tt], acc[2 * fp + 1][tt], og));
      }
      STAMP(4);
      WAITV(0);
      if (lds_inc(smem, M_ACT, lane) == 2u && lane == 0) set_flag(rW, OFF_FA + (rc * 4 + cg) * 128 + jl * 8, tlo, thi);
      STAMP(5);
    } else {
      f32x4 acc[4][5], gac[4];
      const unsigned bo0 = ST_BM + (40 * (r16 & 1) + (r16 >> 1)) * 128 + ((g ^ ((((r16 >> 2) & 3) << 1) | (r16 & 1))) * 16);
      const unsigned bo1 = bo0 ^ 64;
      struct Frag { bf16x8 x[4], w[5], gf; };
      Frag F0, F1;
      int kc = 0;
      auto loadf = [&](Frag& F, unsigned boff, int kk) {
        const char* sb = smem + boff;
#pragma unroll
        for (int tt = 0; tt < 4; ++tt) F.x[tt] = *(const bf16x8*)(sb + (kk ? xo1 : xo0) + tt * 2048);
#pragma unroll
        for (int nt = 0; nt < 5; ++nt) F.w[nt] = *(const bf16x8*)(sb + (kk ? bo1 : bo0) + nt * 1024);
        F.gf = *(const bf16x8*)(smem + SP_GW + kc * 64 + g * 16);
        ++kc;
      };
      auto mma = [&](Frag& F) {
#pragma unroll
        for (int tt = 0; tt < 4; ++tt) gac[tt] = __builtin_amdgcn_mfma_f32_16x16x32_bf16(F.x[tt], F.gf, gac[tt], 0, 0, 0);
#pragma unroll
        for (int m = 0; m < 20; ++m)
          acc[m & 3][m >> 2] = __builtin_amdgcn_mfma_f32_16x16x32_bf16(F.x[m & 3], F.w[m >> 2], acc[m & 3][m >> 2], 0, 0, 0);
      };
      auto step = [&](Frag& C, Frag& N, unsigned boff, const int kk) {
        const char* sb = smem + boff;
#pragma unroll
        for (int m = 0; m < 24; ++m) {
          SCHED;
          if (m < 20) acc[m & 3][m >> 2] = __builtin_amdgcn_mfma_f32_16x16x32_bf16(C.x[m & 3], C.w[m >> 2], acc[m & 3][m >> 2], 0, 0, 0);
          else gac[m & 3] = __builtin_amdgcn_mfma_f32_16x16x32_bf16(C.x[m & 3], C.gf, gac[m & 3], 0, 0, 0);
          SCHED;
          if (m < 4) N.x[m] = *(const bf16x8*)(sb + (kk ? xo1 : xo0) + m * 2048);
          if (m >= 4 && m < 9) N.w[m - 4] = *(const bf16x8*)(sb + (kk ? bo1 : bo0) + (m - 4) * 1024);
          if (m == 9) N.gf = *(const bf16x8*)(smem + SP_GW + kc * 64 + g * 16);
        }
        ++kc;
        SCHED;
      };
      auto init = [&]() {
#pragma unroll
        for (int tt = 0; tt < 4; ++tt) {
          gac[tt] = (f32x4){0.f, 0.f, 0.f, 0.f};
#pragma unroll
          for (int nt = 0; nt < 5; ++nt) acc[tt][nt] = (f32x4){0.f, 0.f, 0.f, 0.f};
        }
      };
      P1_PIPE(loadf, step, mma, init());
      STAMP(3);
      tlo = misc[M_TAG]; thi = misc[M_TAG + 1];
      {
        char* bw = smem + L_BU + r16 * BUS + (64 * th + 4 * g) * 4;
#pragma unroll
        for (int tt = 0; tt < 4; ++tt)
#pragma unroll
          for (int nt = 0; nt < 5; ++nt) *(f32x4*)(bw + nt * 16 * BUS + tt * 64) = acc[tt][nt];
        f32x4 lg = r16 == 0 ? gac[0] : r16 == 1 ? gac[1] : r16 == 2 ? gac[2] : gac[3];
        f32x4 gg;
#pragma unroll
        for (int r = 0; r < 4; ++r) gg[r] = 2.f * __builtin_amdgcn_rcpf(1.f + __expf(-lg[r]));
        if (r16 < 4) *(f32x4*)(smem + SP_G + (64 * th + 16 * r16 + 4 * g) * 4) = gg;
        asm volatile("s_waitcnt lgkmcnt(0)" ::: "memory");
        lds_inc(smem, M_GRDY, lane);
        lds_inc(smem, M_BUW, lane);
      }
      STAMP(4);
      scan_task(rW, smem, misc, th, lane, rc, j, tlo, thi);
      STAMP(5);
    }

    const int wn = wave & 1, wt = wave >> 1;
    f32x4 acc[5][4];
#pragma unroll
    for (int nt = 0; nt < 5; ++nt)
#pragma unroll
      for (int tt = 0; tt < 4; ++tt) acc[nt][tt] = (f32x4){0.f, 0.f, 0.f, 0.f};
    struct Frag2 { bf16x8 x[4], w[5]; };
    Frag2 G0, G1;
    const unsigned ao = 4 * wt * 1024 + lane * 16;
    const unsigned twb = 8192 + (4 * g + q4) * 320 + (p4 >> 1) * 16 + (p4 & 1) * 8;
    const unsigned twa = twb + (5 * wn + (g & 1)) * 32;
    const unsigned twl = (wn == 1 && (g & 1)) ? twb : twa + 128;
    const int f4 = (r16 >> 2) & 3, fx = (f4 == 0 ? 0 : f4 == 1 ? 2 : f4 == 2 ? 3 : 1);
    const unsigned cwo = 8192 + (80 * wn + r16) * 64 + ((g ^ fx) * 16);
    auto rd2 = [&](Frag2& N, const char* sb, int e, const bool ssm) {
      if (e < 4) N.x[e] = *(const bf16x8*)(sb + ao + e * 1024);
      else if (ssm) N.w[e - 4] = *(const bf16x8*)(sb + cwo + (e - 4) * 1024);
      else {
        const unsigned ad = e - 4 < 4 ? twa + 32 * (e - 4) : twl;
        s16x4 lo = __builtin_amdgcn_ds_read_tr16_b64_v4i16((LAS s16x4*)(sb + ad));
        s16x4 hi = __builtin_amdgcn_ds_read_tr16_b64_v4i16((LAS s16x4*)(sb + ad + 16 * 320));
        N.w[e - 4] = __builtin_bit_cast(bf16x8, __builtin_shufflevector(lo, hi, 0, 1, 2, 3, 4, 5, 6, 7));
      }
    };
    auto wait_full = [&](int st) { lds_wait_ge(misc + M_FULL + st, 1u); };
    int slot = 0;
    unsigned fpre = 0;
    auto step2 = [&](Frag2& C, Frag2& N, int n, const bool next_ssm, const bool last) {
      if (!last && fpre == 0u) wait_full(n + 1);
      asm volatile("s_waitcnt lgkmcnt(0)" ::: "memory");
      const char* sb = smem + slot * SLOT;
      slot = slot == NSLOT - 1 ? 0 : slot + 1;
#pragma unroll
      for (int m = 0; m < 20; ++m) {
        SCHED;
        acc[m >> 2][m & 3] = __builtin_amdgcn_mfma_f32_16x16x32_bf16(C.w[m >> 2], C.x[m & 3], acc[m >> 2][m & 3], 0, 0, 0);
        SCHED;
        if (!last && m < 9) rd2(N, sb, m, next_ssm);
        if (m == 9) { if (lane == 0) misc[M_CONS + wave] = n + 1; }
        if (m == 12) fpre = misc[M_FULL + n + 2];
      }
      SCHED;
    };
    wait_full(0);
    __builtin_amdgcn_s_setprio(1);
    STAMP(6);
#pragma unroll
    for (int e = 0; e < 9; ++e) rd2(G0, smem, e, false);
    slot = 1;
#pragma unroll 1
    for (int n = 0; n < 10; n += 2) { step2(G0, G1, n, false, false); step2(G1, G0, n + 1, false, false); }
    STAMP(7);
    step2(G0, G1, 10, false, false); step2(G1, G0, 11, true, false);
    STAMP(8);
#pragma unroll 1
    for (int n = 12; n < 20; n += 2) { step2(G0, G1, n, true, false); step2(G1, G0, n + 1, true, false); }
    step2(G0, G1, 20, true, false); step2(G1, G0, 21, true, true);
    __builtin_amdgcn_s_setprio(0);
    STAMP(9);
#pragma unroll
    for (int t2 = 0; t2 < 2; ++t2) {
      const int owner = 2 * wt + t2;
      if (owner != cg) {
        const unsigned pb = OFF_PB + (((rc * 4 + jl) * 4 + owner) * 4 + cg) * 10240 + wn * 5120 + lane * 16;
#pragma unroll
        for (int nt = 0; nt < 5; ++nt) {
          const f32x4 v0 = acc[nt][2 * t2], v1 = acc[nt][2 * t2 + 1];
          u32x4 o; o[0] = pack2(v0[0], v0[1]); o[1] = pack2(v0[2], v0[3]); o[2] = pack2(v1[0], v1[1]); o[3] = pack2(v1[2], v1[3]);
          st128(rW, pb + nt * 1024, o);
        }
      }
    }
    lds_wait_ge(misc + M_CONS, 22u); lds_wait_ge(misc + M_CONS + 1, 22u); lds_wait_ge(misc + M_CONS + 2, 22u); lds_wait_ge(misc + M_CONS + 3, 22u);
#pragma unroll
    for (int tt = 0; tt < 4; ++tt) {
      if (2 * wt + (tt >> 1) == cg) {
#pragma unroll
        for (int nt = 0; nt < 5; ++nt) *(f32x4*)(smem + (5 * (2 * wn + (tt & 1)) + nt) * 1024 + lane * 16) = acc[nt][tt];
      }
    }
    STAMP(10);
    asm volatile("s_waitcnt vmcnt(0) lgkmcnt(0)" ::: "memory");
    if (lds_inc(smem, M_RED + 1, lane) == 3u) {
      if (lane == 0) { set_flag(rW, OFF_FR + (rc * 4 + jl) * 128 + cg * 8, tlo, thi); misc[M_RED] = 4u; }
    }
    STAMP(11);
  }
  STAMP(25);
  STAMP(26);
  STAMP_OUT(0);
}

extern "C" void kernel_launch(void* const* d_in, const int* in_sizes, int n_in,
                              void* d_out, int out_size, void* d_ws, size_t ws_size,
                              hipStream_t stream) {
  Args a;
  a.x   = (const u16*)d_in[0];
  a.Lre = (const u16*)d_in[1];
  a.Lim = (const u16*)d_in[2];
  a.Bre = (const u16*)d_in[3];
  a.Bim = (const u16*)d_in[4];
  a.Cre = (const u16*)d_in[5];
  a.Cim = (const u16*)d_in[6];
  a.ldt = (const u16*)d_in[7];
  a.Dsk = (const u16*)d_in[8];
  a.w1  = (const u16*)d_in[10];
  a.b1  = (const u16*)d_in[11];
  a.w2  = (const u16*)d_in[12];
  a.gw  = (const u16*)d_in[13];
  a.ws  = (char*)d_ws;
  a.out = (float*)d_out;
  fused<<<256, 512, 0, stream>>>(a);
}
```

```cpp
#include <hip/hip_runtime.h>
#include <hip/hip_bf16.h>
#include <stdint.h>
#pragma clang diagnostic ignored "-Winline-asm"

typedef __attribute__((ext_vector_type(8))) __bf16 bf16x8;
typedef __attribute__((ext_vector_type(2))) __bf16 bf16x2;
typedef __attribute__((ext_vector_type(4))) float f32x4;
typedef __attribute__((ext_vector_type(2))) float f32x2;
typedef __attribute__((ext_vector_type(4))) unsigned u32x4;
typedef __attribute__((ext_vector_type(2))) unsigned u32x2;
typedef __attribute__((ext_vector_type(4))) short s16x4;
typedef unsigned short u16;
#define LAS __attribute__((address_space(3)))
#define GAS __attribute__((address_space(1)))
#define DI __device__ __forceinline__

constexpr int T_ = 2048, D_ = 640, P_ = 640, F_ = 1536;

constexpr unsigned OFF_AF = 0;
constexpr unsigned OFF_PB = OFF_AF + 64u * 22 * 8192;
constexpr unsigned OFF_E  = OFF_PB + 64u * 16 * 10240;
constexpr unsigned OFF_FA = OFF_E + 256u * 640;
constexpr unsigned OFF_FE = OFF_FA + 64 * 128;
constexpr unsigned OFF_FR = OFF_FE + 16 * 128;
constexpr unsigned OFF_EPOCH = OFF_FR + 64 * 128;
constexpr unsigned WS_BYTES = OFF_EPOCH + 256;

#define STAMP(i)
#define STAMP_DECL
#define STAMP_OUT(kid)

DI float bflo(unsigned v) { return __uint_as_float(v << 16); }
DI float bfhi(unsigned v) { return __uint_as_float(v & 0xffff0000u); }
DI unsigned pack2(float a, float b) {
  bf16x2 r; r[0] = (__bf16)a; r[1] = (__bf16)b; return __builtin_bit_cast(unsigned, r);
}
DI float dswap(float v) {
  return __int_as_float(__builtin_amdgcn_mov_dpp(__float_as_int(v), 0xB1, 0xf, 0xf, true));
}
DI float cmul(float lr, float ls, float v) { return fmaf(lr, v, ls * dswap(v)); }
DI float cmuladd(float lr, float ls, float v, float a) { return fmaf(lr, v, fmaf(ls, dswap(v), a)); }
DI f32x2 cm2(f32x2 a, f32x2 b) { return (f32x2){a[0] * b[0] - a[1] * b[1], a[0] * b[1] + a[1] * b[0]}; }
DI float sfma(float a, float b, float c) { float d; asm("v_fma_f32 %0, %1, %2, %3" : "=v"(d) : "v"(a), "v"(b), "v"(c)); return d; }
DI float smul(float a, float b) { float d; asm("v_mul_f32 %0, %1, %2" : "=v"(d) : "v"(a), "v"(b)); return d; }

DI f32x2 mish2(f32x2 h) {
  f32x2 z = h * 1.44269504088896341f;
  f32x2 w; w[0] = __builtin_amdgcn_exp2f(fminf(z[0], 43.f)); w[1] = __builtin_amdgcn_exp2f(fminf(z[1], 43.f));
  f32x2 n = w * (w + 2.f);
  f32x2 d = n + 2.f;
  f32x2 r; r[0] = __builtin_amdgcn_rcpf(d[0]); r[1] = __builtin_amdgcn_rcpf(d[1]);
  return (h * n) * r;
}

DI void glds16(const void* gsrc, unsigned lds_dst) {
  unsigned keep;
  asm volatile("s_mov_b32 %0, m0\n\ts_mov_b32 m0, %2\n\ts_nop 0\n\tglobal_load_lds_dwordx4 %1, off\n\ts_mov_b32 m0, %0"
               : "=&s"(keep) : "v"(gsrc), "s"(lds_dst) : "memory");
}
DI void touch(const void* gsrc, unsigned lds_dst) {
  unsigned keep;
  asm volatile("s_mov_b32 %0, m0\n\ts_mov_b32 m0, %2\n\ts_nop 0\n\tglobal_load_lds_dword %1, off\n\ts_mov_b32 m0, %0"
               : "=&s"(keep) : "v"(gsrc), "s"(lds_dst) : "memory");
}
DI float sum16(float v) {
  auto r = __builtin_amdgcn_permlane16_swap(__float_as_uint(v), __float_as_uint(v), false, false);
  return __uint_as_float(r[0]) + __uint_as_float(r[1]);
}
DI float sum32(float v) {
  auto r = __builtin_amdgcn_permlane32_swap(__float_as_uint(v), __float_as_uint(v), false, false);
  return __uint_as_float(r[0]) + __uint_as_float(r[1]);
}

typedef unsigned long long u64;
#define SC1 16
#define SCHED __builtin_amdgcn_sched_barrier(0)
#define RSRC __amdgpu_buffer_rsrc_t
DI void st128(RSRC r, unsigned off, u32x4 v) { __builtin_amdgcn_raw_buffer_store_b128(v, r, off, 0, SC1); }
DI void st128f(RSRC r, unsigned off, f32x4 v) { __builtin_amdgcn_raw_buffer_store_b128(__builtin_bit_cast(u32x4, v), r, off, 0, SC1); }
DI void st64(RSRC r, unsigned off, u32x2 v) { __builtin_amdgcn_raw_buffer_store_b64(v, r, off, 0, SC1); }
DI f32x4 ld128f(RSRC r, unsigned off) { return __builtin_bit_cast(f32x4, __builtin_amdgcn_raw_buffer_load_b128(r, off, 0, SC1)); }
DI f32x2 ld64f(RSRC r, unsigned off) { return __builtin_bit_cast(f32x2, __builtin_amdgcn_raw_buffer_load_b64(r, off, 0, SC1)); }
DI void glds16c(const void* gsrc, unsigned lds_dst) {
  unsigned keep;
  asm volatile("s_mov_b32 %0, m0\n\ts_mov_b32 m0, %2\n\ts_nop 0\n\tglobal_load_lds_dwordx4 %1, off sc1\n\ts_mov_b32 m0, %0"
               : "=&s"(keep) : "v"(gsrc), "s"(lds_dst) : "memory");
}

DI void gldss(unsigned voff, const void* sbase_, unsigned lds_dst) {
  asm volatile("s_mov_b32 m0, %2\n\ts_nop 0\n\tglobal_load_lds_dwordx4 %0, %1" :: "v"(voff), "s"(sbase_), "s"(lds_dst) : "memory", "m0");
}
DI void gldssc(unsigned voff, const void* sbase_, unsigned lds_dst) {
  asm volatile("s_mov_b32 m0, %2\n\ts_nop 0\n\tglobal_load_lds_dwordx4 %0, %1 sc1" :: "v"(voff), "s"(sbase_), "s"(lds_dst) : "memory", "m0");
}

constexpr int ST_X = 0, ST_BM = 16384, ST_W1 = 26624, ST_SZ = 38912, NSTG = 4;
constexpr int SP = NSTG * ST_SZ;
constexpr int PARS = 96;
constexpr int SP_PAR = SP, SP_B1 = SP + 3840, SP_EC = SP + 4224, SP_EL = SP + 5504, SP_GW = SP + 5824, SP_G = SP + 7104,
              SP_MISC = SP + 7616, LDS_BYTES = SP + 7872;
constexpr int BUS = 528, L_BU = SP - 80 * BUS;
constexpr int HIS = 176, L_HI = SP - 128 * HIS;
constexpr int SLOT = 18432, NSLOT = 7;
enum { M_CNTE = 0, M_GRDY, M_P1D, M_ACT, M_BURDY, M_SRD, M_SWR, M_PUB, M_RED, M_NEXT = 10, M_CONS = 12, M_OFF = 16, M_HOK = 18, M_BUW = 19, M_TAG = 24, M_FULL = 32 };
constexpr int SPIN_LDS = 1 << 22, SPIN_G = 1 << 19;

struct Args {
  const u16 *x, *Lre, *Lim, *Bre, *Bim, *Cre, *Cim, *ldt, *Dsk, *w1, *b1, *w2, *gw;
  char* ws;
  float* out;
};

#define CBAR do { SCHED; asm volatile("s_waitcnt lgkmcnt(0)\n\ts_barrier" ::: "memory"); SCHED; } while (0)
#define LBAR(n) asm volatile("s_waitcnt vmcnt(" #n ")\n\ts_barrier" ::: "memory")
#define WAITV(n) asm volatile("s_waitcnt vmcnt(" #n ")" ::: "memory")

DI void lds_wait_ge(volatile LAS unsigned* w, unsigned v) {
  for (int sp = 0; *w < v && sp < SPIN_LDS; ++sp) __builtin_amdgcn_s_sleep(1);
  asm volatile("" ::: "memory");
}
DI unsigned lds_inc(char* smem, int word, int lane) {
  unsigned old = 0;
  if (lane == 0) old = __hip_atomic_fetch_add((LAS unsigned*)(smem + SP_MISC) + word, 1u, __ATOMIC_RELAXED, __HIP_MEMORY_SCOPE_WORKGROUP);
  return __builtin_amdgcn_readfirstlane(old);
}
DI void poll_flags(RSRC rW, unsigned off, int n, unsigned tlo, unsigned thi, int lane) {
  const unsigned o = off + (lane < n ? lane : 0) * 8;
  for (int sp = 0; n > 0 && sp < SPIN_G; ++sp) {
    const u32x2 f = __builtin_amdgcn_raw_buffer_load_b64(rW, o, 0, SC1);
    if (__all(f[0] == tlo && f[1] == thi)) break;
    __builtin_amdgcn_s_sleep(1);
    asm volatile("" ::: "memory");
  }
  asm volatile("" ::: "memory");
}
DI void set_flag(RSRC rW, unsigned off, unsigned tlo, unsigned thi) { u32x2 t; t[0] = tlo; t[1] = thi; st64(rW, off, t); }

DI u32x4 mish_unit(f32x4 a0, f32x4 a1, float og) {
  f32x2 m0 = mish2((f32x2){a0[0], a0[1]}) * og, m1 = mish2((f32x2){a0[2], a0[3]}) * og;
  f32x2 m2 = mish2((f32x2){a1[0], a1[1]}) * og, m3 = mish2((f32x2){a1[2], a1[3]}) * og;
  u32x4 o;
  o[0] = pack2(m0[0], m0[1]); o[1] = pack2(m1[0], m1[1]); o[2] = pack2(m2[0], m2[1]); o[3] = pack2(m3[0], m3[1]);
  return o;
}

DI void scan_task(RSRC rW, char* smem, volatile LAS unsigned* misc, int u, int lane, int rc, int j, unsigned tlo, unsigned thi) {
  int L = 64 * u + lane; const bool act = L < 160; L = act ? L : 159;
  const int seg = L / 40, chl = L - 40 * seg;
  const float* pp = (const float*)(smem + SP_PAR + chl * PARS);
  const f32x2 l1 = *(const f32x2*)pp, l128 = *(const f32x2*)(pp + 20);
  const f32x4 cl = *(const f32x4*)(pp + 4);
  f32x2 pw = seg == 1 ? (f32x2){cl[2], cl[3]} : *(const f32x2*)(pp + 16 + (seg == 3 ? 2 : 0));
  if (seg == 0) pw = (f32x2){1.f, 0.f};
  const float lr = l1[0], li = l1[1], nli = -l1[1];
  lds_wait_ge(misc + M_BUW, 2u);
  f32x4 ar[8], ai[8], wr[8], wi[8];
  {
    const char* bp = smem + L_BU + 2 * chl * BUS + seg * 128;
#pragma unroll
    for (int i = 0; i < 8; ++i) { ar[i] = *(const f32x4*)(bp + i * 16); ai[i] = *(const f32x4*)(bp + BUS + i * 16); }
  }
  float sr = 0.f, si = 0.f;
#pragma unroll
  for (int t = 0; t < 32; ++t) {
    const float tr = fmaf(nli, si, ar[t >> 2][t & 3]), ti = fmaf(li, sr, ai[t >> 2][t & 3]);
    sr = fmaf(lr, sr, tr); si = fmaf(lr, si, ti);
    ar[t >> 2][t & 3] = sr; ai[t >> 2][t & 3] = si;
  }
  if (act) *(f32x2*)(smem + SP_EC + (seg * 40 + chl) * 8) = (f32x2){sr, si};
  asm volatile("s_waitcnt lgkmcnt(0)" ::: "memory");
  lds_inc(smem, M_SRD, lane);
  lds_wait_ge(misc + M_SRD, 3u);
  float qr = 0.f, qi = 0.f;
#pragma unroll
  for (int i = 0; i < 3; ++i) {
    const f32x2 e = *(const f32x2*)(smem + SP_EC + (i * 40 + chl) * 8);
    const float tr = fmaf(-cl[3], qi, e[0]), ti = fmaf(cl[3], qr, e[1]);
    const float nr = fmaf(cl[2], qr, tr), ni = fmaf(cl[2], qi, ti);
    qr = i < seg ? nr : qr; qi = i < seg ? ni : qi;
  }
  if (act && seg == 3) {
    const float er = fmaf(cl[2], qr, fmaf(-cl[3], qi, sr)), ei = fmaf(cl[2], qi, fmaf(cl[3], qr, si));
    st128(rW, OFF_E + ((j * 16 + rc) * 40 + chl) * 16, (u32x4){__float_as_uint(er), __float_as_uint(ei), tlo, thi});
  }
  lds_wait_ge(misc + M_GRDY, 2u);
  {
    float pr = cl[0], pi = cl[1];
    const float nci_ = -cl[1];
#pragma unroll
    for (int i = 0; i < 8; ++i) {
      const f32x4 gv = *(const f32x4*)(smem + SP_G + seg * 128 + i * 16);
#pragma unroll
      for (int r = 0; r < 4; ++r) {
        const float xr = fmaf(cl[0], ar[i][r], (nci_ * ai[i][r])), xi = fmaf(cl[0], ai[i][r], (cl[1] * ar[i][r]));
        ar[i][r] = (xr * gv[r]); ai[i][r] = (xi * gv[r]);
        const float tr = (nli * pi), ti = (li * pr);
        pr = fmaf(lr, pr, tr); pi = fmaf(lr, pi, ti);
        wr[i][r] = (pr * gv[r]); wi[i][r] = (pi * gv[r]);
      }
    }
  }
  float cr = 0.f, ci = 0.f;
  {
    u32x4 ev[15];
    for (int sp = 0; sp < SPIN_G; ++sp) {
      bool ok = true;
#pragma unroll
      for (int r = 0; r < 15; ++r) {
        ev[r] = (u32x4){0u, 0u, tlo, thi};
        if (r < rc) ev[r] = __builtin_amdgcn_raw_buffer_load_b128(rW, OFF_E + ((j * 16 + r) * 40 + chl) * 16, 0, SC1);
      }
#pragma unroll
      for (int r = 0; r < 15; ++r) ok = ok && ev[r][2] == tlo && ev[r][3] == thi;
      if (__all(ok)) break;
      __builtin_amdgcn_s_sleep(1);
      asm volatile("" ::: "memory");
    }
    const float er = l128[0], ei = l128[1], nei = -l128[1];
#pragma unroll
    for (int r = 0; r < 15; ++r)
      if (r < rc) {
        const float tr = fmaf(nei, ci, __uint_as_float(ev[r][0])), ti = fmaf(ei, cr, __uint_as_float(ev[r][1]));
        cr = fmaf(er, cr, tr); ci = fmaf(er, ci, ti);
      }
    const float tr = fmaf(-pw[1], ci, qr), ti = fmaf(pw[1], cr, qi);
    cr = fmaf(pw[0], cr, tr); ci = fmaf(pw[0], ci, ti);
  }
  char* wp = smem + L_HI + seg * 32 * HIS + chl * 2;
  {
    const float nci = -ci, ncr = -cr;
#pragma unroll
    for (int t = 0; t < 32; ++t) {
      const float orr = fmaf(wr[t >> 2][t & 3], cr, fmaf(wi[t >> 2][t & 3], nci, ar[t >> 2][t & 3]));
      const float oii = fmaf(wr[t >> 2][t & 3], nci, fmaf(wi[t >> 2][t & 3], ncr, -ai[t >> 2][t & 3]));
      const unsigned pk = pack2(orr, oii);
      *(u16*)(wp + t * HIS) = (u16)pk;
      *(u16*)(wp + t * HIS + 80) = (u16)(pk >> 16);
    }
  }
  asm volatile("s_waitcnt lgkmcnt(0)" ::: "memory");
  lds_inc(smem, M_SWR, lane);
}
DI void publish_h(RSRC rW, char* smem, volatile LAS unsigned* misc, int hv, int lane, int rc, int cg, int jl, unsigned tlo, unsigned thi) {
  lds_wait_ge(misc + M_SWR, 3u);
  __builtin_amdgcn_s_setprio(3);
#pragma unroll
  for (int pe = 0; pe < 10; ++pe) {
    const int part = pe / 5, e = pe % 5;
    const int t = 64 * hv + lane, pi = 5 * jl + e, step = 12 + 2 * (pi >> 2) + part;
    const u32x4 v = *(const u32x4*)(smem + L_HI + t * HIS + part * 80 + e * 16);
    st128(rW, OFF_AF + (((rc * 4 + cg) * 22 + step) * 8 + (t >> 4)) * 1024 + ((pi & 3) * 16 + (t & 15)) * 16, v);
  }
  WAITV(0);
  if (lds_inc(smem, M_PUB, lane) == 1u) {
    if (lane == 0) set_flag(rW, OFF_FA + (rc * 4 + cg) * 128 + 32 + jl * 8, tlo, thi);
    poll_flags(rW, OFF_FA + (rc * 4 + cg) * 128 + 32, 4, tlo, thi, lane);
    if (lane == 0) misc[M_HOK] = 1u;
  }
  __builtin_amdgcn_s_setprio(0);
}

__global__ __launch_bounds__(512, 2) void fused(Args a) {
  __shared__ __attribute__((aligned(16))) char smem[LDS_BYTES];
  const int tid = threadIdx.x, lane = tid & 63;
  const int wave = __builtin_amdgcn_readfirstlane(tid >> 6);
  const int b = blockIdx.x, grp = b & 7, idx = b >> 3;
  const int cg = grp & 3, jl = idx & 3;
  const int rc = 8 * (grp >> 2) + (idx >> 2), j = 4 * cg + jl;
  const int r16 = lane & 15, g = lane >> 4;
  char* ws = a.ws;
  const RSRC rW = __builtin_amdgcn_make_buffer_rsrc((void*)ws, 0, WS_BYTES, 0x00020000);
  volatile LAS unsigned* misc = (volatile LAS unsigned*)(smem + SP_MISC);
  const unsigned sbase = (unsigned)(size_t)(LAS char*)smem;
  STAMP_DECL
  STAMP(0);

  if (wave >= 4) {
    const int lw = wave - 4;
    {
      unsigned voff[10]; const char* sb_[10]; int inc[10]; unsigned dsto[10];
#pragma unroll
      for (int i = 0; i < 10; ++i) {
        int p = lw + 4 * i; p = p >= 38 ? p - 38 : p;
        dsto[i] = sbase + p * 1024;
        const int rw = lane >> 3;
        const unsigned ox = (8 * p + rw) * (D_ * 2) + 16 * ((lane & 7) ^ (((8 * p + rw) >> 1) & 7));
        const int pb = p < 21 ? p - 16 : p - 21;
        const unsigned ob = (8 * pb + rw) * (D_ * 2) + 16 * ((lane & 7) ^ ((((rw >> 1) & 3) << 1) | (p >= 21 ? 1 : 0)));
        const int ci = 64 * (p >= 26 ? p - 26 : 0) + lane, k = ci / 12, cp = ci % 12;
        int cc = cp - 2 * ((k >> 3) & 1); cc = cc < 0 ? cc + 12 : cc;
        const unsigned ow = k * (F_ * 2) + 16 * cc;
        voff[i] = p < 16 ? ox : p < 26 ? ob : ow;
        inc[i] = p < 26 ? 128 : 64 * F_ * 2;
        sb_[i] = (const char*)(p < 16 ? a.x + (size_t)128 * rc * D_ : p < 21 ? a.Bre + (size_t)40 * j * D_ : p < 26 ? a.Bim + (size_t)40 * j * D_ : a.w1 + 96 * j);
      }
      auto stage = [&](unsigned boff) {
#pragma unroll
        for (int i = 0; i < 10; ++i) { gldss(voff[i], sb_[i], dsto[i] + boff); sb_[i] += inc[i]; }
      };
      stage(0); stage(ST_SZ);
      STAMP(1);
      LBAR(10);
      STAMP(2);
      stage(2 * ST_SZ);
      LBAR(10);
      stage(3 * ST_SZ); stage(0);
#pragma unroll 1
      for (int s = 2; s <= 6; ++s) {
        LBAR(20);
        stage(((s + 3) & 3) * ST_SZ);
      }
      LBAR(20); LBAR(10); LBAR(0);
    }
    STAMP(3);
    const unsigned tlo = misc[M_TAG], thi = misc[M_TAG + 1];

    unsigned woff[10], coff[10];
#pragma unroll
    for (int i = 0; i < 10; ++i) {
      const int ci = 64 * i + lane;
      const int k = ci / 20, cp = ci % 20;
      int cc = cp - 2 * ((k >> 2) & 1); cc = cc < 0 ? cc + 20 : cc;
      woff[i] = k * (D_ * 2) + 16 * cc;
      const int row = ci >> 2, f4 = (row >> 2) & 3, fx = (f4 == 0 ? 0 : f4 == 1 ? 2 : f4 == 2 ? 3 : 1);
      coff[i] = row * (P_ * 2) + 16 * ((ci & 3) ^ fx);
    }
    const char* wbase2 = (const char*)(a.w2 + (size_t)384 * cg * D_ + 160 * jl);
    const char* abase = ws + OFF_AF + (size_t)(rc * 4 + cg) * 22 * 8192;
    const unsigned aoff = lane * 16;
    auto issueB = [&](int n, unsigned sl) {
      if (n < 12) {
        const char* p = wbase2 + (size_t)n * (32 * D_ * 2);
#pragma unroll
        for (int i = 0; i < 10; ++i) gldss(woff[i], p, sl + 8192 + i * 1024);
      } else {
        const char* p = (const char*)(((n & 1) ? a.Cim : a.Cre) + (size_t)160 * jl * P_ + 160 * cg) + ((n - 12) >> 1) * 64;
#pragma unroll
        for (int i = 0; i < 10; ++i) gldss(coff[i], p, sl + 8192 + i * 1024);
      }
    };
    auto issueA = [&](int n, unsigned sl) {
#pragma unroll
      for (int i = 0; i < 8; ++i) gldssc(aoff, abase + (size_t)n * 8192 + i * 1024, sl + i * 1024);
    };
    int p1 = -1, p2 = -1;
    if (lw >= 2) {
      if (lw == 2) scan_task(rW, smem, misc, 2, lane, rc, j, tlo, thi);
      else {
        lds_wait_ge(misc + M_GRDY, 2u);
#pragma unroll
        for (int hh = 0; hh < 2; ++hh) {
          lds_wait_ge(misc + M_OFF + hh, 1u);
          const float og = 1.f - 0.5f * *(const float*)(smem + SP_G + (64 * hh + 48 + r16) * 4);
#pragma unroll
          for (int fp = 0; fp < 3; ++fp) {
            const char* sp = smem + hh * SLOT + fp * 2048 + lane * 16;
            st128(rW, OFF_AF + (((rc * 4 + cg) * 22 + 3 * jl + fp) * 8 + 4 * hh + 3) * 1024 + lane * 16,
                  mish_unit(*(const f32x4*)sp, *(const f32x4*)(sp + 1024), og));
          }
        }
        WAITV(0);
        if (lds_inc(smem, M_ACT, lane) == 2u && lane == 0) set_flag(rW, OFF_FA + (rc * 4 + cg) * 128 + jl * 8, tlo, thi);
      }
      STAMP(4);
      publish_h(rW, smem, misc, lw - 2, lane, rc, cg, jl, tlo, thi);
      lds_wait_ge(misc + M_FULL, 1u);
      STAMP(6);
    } else {
      const int h = lw;
      lds_wait_ge(misc + M_P1D, 4u);
#pragma unroll 1
      for (int k = 0; k < 3; ++k) issueB(2 * k + h, sbase + (2 * k + h) * SLOT);
      STAMP(4);
      poll_flags(rW, OFF_FA + (rc * 4 + cg) * 128, 4, tlo, thi, lane);
      STAMP(5);
#pragma unroll 1
      for (int k = 0; k < 3; ++k) issueA(2 * k + h, sbase + (2 * k + h) * SLOT);
      WAITV(16); if (lane == 0) misc[M_FULL + h] = 1u;
      WAITV(8);  if (lane == 0) misc[M_FULL + 2 + h] = 1u;
      p2 = 4 + h;
      lds_wait_ge(misc + M_SRD, 3u);
    }
#pragma unroll 1
    for (;;) {
      const int n = (int)lds_inc(smem, M_NEXT, lane);
      if (n >= 22) break;
      const unsigned need = n - NSLOT + 1;
      bool ok = n < NSLOT || (misc[M_CONS] >= need && misc[M_CONS + 1] >= need && misc[M_CONS + 2] >= need && misc[M_CONS + 3] >= need);
      ok = ok && (n < 12 || misc[M_HOK] != 0u);
      if (!ok) {
        WAITV(0);
        if (lane == 0) { if (p1 >= 0) misc[M_FULL + p1] = 1u; if (p2 >= 0) misc[M_FULL + p2] = 1u; }
        p1 = p2 = -1;
        if (n >= NSLOT) {
          lds_wait_ge(misc + M_CONS, need); lds_wait_ge(misc + M_CONS + 1, need); lds_wait_ge(misc + M_CONS + 2, need); lds_wait_ge(misc + M_CONS + 3, need);
        }
        if (n >= 12) lds_wait_ge(misc + M_HOK, 1u);
      }
      asm volatile("" ::: "memory");
      const unsigned sl = sbase + (n % NSLOT) * SLOT;
      issueB(n, sl); issueA(n, sl);
      WAITV(36);
      if (lane == 0 && p1 >= 0) misc[M_FULL + p1] = 1u;
      p1 = p2; p2 = n;
    }
    WAITV(18); if (lane == 0 && p1 >= 0) misc[M_FULL + p1] = 1u;
    WAITV(0);  if (lane == 0 && p2 >= 0) misc[M_FULL + p2] = 1u;
    STAMP(8);
    const int tl = 16 * (lw & 1) + r16, n0 = 160 * jl + 80 * (lw >> 1) + 4 * g;
    const size_t orow = (size_t)(128 * rc + 32 * cg + tl) * D_ + n0;
    f32x4 sk[5];
    {
      const float gt = 0.5f * *(const float*)(smem + SP_G + (32 * cg + tl) * 4);
#pragma unroll
      for (int i = 0; i < 5; ++i) {
        u32x2 xv = *(const u32x2*)(a.x + orow + 16 * i);
        u32x2 dv = *(const u32x2*)(a.Dsk + n0 + 16 * i);
        sk[i][0] = gt * bflo(xv[0]) * bflo(dv[0]); sk[i][1] = gt * bfhi(xv[0]) * bfhi(dv[0]);
        sk[i][2] = gt * bflo(xv[1]) * bflo(dv[1]); sk[i][3] = gt * bfhi(xv[1]) * bfhi(dv[1]);
      }
    }
    STAMP(22);
    for (int sp = 0; misc[M_RED] < 4u && sp < SPIN_LDS; ++sp) __builtin_amdgcn_s_sleep(4);
    asm volatile("" ::: "memory");
    STAMP(23);
    poll_flags(rW, OFF_FR + (rc * 4 + jl) * 128, 4, tlo, thi, lane);
    STAMP(24);
    if (rc == 15 && j == 0 && tid == 256) set_flag(rW, OFF_EPOCH, tlo, thi);
    u32x2 pv[3][5];
#pragma unroll
    for (int s = 0; s < 3; ++s) {
      const int sc = (cg + 1 + s) & 3;
#pragma unroll
      for (int i = 0; i < 5; ++i)
        pv[s][i] = __builtin_amdgcn_raw_buffer_load_b64(rW, OFF_PB + (((rc * 4 + jl) * 4 + cg) * 4 + sc) * 10240 + (lw >> 1) * 5120 + i * 1024 + lane * 16 + (lw & 1) * 8, 0, SC1);
    }
#pragma unroll
    for (int i = 0; i < 5; ++i) {
      f32x4 v = *(const f32x4*)(smem + (5 * lw + i) * 1024 + lane * 16) + sk[i];
#pragma unroll
      for (int s = 0; s < 3; ++s) v += (f32x4){bflo(pv[s][i][0]), bfhi(pv[s][i][0]), bflo(pv[s][i][1]), bfhi(pv[s][i][1])};
      *(f32x4*)(a.out + orow + 16 * i) = v;
    }
  } else {
    if (wave == 0 || wave == 3) {
      unsigned acc_ = 0;
      const int l = lane;
      if (wave == 0) {
        acc_ ^= *(const volatile unsigned*)((const char*)a.w1 + (size_t)(b * 60 + (l < 60 ? l : 0)) * 128);
        acc_ ^= *(const volatile unsigned*)((const char*)a.x + (size_t)(b * 80 + l) * 128);
        acc_ ^= *(const volatile unsigned*)((const char*)a.x + (size_t)(b * 80 + 64 + (l & 15)) * 128);
        acc_ ^= *(const volatile unsigned*)((const char*)(l < 32 ? a.Bre : a.Bim) + (size_t)(b * 25 + ((l & 31) < 25 ? (l & 31) : 0)) * 128);
      } else {
        acc_ ^= *(const volatile unsigned*)((const char*)a.w2 + (size_t)(b * 60 + (l < 60 ? l : 0)) * 128);
        acc_ ^= *(const volatile unsigned*)((const char*)(l < 32 ? a.Cre : a.Cim) + (size_t)(b * 25 + ((l & 31) < 25 ? (l & 31) : 0)) * 128);
      }
      asm volatile("" :: "v"(acc_));
    }
    if (wave == 1) {
      const u64 ep = __hip_atomic_load((GAS u64*)(ws + OFF_EPOCH), __ATOMIC_RELAXED, __HIP_MEMORY_SCOPE_AGENT);
      const unsigned v = *(const unsigned*)(a.b1 + 96 * j + 2 * (lane < 48 ? lane : 0));
      const u32x4 g0 = *(const u32x4*)(a.gw + 8 * lane), g1 = *(const u32x4*)(a.gw + 8 * (64 + r16));
      if (lane < 48) *(f32x2*)(smem + SP_B1 + lane * 8) = (f32x2){bflo(v), bfhi(v)};
      *(u32x4*)(smem + SP_GW + lane * 16) = g0;
      if (lane < 16) *(u32x4*)(smem + SP_GW + 1024 + lane * 16) = g1;
      if (lane < 24 || lane >= 32) misc[lane] = lane == M_NEXT ? 6u : 0u;
      if (lane == 0) { const u64 tg = ep + 1; misc[M_TAG] = (unsigned)tg; misc[M_TAG + 1] = (unsigned)(tg >> 32); }
    } else if (wave == 2) {
      const int ch = 40 * j + (lane < 40 ? lane : 0);
      const float lre = bflo(a.Lre[ch]), lim = bflo(a.Lim[ch]), dt = expf(bflo(a.ldt[ch]));
      float e = expf(dt * lre), sn, cs;
      sincosf(dt * lim, &sn, &cs);
      f32x2 l1 = {e * cs, e * sn};
      const float nr = l1[0] - 1.f, ni = l1[1], den = lre * lre + lim * lim;
      f32x2 cf = {(nr * lre + ni * lim) / den, (ni * lre - nr * lim) / den};
      f32x2 l2 = cm2(l1, l1), l4 = cm2(l2, l2), l8 = cm2(l4, l4), l12 = cm2(l8, l4), l16 = cm2(l8, l8);
      f32x2 l32 = cm2(l16, l16), l64 = cm2(l32, l32), l128 = cm2(l64, l64), l96 = cm2(l64, l32);
      if (lane < 40) {
        float* pp = (float*)(smem + SP_PAR + lane * PARS);
        *(f32x4*)(pp) = (f32x4){l1[0], l1[1], l16[0], l16[1]};
        *(f32x4*)(pp + 4) = (f32x4){cf[0], cf[1], l32[0], l32[1]};
        *(f32x4*)(pp + 8) = (f32x4){l12[0], l12[1], l8[0], l8[1]};
        *(f32x4*)(pp + 12) = (f32x4){l4[0], l4[1], 1.f, 0.f};
        *(f32x4*)(pp + 16) = (f32x4){l64[0], l64[1], l96[0], l96[1]};
        *(f32x2*)(pp + 20) = l128;
      }
    }
    STAMP(1);
    const int th = wave & 1;
    const unsigned xo0 = ST_X + (64 * th + r16) * 128 + ((g ^ (r16 >> 1)) * 16);
    const unsigned xo1 = xo0 ^ 64;
    const int q4 = r16 >> 2, p4 = r16 & 3;

#define P1_PIPE(LOADF, STEP, MMA, INIT)                                             \
    CBAR;                                                                           \
    __builtin_amdgcn_s_setprio(3);                                                  \
    STAMP(2);                                                                       \
    INIT;                                                                           \
    LOADF(F0, 0u, 0);                                                               \
    STEP(F0, F1, 0u, 1);                                                            \
    _Pragma("unroll 1") for (int s = 1; s < 10; ++s) {                              \
      CBAR;                                                                         \
      STEP(F1, F0, (s & 3) * ST_SZ, 0);                                             \
      STEP(F0, F1, (s & 3) * ST_SZ, 1);                                             \
    }                                                                               \
    asm volatile("s_waitcnt lgkmcnt(0)" ::: "memory");                              \
    lds_inc(smem, M_P1D, lane);                                                     \
    MMA(F1); SCHED; __builtin_amdgcn_s_setprio(0);

    unsigned tlo, thi;
    if (wave < 2) {
      f32x4 acc[6][4];
      const unsigned wbase = ST_W1 + (8 * g + q4) * 192 + (p4 >> 1) * 16 + (p4 & 1) * 8;
      const unsigned wa = wbase + (g & 1) * 32;
      const unsigned wb = wbase + ((g & 1) ? 0 : 160);
      struct Frag { bf16x8 x[4], w[6]; };
      Frag F0, F1;
      auto rdw = [&](const char* sb, int ft, int kk) {
        unsigned ad = (ft < 5 ? wa + 32 * ft : wb) + kk * 32 * 192;
        s16x4 lo = __builtin_amdgcn_ds_read_tr16_b64_v4i16((LAS s16x4*)(sb + ad));
        s16x4 hi = __builtin_amdgcn_ds_read_tr16_b64_v4i16((LAS s16x4*)(sb + ad + 4 * 192));
        return __builtin_bit_cast(bf16x8, __builtin_shufflevector(lo, hi, 0, 1, 2, 3, 4, 5, 6, 7));
      };
      auto loadf = [&](Frag& F, unsigned boff, int kk) {
        const char* sb = smem + boff;
#pragma unroll
        for (int tt = 0; tt < 4; ++tt) F.x[tt] = *(const bf16x8*)(sb + (kk ? xo1 : xo0) + tt * 2048);
#pragma unroll
        for (int ft = 0; ft < 6; ++ft) F.w[ft] = rdw(sb, ft, kk);
      };
      auto mma = [&](Frag& F) {
#pragma unroll
        for (int m = 0; m < 24; ++m)
          acc[m >> 2][m & 3] = __builtin_amdgcn_mfma_f32_16x16x32_bf16(F.w[m >> 2], F.x[m & 3], acc[m >> 2][m & 3], 0, 0, 0);
      };
      auto step = [&](Frag& C, Frag& N, unsigned boff, const int kk) {
        const char* sb = smem + boff;
#pragma unroll
        for (int m = 0; m < 24; ++m) {
          SCHED;
          acc[m >> 2][m & 3] = __builtin_amdgcn_mfma_f32_16x16x32_bf16(C.w[m >> 2], C.x[m & 3], acc[m >> 2][m & 3], 0, 0, 0);
          SCHED;
          if (m < 4) N.x[m] = *(const bf16x8*)(sb + (kk ? xo1 : xo0) + m * 2048);
          if (m >= 4 && m < 10) N.w[m - 4] = rdw(sb, m - 4, kk);
        }
        SCHED;
      };
      auto init = [&]() {
#pragma unroll
        for (int ft = 0; ft < 6; ++ft) {
          f32x4 bv = *(const f32x4*)(smem + SP_B1 + (16 * ft + 4 * g) * 4);
#pragma unroll
          for (int tt = 0; tt < 4; ++tt) acc[ft][tt] = bv;
        }
      };
      P1_PIPE(loadf, step, mma, init());
      STAMP(3);
      tlo = misc[M_TAG]; thi = misc[M_TAG + 1];
      lds_wait_ge(misc + M_P1D, 4u);
#pragma unroll
      for (int fp = 0; fp < 3; ++fp) {
        char* sp = smem + th * SLOT + fp * 2048 + lane * 16;
        *(f32x4*)sp = acc[2 * fp][3]; *(f32x4*)(sp + 1024) = acc[2 * fp + 1][3];
      }
      asm volatile("s_waitcnt lgkmcnt(0)" ::: "memory");
      if (lane == 0) misc[M_OFF + th] = 1u;
      lds_wait_ge(misc + M_GRDY, 2u);
#pragma unroll
      for (int tt = 0; tt < 3; ++tt) {
        const float og = 1.f - 0.5f * *(const float*)(smem + SP_G + (64 * th + 16 * tt + r16) * 4);
#pragma unroll
        for (int fp = 0; fp < 3; ++fp)
          st128(rW, OFF_AF + (((rc * 4 + cg) * 22 + 3 * jl + fp) * 8 + 4 * th + tt) * 1024 + lane * 16,
                mish_unit(acc[2 * fp][tt], acc[2 * fp + 1][tt], og));
      }
      STAMP(4);
      WAITV(0);
      if (lds_inc(smem, M_ACT, lane) == 2u && lane == 0) set_flag(rW, OFF_FA + (rc * 4 + cg) * 128 + jl * 8, tlo, thi);
      STAMP(5);
    } else {
      f32x4 acc[4][5], gac[4];
      const unsigned bo0 = ST_BM + (40 * (r16 & 1) + (r16 >> 1)) * 128 + ((g ^ ((((r16 >> 2) & 3) << 1) | (r16 & 1))) * 16);
      const unsigned bo1 = bo0 ^ 64;
      struct Frag { bf16x8 x[4], w[5], gf; };
      Frag F0, F1;
      int kc = 0;
      auto loadf = [&](Frag& F, unsigned boff, int kk) {
        const char* sb = smem + boff;
#pragma unroll
        for (int tt = 0; tt < 4; ++tt) F.x[tt] = *(const bf16x8*)(sb + (kk ? xo1 : xo0) + tt * 2048);
#pragma unroll
        for (int nt = 0; nt < 5; ++nt) F.w[nt] = *(const bf16x8*)(sb + (kk ? bo1 : bo0) + nt * 1024);
        F.gf = *(const bf16x8*)(smem + SP_GW + kc * 64 + g * 16);
        ++kc;
      };
      auto mma = [&](Frag& F) {
#pragma unroll
        for (int tt = 0; tt < 4; ++tt) gac[tt] = __builtin_amdgcn_mfma_f32_16x16x32_bf16(F.x[tt], F.gf, gac[tt], 0, 0, 0);
#pragma unroll
        for (int m = 0; m < 20; ++m)
          acc[m & 3][m >> 2] = __builtin_amdgcn_mfma_f32_16x16x32_bf16(F.x[m & 3], F.w[m >> 2], acc[m & 3][m >> 2], 0, 0, 0);
      };
      auto step = [&](Frag& C, Frag& N, unsigned boff, const int kk) {
        const char* sb = smem + boff;
#pragma unroll
        for (int m = 0; m < 24; ++m) {
          SCHED;
          if (m < 20) acc[m & 3][m >> 2] = __builtin_amdgcn_mfma_f32_16x16x32_bf16(C.x[m & 3], C.w[m >> 2], acc[m & 3][m >> 2], 0, 0, 0);
          else gac[m & 3] = __builtin_amdgcn_mfma_f32_16x16x32_bf16(C.x[m & 3], C.gf, gac[m & 3], 0, 0, 0);
          SCHED;
          if (m < 4) N.x[m] = *(const bf16x8*)(sb + (kk ? xo1 : xo0) + m * 2048);
          if (m >= 4 && m < 9) N.w[m - 4] = *(const bf16x8*)(sb + (kk ? bo1 : bo0) + (m - 4) * 1024);
          if (m == 9) N.gf = *(const bf16x8*)(smem + SP_GW + kc * 64 + g * 16);
        }
        ++kc;
        SCHED;
      };
      auto init = [&]() {
#pragma unroll
        for (int tt = 0; tt < 4; ++tt) {
          gac[tt] = (f32x4){0.f, 0.f, 0.f, 0.f};
#pragma unroll
          for (int nt = 0; nt < 5; ++nt) acc[tt][nt] = (f32x4){0.f, 0.f, 0.f, 0.f};
        }
      };
      P1_PIPE(loadf, step, mma, init());
      STAMP(3);
      tlo = misc[M_TAG]; thi = misc[M_TAG + 1];
      {
        char* bw = smem + L_BU + r16 * BUS + (64 * th + 4 * g) * 4;
#pragma unroll
        for (int tt = 0; tt < 4; ++tt)
#pragma unroll
          for (int nt = 0; nt < 5; ++nt) *(f32x4*)(bw + nt * 16 * BUS + tt * 64) = acc[tt][nt];
        f32x4 lg = r16 == 0 ? gac[0] : r16 == 1 ? gac[1] : r16 == 2 ? gac[2] : gac[3];
        f32x4 gg;
#pragma unroll
        for (int r = 0; r < 4; ++r) gg[r] = 2.f * __builtin_amdgcn_rcpf(1.f + __expf(-lg[r]));
        if (r16 < 4) *(f32x4*)(smem + SP_G + (64 * th + 16 * r16 + 4 * g) * 4) = gg;
        asm volatile("s_waitcnt lgkmcnt(0)" ::: "memory");
        lds_inc(smem, M_GRDY, lane);
        lds_inc(smem, M_BUW, lane);
      }
      STAMP(4);
      scan_task(rW, smem, misc, th, lane, rc, j, tlo, thi);
      STAMP(5);
    }

    const int wn = wave & 1, wt = wave >> 1;
    f32x4 acc[5][4];
#pragma unroll
    for (int nt = 0; nt < 5; ++nt)
#pragma unroll
      for (int tt = 0; tt < 4; ++tt) acc[nt][tt] = (f32x4){0.f, 0.f, 0.f, 0.f};
    struct Frag2 { bf16x8 x[4], w[5]; };
    Frag2 G0, G1;
    const unsigned ao = 4 * wt * 1024 + lane * 16;
    const unsigned twb = 8192 + (4 * g + q4) * 320 + (p4 >> 1) * 16 + (p4 & 1) * 8;
    const unsigned twa = twb + (5 * wn + (g & 1)) * 32;
    const unsigned twl = (wn == 1 && (g & 1)) ? twb : twa + 128;
    const int f4 = (r16 >> 2) & 3, fx = (f4 == 0 ? 0 : f4 == 1 ? 2 : f4 == 2 ? 3 : 1);
    const unsigned cwo = 8192 + (80 * wn + r16) * 64 + ((g ^ fx) * 16);
    auto rd2 = [&](Frag2& N, const char* sb, int e, const bool ssm) {
      if (e < 4) N.x[e] = *(const bf16x8*)(sb + ao + e * 1024);
      else if (ssm) N.w[e - 4] = *(const bf16x8*)(sb + cwo + (e - 4) * 1024);
      else {
        const unsigned ad = e - 4 < 4 ? twa + 32 * (e - 4) : twl;
        s16x4 lo = __builtin_amdgcn_ds_read_tr16_b64_v4i16((LAS s16x4*)(sb + ad));
        s16x4 hi = __builtin_amdgcn_ds_read_tr16_b64_v4i16((LAS s16x4*)(sb + ad + 16 * 320));
        N.w[e - 4] = __builtin_bit_cast(bf16x8, __builtin_shufflevector(lo, hi, 0, 1, 2, 3, 4, 5, 6, 7));
      }
    };
    auto wait_full = [&](int st) { lds_wait_ge(misc + M_FULL + st, 1u); };
    int slot = 0;
    unsigned fpre = 0;
    auto step2 = [&](Frag2& C, Frag2& N, int n, const bool next_ssm, const bool last) {
      if (!last && fpre == 0u) wait_full(n + 1);
      asm volatile("s_waitcnt lgkmcnt(0)" ::: "memory");
      const char* sb = smem + slot * SLOT;
      slot = slot == NSLOT - 1 ? 0 : slot + 1;
#pragma unroll
      for (int m = 0; m < 20; ++m) {
        SCHED;
        acc[m >> 2][m & 3] = __builtin_amdgcn_mfma_f32_16x16x32_bf16(C.w[m >> 2], C.x[m & 3], acc[m >> 2][m & 3], 0, 0, 0);
        SCHED;
        if (!last && m < 9) rd2(N, sb, m, next_ssm);
        if (m == 9) { if (lane == 0) misc[M_CONS + wave] = n + 1; }
        if (m == 12) fpre = misc[M_FULL + n + 2];
      }
      SCHED;
    };
    wait_full(0);
    __builtin_amdgcn_s_setprio(1);
    STAMP(6);
#pragma unroll
    for (int e = 0; e < 9; ++e) rd2(G0, smem, e, false);
    slot = 1;
#pragma unroll 1
    for (int n = 0; n < 10; n += 2) { step2(G0, G1, n, false, false); step2(G1, G0, n + 1, false, false); }
    STAMP(7);
    step2(G0, G1, 10, false, false); step2(G1, G0, 11, true, false);
    STAMP(8);
#pragma unroll 1
    for (int n = 12; n < 20; n += 2) { step2(G0, G1, n, true, false); step2(G1, G0, n + 1, true, false); }
    step2(G0, G1, 20, true, false); step2(G1, G0, 21, true, true);
    __builtin_amdgcn_s_setprio(0);
    STAMP(9);
#pragma unroll
    for (int t2 = 0; t2 < 2; ++t2) {
      const int owner = 2 * wt + t2;
      if (owner != cg) {
        const unsigned pb = OFF_PB + (((rc * 4 + jl) * 4 + owner) * 4 + cg) * 10240 + wn * 5120 + lane * 16;
#pragma unroll
        for (int nt = 0; nt < 5; ++nt) {
          const f32x4 v0 = acc[nt][2 * t2], v1 = acc[nt][2 * t2 + 1];
          u32x4 o; o[0] = pack2(v0[0], v0[1]); o[1] = pack2(v0[2], v0[3]); o[2] = pack2(v1[0], v1[1]); o[3] = pack2(v1[2], v1[3]);
          st128(rW, pb + nt * 1024, o);
        }
      }
    }
    lds_wait_ge(misc + M_CONS, 22u); lds_wait_ge(misc + M_CONS + 1, 22u); lds_wait_ge(misc + M_CONS + 2, 22u); lds_wait_ge(misc + M_CONS + 3, 22u);
#pragma unroll
    for (int tt = 0; tt < 4; ++tt) {
      if (2 * wt + (tt >> 1) == cg) {
#pragma unroll
        for (int nt = 0; nt < 5; ++nt) *(f32x4*)(smem + (5 * (2 * wn + (tt & 1)) + nt) * 1024 + lane * 16) = acc[nt][tt];
      }
    }
    STAMP(10);
    asm volatile("s_waitcnt vmcnt(0) lgkmcnt(0)" ::: "memory");
    if (lds_inc(smem, M_RED + 1, lane) == 3u) {
      if (lane == 0) { set_flag(rW, OFF_FR + (rc * 4 + jl) * 128 + cg * 8, tlo, thi); misc[M_RED] = 4u; }
    }
    STAMP(11);
  }
  STAMP(25);
  STAMP(26);
  STAMP_OUT(0);
}

extern "C" void kernel_launch(void* const* d_in, const int* in_sizes, int n_in,
                              void* d_out, int out_size, void* d_ws, size_t ws_size,
                              hipStream_t stream) {
  Args a;
  a.x   = (const u16*)d_in[0];
  a.Lre = (const u16*)d_in[1];
  a.Lim = (const u16*)d_in[2];
  a.Bre = (const u16*)d_in[3];
  a.Bim = (const u16*)d_in[4];
  a.Cre = (const u16*)d_in[5];
  a.Cim = (const u16*)d_in[6];
  a.ldt = (const u16*)d_in[7];
  a.Dsk = (const u16*)d_in[8];
  a.w1  = (const u16*)d_in[10];
  a.b1  = (const u16*)d_in[11];
  a.w2  = (const u16*)d_in[12];
  a.gw  = (const u16*)d_in[13];
  a.ws  = (char*)d_ws;
  a.out = (float*)d_out;
  fused<<<256, 512, 0, stream>>>(a);
}
```

```cpp
#include <hip/hip_runtime.h>
#include <hip/hip_bf16.h>
#include <stdint.h>
#pragma clang diagnostic ignored "-Winline-asm"

typedef __attribute__((ext_vector_type(8))) __bf16 bf16x8;
typedef __attribute__((ext_vector_type(2))) __bf16 bf16x2;
typedef __attribute__((ext_vector_type(4))) float f32x4;
typedef __attribute__((ext_vector_type(2))) float f32x2;
typedef __attribute__((ext_vector_type(4))) unsigned u32x4;
typedef __attribute__((ext_vector_type(2))) unsigned u32x2;
typedef __attribute__((ext_vector_type(4))) short s16x4;
typedef unsigned short u16;
#define LAS __attribute__((address_space(3)))
#define GAS __attribute__((address_space(1)))
#define DI __device__ __forceinline__

constexpr int T_ = 2048, D_ = 640, P_ = 640, F_ = 1536;

constexpr unsigned OFF_AF = 0;
constexpr unsigned OFF_PB = OFF_AF + 64u * 22 * 8192;
constexpr unsigned OFF_E  = OFF_PB + 64u * 16 * 10240;
constexpr unsigned OFF_FA = OFF_E + 256u * 640;
constexpr unsigned OFF_FE = OFF_FA + 64 * 128;
constexpr unsigned OFF_FR = OFF_FE + 16 * 128;
constexpr unsigned OFF_EPOCH = OFF_FR + 64 * 128;
constexpr unsigned WS_BYTES = OFF_EPOCH + 256;

#define STAMP(i)
#define STAMP_DECL
#define STAMP_OUT(kid)

DI float bflo(unsigned v) { return __uint_as_float(v << 16); }
DI float bfhi(unsigned v) { return __uint_as_float(v & 0xffff0000u); }
DI unsigned pack2(float a, float b) {
  bf16x2 r; r[0] = (__bf16)a; r[1] = (__bf16)b; return __builtin_bit_cast(unsigned, r);
}
DI float dswap(float v) {
  return __int_as_float(__builtin_amdgcn_mov_dpp(__float_as_int(v), 0xB1, 0xf, 0xf, true));
}
DI float cmul(float lr, float ls, float v) { return fmaf(lr, v, ls * dswap(v)); }
DI float cmuladd(float lr, float ls, float v, float a) { return fmaf(lr, v, fmaf(ls, dswap(v), a)); }
DI f32x2 cm2(f32x2 a, f32x2 b) { return (f32x2){a[0] * b[0] - a[1] * b[1], a[0] * b[1] + a[1] * b[0]}; }
DI float sfma(float a, float b, float c) { float d; asm("v_fma_f32 %0, %1, %2, %3" : "=v"(d) : "v"(a), "v"(b), "v"(c)); return d; }
DI float smul(float a, float b) { float d; asm("v_mul_f32 %0, %1, %2" : "=v"(d) : "v"(a), "v"(b)); return d; }

DI f32x2 mish2(f32x2 h) {
  f32x2 z = h * 1.44269504088896341f;
  f32x2 w; w[0] = __builtin_amdgcn_exp2f(fminf(z[0], 43.f)); w[1] = __builtin_amdgcn_exp2f(fminf(z[1], 43.f));
  f32x2 n = w * (w + 2.f);
  f32x2 d = n + 2.f;
  f32x2 r; r[0] = __builtin_amdgcn_rcpf(d[0]); r[1] = __builtin_amdgcn_rcpf(d[1]);
  return (h * n) * r;
}

DI void glds16(const void* gsrc, unsigned lds_dst) {
  unsigned keep;
  asm volatile("s_mov_b32 %0, m0\n\ts_mov_b32 m0, %2\n\ts_nop 0\n\tglobal_load_lds_dwordx4 %1, off\n\ts_mov_b32 m0, %0"
               : "=&s"(keep) : "v"(gsrc), "s"(lds_dst) : "memory");
}
DI void touch(const void* gsrc, unsigned lds_dst) {
  unsigned keep;
  asm volatile("s_mov_b32 %0, m0\n\ts_mov_b32 m0, %2\n\ts_nop 0\n\tglobal_load_lds_dword %1, off\n\ts_mov_b32 m0, %0"
               : "=&s"(keep) : "v"(gsrc), "s"(lds_dst) : "memory");
}
DI float sum16(float v) {
  auto r = __builtin_amdgcn_permlane16_swap(__float_as_uint(v), __float_as_uint(v), false, false);
  return __uint_as_float(r[0]) + __uint_as_float(r[1]);
}
DI float sum32(float v) {
  auto r = __builtin_amdgcn_permlane32_swap(__float_as_uint(v), __float_as_uint(v), false, false);
  return __uint_as_float(r[0]) + __uint_as_float(r[1]);
}

typedef unsigned long long u64;
#define SC1 16
#define SCHED __builtin_amdgcn_sched_barrier(0)
#define RSRC __amdgpu_buffer_rsrc_t
DI void st128(RSRC r, unsigned off, u32x4 v) { __builtin_amdgcn_raw_buffer_store_b128(v, r, off, 0, SC1); }
DI void st128f(RSRC r, unsigned off, f32x4 v) { __builtin_amdgcn_raw_buffer_store_b128(__builtin_bit_cast(u32x4, v), r, off, 0, SC1); }
DI void st64(RSRC r, unsigned off, u32x2 v) { __builtin_amdgcn_raw_buffer_store_b64(v, r, off, 0, SC1); }
DI f32x4 ld128f(RSRC r, unsigned off) { return __builtin_bit_cast(f32x4, __builtin_amdgcn_raw_buffer_load_b128(r, off, 0, SC1)); }
DI f32x2 ld64f(RSRC r, unsigned off) { return __builtin_bit_cast(f32x2, __builtin_amdgcn_raw_buffer_load_b64(r, off, 0, SC1)); }
DI void glds16c(const void* gsrc, unsigned lds_dst) {
  unsigned keep;
  asm volatile("s_mov_b32 %0, m0\n\ts_mov_b32 m0, %2\n\ts_nop 0\n\tglobal_load_lds_dwordx4 %1, off sc1\n\ts_mov_b32 m0, %0"
               : "=&s"(keep) : "v"(gsrc), "s"(lds_dst) : "memory");
}

DI void gldss(unsigned voff, const void* sbase_, unsigned lds_dst) {
  asm volatile("s_mov_b32 m0, %2\n\ts_nop 0\n\tglobal_load_lds_dwordx4 %0, %1" :: "v"(voff), "s"(sbase_), "s"(lds_dst) : "memory", "m0");
}
DI void gldssc(unsigned voff, const void* sbase_, unsigned lds_dst) {
  asm volatile("s_mov_b32 m0, %2\n\ts_nop 0\n\tglobal_load_lds_dwordx4 %0, %1 sc1" :: "v"(voff), "s"(sbase_), "s"(lds_dst) : "memory", "m0");
}

constexpr int ST_X = 0, ST_BM = 16384, ST_W1 = 26624, ST_SZ = 38912, NSTG = 4;
constexpr int SP = NSTG * ST_SZ;
constexpr int PARS = 96;
constexpr int SP_PAR = SP, SP_B1 = SP + 3840, SP_EC = SP + 4224, SP_EL = SP + 5504, SP_GW = SP + 5824, SP_G = SP + 7104,
              SP_MISC = SP + 7616, LDS_BYTES = SP + 7872;
constexpr int BUS = 528, L_BU = SP - 80 * BUS;
constexpr int HIS = 176, L_HI = SP - 128 * HIS;
constexpr int SLOT = 18432, NSLOT = 7;
enum { M_CNTE = 0, M_GRDY, M_P1D, M_ACT, M_BURDY, M_SRD, M_SWR, M_PUB, M_RED, M_NEXT = 10, M_CONS = 12, M_OFF = 16, M_HOK = 18, M_BUW = 19, M_TAG = 24, M_FULL = 32 };
constexpr int SPIN_LDS = 1 << 22, SPIN_G = 1 << 19;

struct Args {
  const u16 *x, *Lre, *Lim, *Bre, *Bim, *Cre, *Cim, *ldt, *Dsk, *w1, *b1, *w2, *gw;
  char* ws;
  float* out;
};

#define CBAR do { SCHED; asm volatile("s_waitcnt lgkmcnt(0)\n\ts_barrier" ::: "memory"); SCHED; } while (0)
#define LBAR(n) asm volatile("s_waitcnt vmcnt(" #n ")\n\ts_barrier" ::: "memory")
#define WAITV(n) asm volatile("s_waitcnt vmcnt(" #n ")" ::: "memory")

DI void lds_wait_ge(volatile LAS unsigned* w, unsigned v) {
  for (int sp = 0; *w < v && sp < SPIN_LDS; ++sp) __builtin_amdgcn_s_sleep(1);
  asm volatile("" ::: "memory");
}
DI unsigned lds_inc(char* smem, int word, int lane) {
  unsigned old = 0;
  if (lane == 0) old = __hip_atomic_fetch_add((LAS unsigned*)(smem + SP_MISC) + word, 1u, __ATOMIC_RELAXED, __HIP_MEMORY_SCOPE_WORKGROUP);
  return __builtin_amdgcn_readfirstlane(old);
}
DI void poll_flags(RSRC rW, unsigned off, int n, unsigned tlo, unsigned thi, int lane) {
  const unsigned o = off + (lane < n ? lane : 0) * 8;
  for (int sp = 0; n > 0 && sp < SPIN_G; ++sp) {
    const u32x2 f = __builtin_amdgcn_raw_buffer_load_b64(rW, o, 0, SC1);
    if (__all(f[0] == tlo && f[1] == thi)) break;
    __builtin_amdgcn_s_sleep(1);
    asm volatile("" ::: "memory");
  }
  asm volatile("" ::: "memory");
}
DI void set_flag(RSRC rW, unsigned off, unsigned tlo, unsigned thi) { u32x2 t; t[0] = tlo; t[1] = thi; st64(rW, off, t); }

DI u32x4 mish_unit(f32x4 a0, f32x4 a1, float og) {
  f32x2 m0 = mish2((f32x2){a0[0], a0[1]}) * og, m1 = mish2((f32x2){a0[2], a0[3]}) * og;
  f32x2 m2 = mish2((f32x2){a1[0], a1[1]}) * og, m3 = mish2((f32x2){a1[2], a1[3]}) * og;
  u32x4 o;
  o[0] = pack2(m0[0], m0[1]); o[1] = pack2(m1[0], m1[1]); o[2] = pack2(m2[0], m2[1]); o[3] = pack2(m3[0], m3[1]);
  return o;
}

DI void scan_task(RSRC rW, char* smem, volatile LAS unsigned* misc, int u, int lane, int rc, int j, unsigned tlo, unsigned thi) {
  int L = 64 * u + lane; const bool act = L < 160; L = act ? L : 159;
  const int seg = L / 40, chl = L - 40 * seg;
  const float* pp = (const float*)(smem + SP_PAR + chl * PARS);
  const f32x2 l1 = *(const f32x2*)pp, l128 = *(const f32x2*)(pp + 20);
  const f32x4 cl = *(const f32x4*)(pp + 4);
  f32x2 pw = seg == 1 ? (f32x2){cl[2], cl[3]} : *(const f32x2*)(pp + 16 + (seg == 3 ? 2 : 0));
  if (seg == 0) pw = (f32x2){1.f, 0.f};
  const float lr = l1[0], li = l1[1], nli = -l1[1];
  lds_wait_ge(misc + M_BUW, 2u);
  f32x4 ar[8], ai[8], wr[8], wi[8];
  {
    const char* bp = smem + L_BU + 2 * chl * BUS + seg * 128;
#pragma unroll
    for (int i = 0; i < 8; ++i) { ar[i] = *(const f32x4*)(bp + i * 16); ai[i] = *(const f32x4*)(bp + BUS + i * 16); }
  }
  float sr = 0.f, si = 0.f;
#pragma unroll
  for (int t = 0; t < 32; ++t) {
    const float tr = fmaf(nli, si, ar[t >> 2][t & 3]), ti = fmaf(li, sr, ai[t >> 2][t & 3]);
    sr = fmaf(lr, sr, tr); si = fmaf(lr, si, ti);
    ar[t >> 2][t & 3] = sr; ai[t >> 2][t & 3] = si;
  }
  if (act) *(f32x2*)(smem + SP_EC + (seg * 40 + chl) * 8) = (f32x2){sr, si};
  asm volatile("s_waitcnt lgkmcnt(0)" ::: "memory");
  lds_inc(smem, M_SRD, lane);
  lds_wait_ge(misc + M_SRD, 3u);
  float qr = 0.f, qi = 0.f;
#pragma unroll
  for (int i = 0; i < 3; ++i) {
    const f32x2 e = *(const f32x2*)(smem + SP_EC + (i * 40 + chl) * 8);
    const float tr = fmaf(-cl[3], qi, e[0]), ti = fmaf(cl[3], qr, e[1]);
    const float nr = fmaf(cl[2], qr, tr), ni = fmaf(cl[2], qi, ti);
    qr = i < seg ? nr : qr; qi = i < seg ? ni : qi;
  }
  if (act && seg == 3) {
    const float er = fmaf(cl[2], qr, fmaf(-cl[3], qi, sr)), ei = fmaf(cl[2], qi, fmaf(cl[3], qr, si));
    st128(rW, OFF_E + ((j * 16 + rc) * 40 + chl) * 16, (u32x4){__float_as_uint(er), __float_as_uint(ei), tlo, thi});
  }
  lds_wait_ge(misc + M_GRDY, 2u);
  {
    float pr = cl[0], pi = cl[1];
    const float nci_ = -cl[1];
#pragma unroll
    for (int i = 0; i < 8; ++i) {
      const f32x4 gv = *(const f32x4*)(smem + SP_G + seg * 128 + i * 16);
#pragma unroll
      for (int r = 0; r < 4; ++r) {
        const float xr = fmaf(cl[0], ar[i][r], (nci_ * ai[i][r])), xi = fmaf(cl[0], ai[i][r], (cl[1] * ar[i][r]));
        ar[i][r] = (xr * gv[r]); ai[i][r] = (xi * gv[r]);
        const float tr = (nli * pi), ti = (li * pr);
        pr = fmaf(lr, pr, tr); pi = fmaf(lr, pi, ti);
        wr[i][r] = (pr * gv[r]); wi[i][r] = (pi * gv[r]);
      }
    }
  }
  float cr = 0.f, ci = 0.f;
  {
    u32x4 ev[15];
    for (int sp = 0; sp < SPIN_G; ++sp) {
      bool ok = true;
#pragma unroll
      for (int r = 0; r < 15; ++r) {
        ev[r] = (u32x4){0u, 0u, tlo, thi};
        if (r < rc) ev[r] = __builtin_amdgcn_raw_buffer_load_b128(rW, OFF_E + ((j * 16 + r) * 40 + chl) * 16, 0, SC1);
      }
#pragma unroll
      for (int r = 0; r < 15; ++r) ok = ok && ev[r][2] == tlo && ev[r][3] == thi;
      if (__all(ok)) break;
      __builtin_amdgcn_s_sleep(1);
      asm volatile("" ::: "memory");
    }
    const float er = l128[0], ei = l128[1], nei = -l128[1];
#pragma unroll
    for (int r = 0; r < 15; ++r)
      if (r < rc) {
        const float tr = fmaf(nei, ci, __uint_as_float(ev[r][0])), ti = fmaf(ei, cr, __uint_as_float(ev[r][1]));
        cr = fmaf(er, cr, tr); ci = fmaf(er, ci, ti);
      }
    const float tr = fmaf(-pw[1], ci, qr), ti = fmaf(pw[1], cr, qi);
    cr = fmaf(pw[0], cr, tr); ci = fmaf(pw[0], ci, ti);
  }
  char* wp = smem + L_HI + seg * 32 * HIS + chl * 2;
  {
    const float nci = -ci, ncr = -cr;
#pragma unroll
    for (int t = 0; t < 32; ++t) {
      const float orr = fmaf(wr[t >> 2][t & 3], cr, fmaf(wi[t >> 2][t & 3], nci, ar[t >> 2][t & 3]));
      const float oii = fmaf(wr[t >> 2][t & 3], nci, fmaf(wi[t >> 2][t & 3], ncr, -ai[t >> 2][t & 3]));
      const unsigned pk = pack2(orr, oii);
      *(u16*)(wp + t * HIS) = (u16)pk;
      *(u16*)(wp + t * HIS + 80) = (u16)(pk >> 16);
    }
  }
  asm volatile("s_waitcnt lgkmcnt(0)" ::: "memory");
  lds_inc(smem, M_SWR, lane);
}
DI void publish_h(RSRC rW, char* smem, volatile LAS unsigned* misc, int hv, int lane, int rc, int cg, int jl, unsigned tlo, unsigned thi) {
  lds_wait_ge(misc + M_SWR, 3u);
  __builtin_amdgcn_s_setprio(3);
#pragma unroll
  for (int pe = 0; pe < 10; ++pe) {
    const int part = pe / 5, e = pe % 5;
    const int t = 64 * hv + lane, pi = 5 * jl + e, step = 12 + 2 * (pi >> 2) + part;
    const u32x4 v = *(const u32x4*)(smem + L_HI + t * HIS + part * 80 + e * 16);
    st128(rW, OFF_AF + (((rc * 4 + cg) * 22 + step) * 8 + (t >> 4)) * 1024 + ((pi & 3) * 16 + (t & 15)) * 16, v);
  }
  WAITV(0);
  if (lds_inc(smem, M_PUB, lane) == 1u) {
    if (lane == 0) set_flag(rW, OFF_FA + (rc * 4 + cg) * 128 + 32 + jl * 8, tlo, thi);
    poll_flags(rW, OFF_FA + (rc * 4 + cg) * 128 + 32, 4, tlo, thi, lane);
    if (lane == 0) misc[M_HOK] = 1u;
  }
  __builtin_amdgcn_s_setprio(0);
}

__global__ __launch_bounds__(512, 2) void fused(Args a) {
  __shared__ __attribute__((aligned(16))) char smem[LDS_BYTES];
  const int tid = threadIdx.x, lane = tid & 63;
  const int wave = __builtin_amdgcn_readfirstlane(tid >> 6);
  const int b = blockIdx.x, grp = b & 7, idx = b >> 3;
  const int cg = grp & 3, jl = idx & 3;
  const int rc = 8 * (grp >> 2) + (idx >> 2), j = 4 * cg + jl;
  const int r16 = lane & 15, g = lane >> 4;
  char* ws = a.ws;
  const RSRC rW = __builtin_amdgcn_make_buffer_rsrc((void*)ws, 0, WS_BYTES, 0x00020000);
  volatile LAS unsigned* misc = (volatile LAS unsigned*)(smem + SP_MISC);
  const unsigned sbase = (unsigned)(size_t)(LAS char*)smem;
  STAMP_DECL
  STAMP(0);

  if (wave >= 4) {
    const int lw = wave - 4;
    {
      unsigned voff[10]; const char* sb_[10]; int inc[10]; unsigned dsto[10];
#pragma unroll
      for (int i = 0; i < 10; ++i) {
        int p = lw + 4 * i; p = p >= 38 ? p - 38 : p;
        dsto[i] = sbase + p * 1024;
        const int rw = lane >> 3;
        const unsigned ox = (8 * p + rw) * (D_ * 2) + 16 * ((lane & 7) ^ (((8 * p + rw) >> 1) & 7));
        const int pb = p < 21 ? p - 16 : p - 21;
        const unsigned ob = (8 * pb + rw) * (D_ * 2) + 16 * ((lane & 7) ^ ((((rw >> 1) & 3) << 1) | (p >= 21 ? 1 : 0)));
        const int ci = 64 * (p >= 26 ? p - 26 : 0) + lane, k = ci / 12, cp = ci % 12;
        int cc = cp - 2 * ((k >> 3) & 1); cc = cc < 0 ? cc + 12 : cc;
        const unsigned ow = k * (F_ * 2) + 16 * cc;
        voff[i] = p < 16 ? ox : p < 26 ? ob : ow;
        inc[i] = p < 26 ? 128 : 64 * F_ * 2;
        sb_[i] = (const char*)(p < 16 ? a.x + (size_t)128 * rc * D_ : p < 21 ? a.Bre + (size_t)40 * j * D_ : p < 26 ? a.Bim + (size_t)40 * j * D_ : a.w1 + 96 * j);
      }
      auto stage = [&](unsigned boff) {
#pragma unroll
        for (int i = 0; i < 10; ++i) { gldss(voff[i], sb_[i], dsto[i] + boff); sb_[i] += inc[i]; }
      };
      stage(0); stage(ST_SZ);
      STAMP(1);
      LBAR(10);
      STAMP(2);
      stage(2 * ST_SZ);
      LBAR(10);
      stage(3 * ST_SZ); stage(0);
#pragma unroll 1
      for (int s = 2; s <= 6; ++s) {
        LBAR(20);
        stage(((s + 3) & 3) * ST_SZ);
      }
      LBAR(20); LBAR(10); LBAR(0);
    }
    STAMP(3);
    const unsigned tlo = misc[M_TAG], thi = misc[M_TAG + 1];

    unsigned woff[10], coff[10];
#pragma unroll
    for (int i = 0; i < 10; ++i) {
      const int ci = 64 * i + lane;
      const int k = ci / 20, cp = ci % 20;
      int cc = cp - 2 * ((k >> 2) & 1); cc = cc < 0 ? cc + 20 : cc;
      woff[i] = k * (D_ * 2) + 16 * cc;
      const int row = ci >> 2, f4 = (row >> 2) & 3, fx = (f4 == 0 ? 0 : f4 == 1 ? 2 : f4 == 2 ? 3 : 1);
      coff[i] = row * (P_ * 2) + 16 * ((ci & 3) ^ fx);
    }
    const char* wbase2 = (const char*)(a.w2 + (size_t)384 * cg * D_ + 160 * jl);
    const char* abase = ws + OFF_AF + (size_t)(rc * 4 + cg) * 22 * 8192;
    const unsigned aoff = lane * 16;
    auto issueB = [&](int n, unsigned sl) {
      if (n < 12) {
        const char* p = wbase2 + (size_t)n * (32 * D_ * 2);
#pragma unroll
        for (int i = 0; i < 10; ++i) gldss(woff[i], p, sl + 8192 + i * 1024);
      } else {
        const char* p = (const char*)(((n & 1) ? a.Cim : a.Cre) + (size_t)160 * jl * P_ + 160 * cg) + ((n - 12) >> 1) * 64;
#pragma unroll
        for (int i = 0; i < 10; ++i) gldss(coff[i], p, sl + 8192 + i * 1024);
      }
    };
    auto issueA = [&](int n, unsigned sl) {
#pragma unroll
      for (int i = 0; i < 8; ++i) gldssc(aoff, abase + (size_t)n * 8192 + i * 1024, sl + i * 1024);
    };
    int p1 = -1, p2 = -1;
    if (lw >= 2) {
      if (lw == 2) scan_task(rW, smem, misc, 2, lane, rc, j, tlo, thi);
      else {
        lds_wait_ge(misc + M_GRDY, 2u);
#pragma unroll
        for (int hh = 0; hh < 2; ++hh) {
          lds_wait_ge(misc + M_OFF + hh, 1u);
          const float og = 1.f - 0.5f * *(const float*)(smem + SP_G + (64 * hh + 48 + r16) * 4);
#pragma unroll
          for (int fp = 0; fp < 3; ++fp) {
            const char* sp = smem + hh * SLOT + fp * 2048 + lane * 16;
            st128(rW, OFF_AF + (((rc * 4 + cg) * 22 + 3 * jl + fp) * 8 + 4 * hh + 3) * 1024 + lane * 16,
                  mish_unit(*(const f32x4*)sp, *(const f32x4*)(sp + 1024), og));
          }
        }
        WAITV(0);
        if (lds_inc(smem, M_ACT, lane) == 2u && lane == 0) set_flag(rW, OFF_FA + (rc * 4 + cg) * 128 + jl * 8, tlo, thi);
      }
      STAMP(4);
      publish_h(rW, smem, misc, lw - 2, lane, rc, cg, jl, tlo, thi);
      lds_wait_ge(misc + M_FULL, 1u);
      STAMP(6);
    } else {
      const int h = lw;
      lds_wait_ge(misc + M_P1D, 4u);
#pragma unroll 1
      for (int k = 0; k < 3; ++k) issueB(2 * k + h, sbase + (2 * k + h) * SLOT);
      STAMP(4);
      poll_flags(rW, OFF_FA + (rc * 4 + cg) * 128, 4, tlo, thi, lane);
      STAMP(5);
#pragma unroll 1
      for (int k = 0; k < 3; ++k) issueA(2 * k + h, sbase + (2 * k + h) * SLOT);
      WAITV(16); if (lane == 0) misc[M_FULL + h] = 1u;
      WAITV(8);  if (lane == 0) misc[M_FULL + 2 + h] = 1u;
      p2 = 4 + h;
      lds_wait_ge(misc + M_SRD, 3u);
    }
#pragma unroll 1
    for (;;) {
      const int n = (int)lds_inc(smem, M_NEXT, lane);
      if (n >= 22) break;
      const unsigned need = n - NSLOT + 1;
      bool ok = n < NSLOT || (misc[M_CONS] >= need && misc[M_CONS + 1] >= need && misc[M_CONS + 2] >= need && misc[M_CONS + 3] >= need);
      ok = ok && (n < 12 || misc[M_HOK] != 0u);
      if (!ok) {
        WAITV(0);
        if (lane == 0) { if (p1 >= 0) misc[M_FULL + p1] = 1u; if (p2 >= 0) misc[M_FULL + p2] = 1u; }
        p1 = p2 = -1;
        if (n >= NSLOT) {
          lds_wait_ge(misc + M_CONS, need); lds_wait_ge(misc + M_CONS + 1, need); lds_wait_ge(misc + M_CONS + 2, need); lds_wait_ge(misc + M_CONS + 3, need);
        }
        if (n >= 12) lds_wait_ge(misc + M_HOK, 1u);
      }
      asm volatile("" ::: "memory");
      const unsigned sl = sbase + (n % NSLOT) * SLOT;
      issueB(n, sl); issueA(n, sl);
      WAITV(36);
      if (lane == 0 && p1 >= 0) misc[M_FULL + p1] = 1u;
      p1 = p2; p2 = n;
    }
    WAITV(18); if (lane == 0 && p1 >= 0) misc[M_FULL + p1] = 1u;
    WAITV(0);  if (lane == 0 && p2 >= 0) misc[M_FULL + p2] = 1u;
    STAMP(8);
    const int tl = 16 * (lw & 1) + r16, n0 = 160 * jl + 80 * (lw >> 1) + 4 * g;
    const size_t orow = (size_t)(128 * rc + 32 * cg + tl) * D_ + n0;
    f32x4 sk[5];
    {
      const float gt = 0.5f * *(const float*)(smem + SP_G + (32 * cg + tl) * 4);
#pragma unroll
      for (int i = 0; i < 5; ++i) {
        u32x2 xv = *(const u32x2*)(a.x + orow + 16 * i);
        u32x2 dv = *(const u32x2*)(a.Dsk + n0 + 16 * i);
        sk[i][0] = gt * bflo(xv[0]) * bflo(dv[0]); sk[i][1] = gt * bfhi(xv[0]) * bfhi(dv[0]);
        sk[i][2] = gt * bflo(xv[1]) * bflo(dv[1]); sk[i][3] = gt * bfhi(xv[1]) * bfhi(dv[1]);
      }
    }
    STAMP(22);
    for (int sp = 0; misc[M_RED] < 4u && sp < SPIN_LDS; ++sp) __builtin_amdgcn_s_sleep(4);
    asm volatile("" ::: "memory");
    STAMP(23);
    poll_flags(rW, OFF_FR + (rc * 4 + jl) * 128, 4, tlo, thi, lane);
    STAMP(24);
    if (rc == 15 && j == 0 && tid == 256) set_flag(rW, OFF_EPOCH, tlo, thi);
    u32x2 pv[3][5];
#pragma unroll
    for (int s = 0; s < 3; ++s) {
      const int sc = (cg + 1 + s) & 3;
#pragma unroll
      for (int i = 0; i < 5; ++i)
        pv[s][i] = __builtin_amdgcn_raw_buffer_load_b64(rW, OFF_PB + (((rc * 4 + jl) * 4 + cg) * 4 + sc) * 10240 + (lw >> 1) * 5120 + i * 1024 + lane * 16 + (lw & 1) * 8, 0, SC1);
    }
#pragma unroll
    for (int i = 0; i < 5; ++i) {
      f32x4 v = *(const f32x4*)(smem + (5 * lw + i) * 1024 + lane * 16) + sk[i];
#pragma unroll
      for (int s = 0; s < 3; ++s) v += (f32x4){bflo(pv[s][i][0]), bfhi(pv[s][i][0]), bflo(pv[s][i][1]), bfhi(pv[s][i][1])};
      *(f32x4*)(a.out + orow + 16 * i) = v;
    }
  } else {
    if (wave == 3) {
      unsigned long long pc; unsigned back;
      asm volatile("s_getpc_b64 %0\n1:\n\ts_mov_b32 %1, 1b-_Z5fused4Args" : "=s"(pc), "=s"(back));
      touch((const char*)(pc - back) + (idx * 12 + (lane < 12 ? lane : 0)) * 128, sbase + SP_EL);
      touch((const char*)a.w1 + (size_t)(b * 60 + (lane < 60 ? lane : 0)) * 128, sbase + SP_EL);
      touch((const char*)(lane < 32 ? a.Bre : a.Bim) + (size_t)(b * 25 + ((lane & 31) < 25 ? (lane & 31) : 0)) * 128, sbase + SP_EL);
    } else if (wave == 0) {
      touch((const char*)a.x + (size_t)(b * 80 + lane) * 128, sbase + SP_EL);
      touch((const char*)a.x + (size_t)(b * 80 + 64 + r16) * 128, sbase + SP_EL);
      touch((const char*)a.w2 + (size_t)(b * 60 + (lane < 60 ? lane : 0)) * 128, sbase + SP_EL);
      touch((const char*)(lane < 32 ? a.Cre : a.Cim) + (size_t)(b * 25 + ((lane & 31) < 25 ? (lane & 31) : 0)) * 128, sbase + SP_EL);
    }
    if (wave == 1) {
      const u64 ep = __hip_atomic_load((GAS u64*)(ws + OFF_EPOCH), __ATOMIC_RELAXED, __HIP_MEMORY_SCOPE_AGENT);
      const unsigned v = *(const unsigned*)(a.b1 + 96 * j + 2 * (lane < 48 ? lane : 0));
      const u32x4 g0 = *(const u32x4*)(a.gw + 8 * lane), g1 = *(const u32x4*)(a.gw + 8 * (64 + r16));
      if (lane < 48) *(f32x2*)(smem + SP_B1 + lane * 8) = (f32x2){bflo(v), bfhi(v)};
      *(u32x4*)(smem + SP_GW + lane * 16) = g0;
      if (lane < 16) *(u32x4*)(smem + SP_GW + 1024 + lane * 16) = g1;
      if (lane < 24 || lane >= 32) misc[lane] = lane == M_NEXT ? 6u : 0u;
      if (lane == 0) { const u64 tg = ep + 1; misc[M_TAG] = (unsigned)tg; misc[M_TAG + 1] = (unsigned)(tg >> 32); }
    } else if (wave == 2) {
      const int ch = 40 * j + (lane < 40 ? lane : 0);
      const float lre = bflo(a.Lre[ch]), lim = bflo(a.Lim[ch]), dt = expf(bflo(a.ldt[ch]));
      float e = expf(dt * lre), sn, cs;
      sincosf(dt * lim, &sn, &cs);
      f32x2 l1 = {e * cs, e * sn};
      const float nr = l1[0] - 1.f, ni = l1[1], den = lre * lre + lim * lim;
      f32x2 cf = {(nr * lre + ni * lim) / den, (ni * lre - nr * lim) / den};
      f32x2 l2 = cm2(l1, l1), l4 = cm2(l2, l2), l8 = cm2(l4, l4), l12 = cm2(l8, l4), l16 = cm2(l8, l8);
      f32x2 l32 = cm2(l16, l16), l64 = cm2(l32, l32), l128 = cm2(l64, l64), l96 = cm2(l64, l32);
      if (lane < 40) {
        float* pp = (float*)(smem + SP_PAR + lane * PARS);
        *(f32x4*)(pp) = (f32x4){l1[0], l1[1], l16[0], l16[1]};
        *(f32x4*)(pp + 4) = (f32x4){cf[0], cf[1], l32[0], l32[1]};
        *(f32x4*)(pp + 8) = (f32x4){l12[0], l12[1], l8[0], l8[1]};
        *(f32x4*)(pp + 12) = (f32x4){l4[0], l4[1], 1.f, 0.f};
        *(f32x4*)(pp + 16) = (f32x4){l64[0], l64[1], l96[0], l96[1]};
        *(f32x2*)(pp + 20) = l128;
      }
    }
    STAMP(1);
    const int th = wave & 1;
    const unsigned xo0 = ST_X + (64 * th + r16) * 128 + ((g ^ (r16 >> 1)) * 16);
    const unsigned xo1 = xo0 ^ 64;
    const int q4 = r16 >> 2, p4 = r16 & 3;

#define P1_PIPE(LOADF, STEP, MMA, INIT)                                             \
    CBAR;                                                                           \
    __builtin_amdgcn_s_setprio(3);                                                  \
    STAMP(2);                                                                       \
    INIT;                                                                           \
    LOADF(F0, 0u, 0);                                                               \
    STEP(F0, F1, 0u, 1);                                                            \
    _Pragma("unroll 1") for (int s = 1; s < 10; ++s) {                              \
      CBAR;                                                                         \
      STEP(F1, F0, (s & 3) * ST_SZ, 0);                                             \
      STEP(F0, F1, (s & 3) * ST_SZ, 1);                                             \
    }                                                                               \
    asm volatile("s_waitcnt lgkmcnt(0)" ::: "memory");                              \
    lds_inc(smem, M_P1D, lane);                                                     \
    MMA(F1); SCHED; __builtin_amdgcn_s_setprio(0);

    unsigned tlo, thi;
    if (wave < 2) {
      f32x4 acc[6][4];
      const unsigned wbase = ST_W1 + (8 * g + q4) * 192 + (p4 >> 1) * 16 + (p4 & 1) * 8;
      const unsigned wa = wbase + (g & 1) * 32;
      const unsigned wb = wbase + ((g & 1) ? 0 : 160);
      struct Frag { bf16x8 x[4], w[6]; };
      Frag F0, F1;
      auto rdw = [&](const char* sb, int ft, int kk) {
        unsigned ad = (ft < 5 ? wa + 32 * ft : wb) + kk * 32 * 192;
        s16x4 lo = __builtin_amdgcn_ds_read_tr16_b64_v4i16((LAS s16x4*)(sb + ad));
        s16x4 hi = __builtin_amdgcn_ds_read_tr16_b64_v4i16((LAS s16x4*)(sb + ad + 4 * 192));
        return __builtin_bit_cast(bf16x8, __builtin_shufflevector(lo, hi, 0, 1, 2, 3, 4, 5, 6, 7));
      };
      auto loadf = [&](Frag& F, unsigned boff, int kk) {
        const char* sb = smem + boff;
#pragma unroll
        for (int tt = 0; tt < 4; ++tt) F.x[tt] = *(const bf16x8*)(sb + (kk ? xo1 : xo0) + tt * 2048);
#pragma unroll
        for (int ft = 0; ft < 6; ++ft) F.w[ft] = rdw(sb, ft, kk);
      };
      auto mma = [&](Frag& F) {
#pragma unroll
        for (int m = 0; m < 24; ++m)
          acc[m >> 2][m & 3] = __builtin_amdgcn_mfma_f32_16x16x32_bf16(F.w[m >> 2], F.x[m & 3], acc[m >> 2][m & 3], 0, 0, 0);
      };
      auto step = [&](Frag& C, Frag& N, unsigned boff, const int kk) {
        const char* sb = smem + boff;
#pragma unroll
        for (int m = 0; m < 24; ++m) {
          SCHED;
          acc[m >> 2][m & 3] = __builtin_amdgcn_mfma_f32_16x16x32_bf16(C.w[m >> 2], C.x[m & 3], acc[m >> 2][m & 3], 0, 0, 0);
          SCHED;
          if (m < 4) N.x[m] = *(const bf16x8*)(sb + (kk ? xo1 : xo0) + m * 2048);
          if (m >= 4 && m < 10) N.w[m - 4] = rdw(sb, m - 4, kk);
        }
        SCHED;
      };
      auto init = [&]() {
#pragma unroll
        for (int ft = 0; ft < 6; ++ft) {
          f32x4 bv = *(const f32x4*)(smem + SP_B1 + (16 * ft + 4 * g) * 4);
#pragma unroll
          for (int tt = 0; tt < 4; ++tt) acc[ft][tt] = bv;
        }
      };
      P1_PIPE(loadf, step, mma, init());
      STAMP(3);
      tlo = misc[M_TAG]; thi = misc[M_TAG + 1];
      lds_wait_ge(misc + M_P1D, 4u);
#pragma unroll
      for (int fp = 0; fp < 3; ++fp) {
        char* sp = smem + th * SLOT + fp * 2048 + lane * 16;
        *(f32x4*)sp = acc[2 * fp][3]; *(f32x4*)(sp + 1024) = acc[2 * fp + 1][3];
      }
      asm volatile("s_waitcnt lgkmcnt(0)" ::: "memory");
      if (lane == 0) misc[M_OFF + th] = 1u;
      lds_wait_ge(misc + M_GRDY, 2u);
#pragma unroll
      for (int tt = 0; tt < 3; ++tt) {
        const float og = 1.f - 0.5f * *(const float*)(smem + SP_G + (64 * th + 16 * tt + r16) * 4);
#pragma unroll
        for (int fp = 0; fp < 3; ++fp)
          st128(rW, OFF_AF + (((rc * 4 + cg) * 22 + 3 * jl + fp) * 8 + 4 * th + tt) * 1024 + lane * 16,
                mish_unit(acc[2 * fp][tt], acc[2 * fp + 1][tt], og));
      }
      STAMP(4);
      WAITV(0);
      if (lds_inc(smem, M_ACT, lane) == 2u && lane == 0) set_flag(rW, OFF_FA + (rc * 4 + cg) * 128 + jl * 8, tlo, thi);
      STAMP(5);
    } else {
      f32x4 acc[4][5], gac[4];
      const unsigned bo0 = ST_BM + (40 * (r16 & 1) + (r16 >> 1)) * 128 + ((g ^ ((((r16 >> 2) & 3) << 1) | (r16 & 1))) * 16);
      const unsigned bo1 = bo0 ^ 64;
      struct Frag { bf16x8 x[4], w[5], gf; };
      Frag F0, F1;
      int kc = 0;
      auto loadf = [&](Frag& F, unsigned boff, int kk) {
        const char* sb = smem + boff;
#pragma unroll
        for (int tt = 0; tt < 4; ++tt) F.x[tt] = *(const bf16x8*)(sb + (kk ? xo1 : xo0) + tt * 2048);
#pragma unroll
        for (int nt = 0; nt < 5; ++nt) F.w[nt] = *(const bf16x8*)(sb + (kk ? bo1 : bo0) + nt * 1024);
        F.gf = *(const bf16x8*)(smem + SP_GW + kc * 64 + g * 16);
        ++kc;
      };
      auto mma = [&](Frag& F) {
#pragma unroll
        for (int tt = 0; tt < 4; ++tt) gac[tt] = __builtin_amdgcn_mfma_f32_16x16x32_bf16(F.x[tt], F.gf, gac[tt], 0, 0, 0);
#pragma unroll
        for (int m = 0; m < 20; ++m)
          acc[m & 3][m >> 2] = __builtin_amdgcn_mfma_f32_16x16x32_bf16(F.x[m & 3], F.w[m >> 2], acc[m & 3][m >> 2], 0, 0, 0);
      };
      auto step = [&](Frag& C, Frag& N, unsigned boff, const int kk) {
        const char* sb = smem + boff;
#pragma unroll
        for (int m = 0; m < 24; ++m) {
          SCHED;
          if (m < 20) acc[m & 3][m >> 2] = __builtin_amdgcn_mfma_f32_16x16x32_bf16(C.x[m & 3], C.w[m >> 2], acc[m & 3][m >> 2], 0, 0, 0);
          else gac[m & 3] = __builtin_amdgcn_mfma_f32_16x16x32_bf16(C.x[m & 3], C.gf, gac[m & 3], 0, 0, 0);
          SCHED;
          if (m < 4) N.x[m] = *(const bf16x8*)(sb + (kk ? xo1 : xo0) + m * 2048);
          if (m >= 4 && m < 9) N.w[m - 4] = *(const bf16x8*)(sb + (kk ? bo1 : bo0) + (m - 4) * 1024);
          if (m == 9) N.gf = *(const bf16x8*)(smem + SP_GW + kc * 64 + g * 16);
        }
        ++kc;
        SCHED;
      };
      auto init = [&]() {
#pragma unroll
        for (int tt = 0; tt < 4; ++tt) {
          gac[tt] = (f32x4){0.f, 0.f, 0.f, 0.f};
#pragma unroll
          for (int nt = 0; nt < 5; ++nt) acc[tt][nt] = (f32x4){0.f, 0.f, 0.f, 0.f};
        }
      };
      P1_PIPE(loadf, step, mma, init());
      STAMP(3);
      tlo = misc[M_TAG]; thi = misc[M_TAG + 1];
      {
        char* bw = smem + L_BU + r16 * BUS + (64 * th + 4 * g) * 4;
#pragma unroll
        for (int tt = 0; tt < 4; ++tt)
#pragma unroll
          for (int nt = 0; nt < 5; ++nt) *(f32x4*)(bw + nt * 16 * BUS + tt * 64) = acc[tt][nt];
        f32x4 lg = r16 == 0 ? gac[0] : r16 == 1 ? gac[1] : r16 == 2 ? gac[2] : gac[3];
        f32x4 gg;
#pragma unroll
        for (int r = 0; r < 4; ++r) gg[r] = 2.f * __builtin_amdgcn_rcpf(1.f + __expf(-lg[r]));
        if (r16 < 4) *(f32x4*)(smem + SP_G + (64 * th + 16 * r16 + 4 * g) * 4) = gg;
        asm volatile("s_waitcnt lgkmcnt(0)" ::: "memory");
        lds_inc(smem, M_GRDY, lane);
        lds_inc(smem, M_BUW, lane);
      }
      STAMP(4);
      scan_task(rW, smem, misc, th, lane, rc, j, tlo, thi);
      STAMP(5);
    }

    const int wn = wave & 1, wt = wave >> 1;
    f32x4 acc[5][4];
#pragma unroll
    for (int nt = 0; nt < 5; ++nt)
#pragma unroll
      for (int tt = 0; tt < 4; ++tt) acc[nt][tt] = (f32x4){0.f, 0.f, 0.f, 0.f};
    struct Frag2 { bf16x8 x[4], w[5]; };
    Frag2 G0, G1;
    const unsigned ao = 4 * wt * 1024 + lane * 16;
    const unsigned twb = 8192 + (4 * g + q4) * 320 + (p4 >> 1) * 16 + (p4 & 1) * 8;
    const unsigned twa = twb + (5 * wn + (g & 1)) * 32;
    const unsigned twl = (wn == 1 && (g & 1)) ? twb : twa + 128;
    const int f4 = (r16 >> 2) & 3, fx = (f4 == 0 ? 0 : f4 == 1 ? 2 : f4 == 2 ? 3 : 1);
    const unsigned cwo = 8192 + (80 * wn + r16) * 64 + ((g ^ fx) * 16);
    auto rd2 = [&](Frag2& N, const char* sb, int e, const bool ssm) {
      if (e < 4) N.x[e] = *(const bf16x8*)(sb + ao + e * 1024);
      else if (ssm) N.w[e - 4] = *(const bf16x8*)(sb + cwo + (e - 4) * 1024);
      else {
        const unsigned ad = e - 4 < 4 ? twa + 32 * (e - 4) : twl;
        s16x4 lo = __builtin_amdgcn_ds_read_tr16_b64_v4i16((LAS s16x4*)(sb + ad));
        s16x4 hi = __builtin_amdgcn_ds_read_tr16_b64_v4i16((LAS s16x4*)(sb + ad + 16 * 320));
        N.w[e - 4] = __builtin_bit_cast(bf16x8, __builtin_shufflevector(lo, hi, 0, 1, 2, 3, 4, 5, 6, 7));
      }
    };
    auto wait_full = [&](int st) { lds_wait_ge(misc + M_FULL + st, 1u); };
    int slot = 0;
    unsigned fpre = 0;
    auto step2 = [&](Frag2& C, Frag2& N, int n, const bool next_ssm, const bool last) {
      if (!last && fpre == 0u) wait_full(n + 1);
      asm volatile("s_waitcnt lgkmcnt(0)" ::: "memory");
      const char* sb = smem + slot * SLOT;
      slot = slot == NSLOT - 1 ? 0 : slot + 1;
#pragma unroll
      for (int m = 0; m < 20; ++m) {
        SCHED;
        acc[m >> 2][m & 3] = __builtin_amdgcn_mfma_f32_16x16x32_bf16(C.w[m >> 2], C.x[m & 3], acc[m >> 2][m & 3], 0, 0, 0);
        SCHED;
        if (!last && m < 9) rd2(N, sb, m, next_ssm);
        if (m == 9) { if (lane == 0) misc[M_CONS + wave] = n + 1; }
        if (m == 12) fpre = misc[M_FULL + n + 2];
      }
      SCHED;
    };
    wait_full(0);
    __builtin_amdgcn_s_setprio(1);
    STAMP(6);
#pragma unroll
    for (int e = 0; e < 9; ++e) rd2(G0, smem, e, false);
    slot = 1;
#pragma unroll 1
    for (int n = 0; n < 10; n += 2) { step2(G0, G1, n, false, false); step2(G1, G0, n + 1, false, false); }
    STAMP(7);
    step2(G0, G1, 10, false, false); step2(G1, G0, 11, true, false);
    STAMP(8);
#pragma unroll 1
    for (int n = 12; n < 20; n += 2) { step2(G0, G1, n, true, false); step2(G1, G0, n + 1, true, false); }
    step2(G0, G1, 20, true, false); step2(G1, G0, 21, true, true);
    __builtin_amdgcn_s_setprio(0);
    STAMP(9);
#pragma unroll
    for (int t2 = 0; t2 < 2; ++t2) {
      const int owner = 2 * wt + t2;
      if (owner != cg) {
        const unsigned pb = OFF_PB + (((rc * 4 + jl) * 4 + owner) * 4 + cg) * 10240 + wn * 5120 + lane * 16;
#pragma unroll
        for (int nt = 0; nt < 5; ++nt) {
          const f32x4 v0 = acc[nt][2 * t2], v1 = acc[nt][2 * t2 + 1];
          u32x4 o; o[0] = pack2(v0[0], v0[1]); o[1] = pack2(v0[2], v0[3]); o[2] = pack2(v1[0], v1[1]); o[3] = pack2(v1[2], v1[3]);
          st128(rW, pb + nt * 1024, o);
        }
      }
    }
    lds_wait_ge(misc + M_CONS, 22u); lds_wait_ge(misc + M_CONS + 1, 22u); lds_wait_ge(misc + M_CONS + 2, 22u); lds_wait_ge(misc + M_CONS + 3, 22u);
#pragma unroll
    for (int tt = 0; tt < 4; ++tt) {
      if (2 * wt + (tt >> 1) == cg) {
#pragma unroll
        for (int nt = 0; nt < 5; ++nt) *(f32x4*)(smem + (5 * (2 * wn + (tt & 1)) + nt) * 1024 + lane * 16) = acc[nt][tt];
      }
    }
    STAMP(10);
    asm volatile("s_waitcnt vmcnt(0) lgkmcnt(0)" ::: "memory");
    if (lds_inc(smem, M_RED + 1, lane) == 3u) {
      if (lane == 0) { set_flag(rW, OFF_FR + (rc * 4 + jl) * 128 + cg * 8, tlo, thi); misc[M_RED] = 4u; }
    }
    STAMP(11);
  }
  STAMP(25);
  STAMP(26);
  STAMP_OUT(0);
}

extern "C" void kernel_launch(void* const* d_in, const int* in_sizes, int n_in,
                              void* d_out, int out_size, void* d_ws, size_t ws_size,
                              hipStream_t stream) {
  Args a;
  a.x   = (const u16*)d_in[0];
  a.Lre = (const u16*)d_in[1];
  a.Lim = (const u16*)d_in[2];
  a.Bre = (const u16*)d_in[3];
  a.Bim = (const u16*)d_in[4];
  a.Cre = (const u16*)d_in[5];
  a.Cim = (const u16*)d_in[6];
  a.ldt = (const u16*)d_in[7];
  a.Dsk = (const u16*)d_in[8];
  a.w1  = (const u16*)d_in[10];
  a.b1  = (const u16*)d_in[11];
  a.w2  = (const u16*)d_in[12];
  a.gw  = (const u16*)d_in[13];
  a.ws  = (char*)d_ws;
  a.out = (float*)d_out;
  fused<<<256, 512, 0, stream>>>(a);
}
```

```cpp
#include <hip/hip_runtime.h>
#include <hip/hip_bf16.h>
#include <stdint.h>
#pragma clang diagnostic ignored "-Winline-asm"

typedef __attribute__((ext_vector_type(8))) __bf16 bf16x8;
typedef __attribute__((ext_vector_type(2))) __bf16 bf16x2;
typedef __attribute__((ext_vector_type(4))) float f32x4;
typedef __attribute__((ext_vector_type(2))) float f32x2;
typedef __attribute__((ext_vector_type(4))) unsigned u32x4;
typedef __attribute__((ext_vector_type(2))) unsigned u32x2;
typedef __attribute__((ext_vector_type(4))) short s16x4;
typedef unsigned short u16;
#define LAS __attribute__((address_space(3)))
#define GAS __attribute__((address_space(1)))
#define DI __device__ __forceinline__

constexpr int T_ = 2048, D_ = 640, P_ = 640, F_ = 1536;

constexpr unsigned OFF_AF = 0;
constexpr unsigned OFF_PB = OFF_AF + 64u * 22 * 8192;
constexpr unsigned OFF_E  = OFF_PB + 64u * 16 * 10240;
constexpr unsigned OFF_FA = OFF_E + 256u * 640;
constexpr unsigned OFF_FE = OFF_FA + 64 * 128;
constexpr unsigned OFF_FR = OFF_FE + 16 * 128;
constexpr unsigned OFF_EPOCH = OFF_FR + 64 * 128;
constexpr unsigned WS_BYTES = OFF_EPOCH + 256;

#define STAMP(i)
#define STAMP_DECL
#define STAMP_OUT(kid)

DI float bflo(unsigned v) { return __uint_as_float(v << 16); }
DI float bfhi(unsigned v) { return __uint_as_float(v & 0xffff0000u); }
DI unsigned pack2(float a, float b) {
  bf16x2 r; r[0] = (__bf16)a; r[1] = (__bf16)b; return __builtin_bit_cast(unsigned, r);
}
DI float dswap(float v) {
  return __int_as_float(__builtin_amdgcn_mov_dpp(__float_as_int(v), 0xB1, 0xf, 0xf, true));
}
DI float cmul(float lr, float ls, float v) { return fmaf(lr, v, ls * dswap(v)); }
DI float cmuladd(float lr, float ls, float v, float a) { return fmaf(lr, v, fmaf(ls, dswap(v), a)); }
DI f32x2 cm2(f32x2 a, f32x2 b) { return (f32x2){a[0] * b[0] - a[1] * b[1], a[0] * b[1] + a[1] * b[0]}; }
DI float sfma(float a, float b, float c) { float d; asm("v_fma_f32 %0, %1, %2, %3" : "=v"(d) : "v"(a), "v"(b), "v"(c)); return d; }
DI float smul(float a, float b) { float d; asm("v_mul_f32 %0, %1, %2" : "=v"(d) : "v"(a), "v"(b)); return d; }

DI f32x2 mish2(f32x2 h) {
  f32x2 z = h * 1.44269504088896341f;
  f32x2 w; w[0] = __builtin_amdgcn_exp2f(fminf(z[0], 43.f)); w[1] = __builtin_amdgcn_exp2f(fminf(z[1], 43.f));
  f32x2 n = w * (w + 2.f);
  f32x2 d = n + 2.f;
  f32x2 r; r[0] = __builtin_amdgcn_rcpf(d[0]); r[1] = __builtin_amdgcn_rcpf(d[1]);
  return (h * n) * r;
}

DI void glds16(const void* gsrc, unsigned lds_dst) {
  unsigned keep;
  asm volatile("s_mov_b32 %0, m0\n\ts_mov_b32 m0, %2\n\ts_nop 0\n\tglobal_load_lds_dwordx4 %1, off\n\ts_mov_b32 m0, %0"
               : "=&s"(keep) : "v"(gsrc), "s"(lds_dst) : "memory");
}
DI void touch(const void* gsrc, unsigned lds_dst) {
  unsigned keep;
  asm volatile("s_mov_b32 %0, m0\n\ts_mov_b32 m0, %2\n\ts_nop 0\n\tglobal_load_lds_dword %1, off\n\ts_mov_b32 m0, %0"
               : "=&s"(keep) : "v"(gsrc), "s"(lds_dst) : "memory");
}
DI float sum16(float v) {
  auto r = __builtin_amdgcn_permlane16_swap(__float_as_uint(v), __float_as_uint(v), false, false);
  return __uint_as_float(r[0]) + __uint_as_float(r[1]);
}
DI float sum32(float v) {
  auto r = __builtin_amdgcn_permlane32_swap(__float_as_uint(v), __float_as_uint(v), false, false);
  return __uint_as_float(r[0]) + __uint_as_float(r[1]);
}

typedef unsigned long long u64;
#define SC1 16
#define SCHED __builtin_amdgcn_sched_barrier(0)
#define RSRC __amdgpu_buffer_rsrc_t
DI void st128(RSRC r, unsigned off, u32x4 v) { __builtin_amdgcn_raw_buffer_store_b128(v, r, off, 0, SC1); }
DI void st128f(RSRC r, unsigned off, f32x4 v) { __builtin_amdgcn_raw_buffer_store_b128(__builtin_bit_cast(u32x4, v), r, off, 0, SC1); }
DI void st64(RSRC r, unsigned off, u32x2 v) { __builtin_amdgcn_raw_buffer_store_b64(v, r, off, 0, SC1); }
DI f32x4 ld128f(RSRC r, unsigned off) { return __builtin_bit_cast(f32x4, __builtin_amdgcn_raw_buffer_load_b128(r, off, 0, SC1)); }
DI f32x2 ld64f(RSRC r, unsigned off) { return __builtin_bit_cast(f32x2, __builtin_amdgcn_raw_buffer_load_b64(r, off, 0, SC1)); }
DI void glds16c(const void* gsrc, unsigned lds_dst) {
  unsigned keep;
  asm volatile("s_mov_b32 %0, m0\n\ts_mov_b32 m0, %2\n\ts_nop 0\n\tglobal_load_lds_dwordx4 %1, off sc1\n\ts_mov_b32 m0, %0"
               : "=&s"(keep) : "v"(gsrc), "s"(lds_dst) : "memory");
}

DI void gldss(unsigned voff, const void* sbase_, unsigned lds_dst) {
  asm volatile("s_mov_b32 m0, %2\n\ts_nop 0\n\tglobal_load_lds_dwordx4 %0, %1" :: "v"(voff), "s"(sbase_), "s"(lds_dst) : "memory", "m0");
}
DI void gldssc(unsigned voff, const void* sbase_, unsigned lds_dst) {
  asm volatile("s_mov_b32 m0, %2\n\ts_nop 0\n\tglobal_load_lds_dwordx4 %0, %1 sc1" :: "v"(voff), "s"(sbase_), "s"(lds_dst) : "memory", "m0");
}

constexpr int ST_X = 0, ST_BM = 16384, ST_W1 = 26624, ST_SZ = 38912, NSTG = 4;
constexpr int SP = NSTG * ST_SZ;
constexpr int PARS = 96;
constexpr int SP_PAR = SP, SP_B1 = SP + 3840, SP_EC = SP + 4224, SP_EL = SP + 5504, SP_GW = SP + 5824, SP_G = SP + 7104,
              SP_MISC = SP + 7616, LDS_BYTES = SP + 7872;
constexpr int BUS = 528, L_BU = SP - 80 * BUS;
constexpr int HIS = 176, L_HI = SP - 128 * HIS;
constexpr int SLOT = 18432, NSLOT = 7;
enum { M_CNTE = 0, M_GRDY, M_P1D, M_ACT, M_BURDY, M_SRD, M_SWR, M_PUB, M_RED, M_NEXT = 10, M_CONS = 12, M_OFF = 16, M_HOK = 18, M_BUW = 19, M_TAG = 24, M_FULL = 32 };
constexpr int SPIN_LDS = 1 << 22, SPIN_G = 1 << 19;

struct Args {
  const u16 *x, *Lre, *Lim, *Bre, *Bim, *Cre, *Cim, *ldt, *Dsk, *w1, *b1, *w2, *gw;
  char* ws;
  float* out;
};

#define CBAR do { SCHED; asm volatile("s_waitcnt lgkmcnt(0)\n\ts_barrier" ::: "memory"); SCHED; } while (0)
#define LBAR(n) asm volatile("s_waitcnt vmcnt(" #n ")\n\ts_barrier" ::: "memory")
#define WAITV(n) asm volatile("s_waitcnt vmcnt(" #n ")" ::: "memory")

DI void lds_wait_ge(volatile LAS unsigned* w, unsigned v) {
  for (int sp = 0; *w < v && sp < SPIN_LDS; ++sp) __builtin_amdgcn_s_sleep(1);
  asm volatile("" ::: "memory");
}
DI unsigned lds_inc(char* smem, int word, int lane) {
  unsigned old = 0;
  asm volatile("; wave converged" ::: "memory");
  if (lane == 0) old = __hip_atomic_fetch_add((LAS unsigned*)(smem + SP_MISC) + word, 1u, __ATOMIC_RELAXED, __HIP_MEMORY_SCOPE_WORKGROUP);
  asm volatile("; wave converged" : "+v"(old) :: "memory");
  return __builtin_amdgcn_readfirstlane(old);
}
DI void poll_flags(RSRC rW, unsigned off, int n, unsigned tlo, unsigned thi, int lane) {
  const unsigned o = off + (lane < n ? lane : 0) * 8;
  for (int sp = 0; n > 0 && sp < SPIN_G; ++sp) {
    const u32x2 f = __builtin_amdgcn_raw_buffer_load_b64(rW, o, 0, SC1);
    if (__all(f[0] == tlo && f[1] == thi)) break;
    __builtin_amdgcn_s_sleep(1);
    asm volatile("" ::: "memory");
  }
  asm volatile("" ::: "memory");
}
DI void set_flag(RSRC rW, unsigned off, unsigned tlo, unsigned thi) { u32x2 t; t[0] = tlo; t[1] = thi; st64(rW, off, t); }

DI u32x4 mish_unit(f32x4 a0, f32x4 a1, float og) {
  f32x2 m0 = mish2((f32x2){a0[0], a0[1]}) * og, m1 = mish2((f32x2){a0[2], a0[3]}) * og;
  f32x2 m2 = mish2((f32x2){a1[0], a1[1]}) * og, m3 = mish2((f32x2){a1[2], a1[3]}) * og;
  u32x4 o;
  o[0] = pack2(m0[0], m0[1]); o[1] = pack2(m1[0], m1[1]); o[2] = pack2(m2[0], m2[1]); o[3] = pack2(m3[0], m3[1]);
  return o;
}

DI void scan_task(RSRC rW, char* smem, volatile LAS unsigned* misc, int u, int lane, int rc, int cg, int jl, unsigned tlo, unsigned thi) {
  const int j = 4 * cg + jl;
  int L = 64 * u + lane; const bool act = L < 160; L = act ? L : 159;
  const int seg = L / 40, chl = L - 40 * seg;
  const float* pp = (const float*)(smem + SP_PAR + chl * PARS);
  const f32x2 l1 = *(const f32x2*)pp, l128 = *(const f32x2*)(pp + 20);
  const f32x4 cl = *(const f32x4*)(pp + 4);
  f32x2 pw = seg == 1 ? (f32x2){cl[2], cl[3]} : *(const f32x2*)(pp + 16 + (seg == 3 ? 2 : 0));
  if (seg == 0) pw = (f32x2){1.f, 0.f};
  const float lr = l1[0], li = l1[1], nli = -l1[1];
  lds_wait_ge(misc + M_BUW, 2u);
  __builtin_amdgcn_s_setprio(3);
  f32x4 ar[8], ai[8];
  {
    const char* bp = smem + L_BU + 2 * chl * BUS + seg * 128;
#pragma unroll
    for (int i = 0; i < 8; ++i) { ar[i] = *(const f32x4*)(bp + i * 16); ai[i] = *(const f32x4*)(bp + BUS + i * 16); }
  }
  float sr = 0.f, si = 0.f;
#pragma unroll
  for (int t = 0; t < 32; ++t) {
    const float tr = fmaf(nli, si, ar[t >> 2][t & 3]), ti = fmaf(li, sr, ai[t >> 2][t & 3]);
    sr = fmaf(lr, sr, tr); si = fmaf(lr, si, ti);
  }
  if (act) *(f32x2*)(smem + SP_EC + (seg * 40 + chl) * 8) = (f32x2){sr, si};
  asm volatile("s_waitcnt lgkmcnt(0)" ::: "memory");
  lds_inc(smem, M_SRD, lane);
  lds_wait_ge(misc + M_SRD, 3u);
  float qr = 0.f, qi = 0.f;
#pragma unroll
  for (int i = 0; i < 3; ++i) {
    const f32x2 e = *(const f32x2*)(smem + SP_EC + (i * 40 + chl) * 8);
    const float tr = fmaf(-cl[3], qi, e[0]), ti = fmaf(cl[3], qr, e[1]);
    const float nr = fmaf(cl[2], qr, tr), ni = fmaf(cl[2], qi, ti);
    qr = i < seg ? nr : qr; qi = i < seg ? ni : qi;
  }
  if (act && seg == 3) {
    const float er = fmaf(cl[2], qr, fmaf(-cl[3], qi, sr)), ei = fmaf(cl[2], qi, fmaf(cl[3], qr, si));
    st128(rW, OFF_E + ((j * 16 + rc) * 40 + chl) * 16, (u32x4){__float_as_uint(er), __float_as_uint(ei), tlo, thi});
  }
  __builtin_amdgcn_s_setprio(0);
#pragma unroll
  for (int i = 0; i < 8; ++i)
#pragma unroll
    for (int r = 0; r < 4; ++r) {
      const float xr = fmaf(cl[0], ar[i][r], -cl[1] * ai[i][r]), xi = fmaf(cl[0], ai[i][r], cl[1] * ar[i][r]);
      ar[i][r] = xr; ai[i][r] = xi;
    }
  lds_wait_ge(misc + M_GRDY, 2u);
  f32x4 gv[8];
#pragma unroll
  for (int i = 0; i < 8; ++i) gv[i] = *(const f32x4*)(smem + SP_G + seg * 128 + i * 16);
  {
    u32x4 ev[15];
    for (int sp = 0; sp < SPIN_G; ++sp) {
      bool ok = true;
#pragma unroll
      for (int r = 0; r < 15; ++r) {
        ev[r] = (u32x4){0u, 0u, tlo, thi};
        if (r < rc) ev[r] = __builtin_amdgcn_raw_buffer_load_b128(rW, OFF_E + ((j * 16 + r) * 40 + chl) * 16, 0, SC1);
      }
#pragma unroll
      for (int r = 0; r < 15; ++r) ok = ok && ev[r][2] == tlo && ev[r][3] == thi;
      if (__all(ok)) break;
      __builtin_amdgcn_s_sleep(1);
      asm volatile("" ::: "memory");
    }
    __builtin_amdgcn_s_setprio(3);
    float cr = 0.f, ci = 0.f;
    const float er = l128[0], ei = l128[1], nei = -l128[1];
#pragma unroll
    for (int r = 0; r < 15; ++r)
      if (r < rc) {
        const float tr = sfma(nei, ci, __uint_as_float(ev[r][0])), ti = sfma(ei, cr, __uint_as_float(ev[r][1]));
        cr = sfma(er, cr, tr); ci = sfma(er, ci, ti);
      }
    const float tr = fmaf(-pw[1], ci, qr), ti = fmaf(pw[1], cr, qi);
    cr = fmaf(pw[0], cr, tr); ci = fmaf(pw[0], ci, ti);
    sr = fmaf(cl[0], cr, -cl[1] * ci); si = fmaf(cl[0], ci, cl[1] * cr);
  }
  char* wp = smem + L_HI + seg * 32 * HIS + chl * 2;
#pragma unroll
  for (int t = 0; t < 32; ++t) {
    const float tr = sfma(nli, si, ar[t >> 2][t & 3]), ti = sfma(li, sr, ai[t >> 2][t & 3]);
    sr = sfma(lr, sr, tr); si = sfma(lr, si, ti);
    const float g2 = gv[t >> 2][t & 3];
    const unsigned pk = pack2(smul(sr, g2), smul(-si, g2));
    *(u16*)(wp + t * HIS) = (u16)pk;
    *(u16*)(wp + t * HIS + 80) = (u16)(pk >> 16);
  }
  asm volatile("s_waitcnt lgkmcnt(0)" ::: "memory");
  lds_inc(smem, M_SWR, lane);
  lds_wait_ge(misc + M_SWR, 3u);
#pragma unroll
  for (int k = 0; k < 7; ++k) {
    const int f = u + 3 * k;
    if (f < 20) {
      const int pe = f >> 1, part = pe / 5, e = pe % 5;
      const int t = 64 * (f & 1) + lane, pi = 5 * jl + e, step = 12 + 2 * (pi >> 2) + part;
      const u32x4 v = *(const u32x4*)(smem + L_HI + t * HIS + part * 80 + e * 16);
      st128(rW, OFF_AF + (((rc * 4 + cg) * 22 + step) * 8 + (t >> 4)) * 1024 + ((pi & 3) * 16 + (t & 15)) * 16, v);
    }
  }
  WAITV(0);
  if (lds_inc(smem, M_PUB, lane) == 2u) {
    if (lane == 0) set_flag(rW, OFF_FA + (rc * 4 + cg) * 128 + 32 + jl * 8, tlo, thi);
    poll_flags(rW, OFF_FA + (rc * 4 + cg) * 128 + 32, 4, tlo, thi, lane);
    if (lane == 0) misc[M_HOK] = 1u;
  }
  __builtin_amdgcn_s_setprio(0);
}

__global__ __launch_bounds__(512, 2) void fused(Args a) {
  __shared__ __attribute__((aligned(16))) char smem[LDS_BYTES];
  const int tid = threadIdx.x, lane = tid & 63;
  const int wave = __builtin_amdgcn_readfirstlane(tid >> 6);
  const int b = blockIdx.x, grp = b & 7, idx = b >> 3;
  const int cg = grp & 3, jl = idx & 3;
  const int rc = 8 * (grp >> 2) + (idx >> 2), j = 4 * cg + jl;
  const int r16 = lane & 15, g = lane >> 4;
  char* ws = a.ws;
  const RSRC rW = __builtin_amdgcn_make_buffer_rsrc((void*)ws, 0, WS_BYTES, 0x00020000);
  volatile LAS unsigned* misc = (volatile LAS unsigned*)(smem + SP_MISC);
  const unsigned sbase = (unsigned)(size_t)(LAS char*)smem;
  STAMP_DECL
  STAMP(0);

  if (wave >= 4) {
    const int lw = wave - 4;
    {
      unsigned voff[10]; const char* sb_[10]; int inc[10]; unsigned dsto[10];
#pragma unroll
      for (int i = 0; i < 10; ++i) {
        int p = lw + 4 * i; p = p >= 38 ? p - 38 : p;
        dsto[i] = sbase + p * 1024;
        const int rw = lane >> 3;
        const unsigned ox = (8 * p + rw) * (D_ * 2) + 16 * ((lane & 7) ^ (((8 * p + rw) >> 1) & 7));
        const int pb = p < 21 ? p - 16 : p - 21;
        const unsigned ob = (8 * pb + rw) * (D_ * 2) + 16 * ((lane & 7) ^ ((((rw >> 1) & 3) << 1) | (p >= 21 ? 1 : 0)));
        const int ci = 64 * (p >= 26 ? p - 26 : 0) + lane, k = ci / 12, cp = ci % 12;
        int cc = cp - 2 * ((k >> 3) & 1); cc = cc < 0 ? cc + 12 : cc;
        const unsigned ow = k * (F_ * 2) + 16 * cc;
        voff[i] = p < 16 ? ox : p < 26 ? ob : ow;
        inc[i] = p < 26 ? 128 : 64 * F_ * 2;
        sb_[i] = (const char*)(p < 16 ? a.x + (size_t)128 * rc * D_ : p < 21 ? a.Bre + (size_t)40 * j * D_ : p < 26 ? a.Bim + (size_t)40 * j * D_ : a.w1 + 96 * j);
      }
      auto stage = [&](unsigned boff) {
#pragma unroll
        for (int i = 0; i < 10; ++i) { gldss(voff[i], sb_[i], dsto[i] + boff); sb_[i] += inc[i]; }
      };
      stage(0); stage(ST_SZ);
      STAMP(1);
      LBAR(10);
      STAMP(2);
      stage(2 * ST_SZ);
      LBAR(10);
      stage(3 * ST_SZ); stage(0);
#pragma unroll 1
      for (int s = 2; s <= 6; ++s) {
        LBAR(20);
        stage(((s + 3) & 3) * ST_SZ);
      }
      LBAR(20); LBAR(10); LBAR(0);
    }
    STAMP(3);
    const unsigned tlo = misc[M_TAG], thi = misc[M_TAG + 1];

    unsigned woff[10], coff[10];
#pragma unroll
    for (int i = 0; i < 10; ++i) {
      const int ci = 64 * i + lane;
      const int k = ci / 20, cp = ci % 20;
      int cc = cp - 2 * ((k >> 2) & 1); cc = cc < 0 ? cc + 20 : cc;
      woff[i] = k * (D_ * 2) + 16 * cc;
      const int row = ci >> 2, f4 = (row >> 2) & 3, fx = (f4 == 0 ? 0 : f4 == 1 ? 2 : f4 == 2 ? 3 : 1);
      coff[i] = row * (P_ * 2) + 16 * ((ci & 3) ^ fx);
    }
    const char* wbase2 = (const char*)(a.w2 + (size_t)384 * cg * D_ + 160 * jl);
    const char* abase = ws + OFF_AF + (size_t)(rc * 4 + cg) * 22 * 8192;
    const unsigned aoff = lane * 16;
    auto issueB = [&](int n, unsigned sl) {
      if (n < 12) {
        const char* p = wbase2 + (size_t)n * (32 * D_ * 2);
#pragma unroll
        for (int i = 0; i < 10; ++i) gldss(woff[i], p, sl + 8192 + i * 1024);
      } else {
        const char* p = (const char*)(((n & 1) ? a.Cim : a.Cre) + (size_t)160 * jl * P_ + 160 * cg) + ((n - 12) >> 1) * 64;
#pragma unroll
        for (int i = 0; i < 10; ++i) gldss(coff[i], p, sl + 8192 + i * 1024);
      }
    };
    auto issueA = [&](int n, unsigned sl) {
#pragma unroll
      for (int i = 0; i < 8; ++i) gldssc(aoff, abase + (size_t)n * 8192 + i * 1024, sl + i * 1024);
    };
    if (lw != 1) {
      scan_task(rW, smem, misc, lw == 0 ? 2 : lw - 2, lane, rc, cg, jl, tlo, thi);
      lds_wait_ge(misc + M_FULL, 1u);
      STAMP(6);
    } else {
      lds_wait_ge(misc + M_P1D, 4u);
#pragma unroll 1
      for (int k = 0; k < 6; ++k) issueB(k, sbase + k * SLOT);
      lds_wait_ge(misc + M_SRD, 3u);
      WAITV(50);
      issueB(6, sbase + 6 * SLOT);
      STAMP(4);
      poll_flags(rW, OFF_FA + (rc * 4 + cg) * 128, 4, tlo, thi, lane);
      STAMP(5);
      issueA(0, sbase); issueA(1, sbase + SLOT); issueA(2, sbase + 2 * SLOT);
#pragma unroll 1
      for (int k = 3; k < 7; ++k) {
        issueA(k, sbase + k * SLOT);
        WAITV(24); if (lane == 0) misc[M_FULL + k - 3] = 1u;
      }
      WAITV(16); if (lane == 0) misc[M_FULL + 4] = 1u;
      WAITV(8);  if (lane == 0) misc[M_FULL + 5] = 1u;
      WAITV(0);  if (lane == 0) misc[M_FULL + 6] = 1u;
    }
#pragma unroll 1
    for (;;) {
      const int n = (int)lds_inc(smem, M_NEXT, lane);
      if (n >= 22) break;
      const unsigned need = n - NSLOT + 1;
      for (int sp = 0; sp < SPIN_LDS; ++sp) {
        if (misc[M_CONS] >= need && misc[M_CONS + 1] >= need && misc[M_CONS + 2] >= need && misc[M_CONS + 3] >= need) break;
        __builtin_amdgcn_s_sleep(1);
      }
      asm volatile("" ::: "memory");
      const unsigned sl = sbase + (n % NSLOT) * SLOT;
      issueB(n, sl);
      if (n >= 12) lds_wait_ge(misc + M_HOK, 1u);
      issueA(n, sl);
      WAITV(0);
      if (lane == 0) misc[M_FULL + n] = 1u;
    }
    STAMP(8);
    const int tl = 16 * (lw & 1) + r16, n0 = 160 * jl + 80 * (lw >> 1) + 4 * g;
    const size_t orow = (size_t)(128 * rc + 32 * cg + tl) * D_ + n0;
    f32x4 sk[5];
    {
      const float gt = 0.5f * *(const float*)(smem + SP_G + (32 * cg + tl) * 4);
#pragma unroll
      for (int i = 0; i < 5; ++i) {
        u32x2 xv = *(const u32x2*)(a.x + orow + 16 * i);
        u32x2 dv = *(const u32x2*)(a.Dsk + n0 + 16 * i);
        sk[i][0] = gt * bflo(xv[0]) * bflo(dv[0]); sk[i][1] = gt * bfhi(xv[0]) * bfhi(dv[0]);
        sk[i][2] = gt * bflo(xv[1]) * bflo(dv[1]); sk[i][3] = gt * bfhi(xv[1]) * bfhi(dv[1]);
      }
    }
    STAMP(22);
    for (int sp = 0; misc[M_RED] < 4u && sp < SPIN_LDS; ++sp) __builtin_amdgcn_s_sleep(4);
    asm volatile("" ::: "memory");
    STAMP(23);
    poll_flags(rW, OFF_FR + (rc * 4 + jl) * 128, 4, tlo, thi, lane);
    STAMP(24);
    if (rc == 15 && j == 0 && tid == 256) set_flag(rW, OFF_EPOCH, tlo, thi);
    u32x2 pv[3][5];
#pragma unroll
    for (int s = 0; s < 3; ++s) {
      const int sc = (cg + 1 + s) & 3;
#pragma unroll
      for (int i = 0; i < 5; ++i)
        pv[s][i] = __builtin_amdgcn_raw_buffer_load_b64(rW, OFF_PB + (((rc * 4 + jl) * 4 + cg) * 4 + sc) * 10240 + (lw >> 1) * 5120 + i * 1024 + lane * 16 + (lw & 1) * 8, 0, SC1);
    }
#pragma unroll
    for (int i = 0; i < 5; ++i) {
      f32x4 v = *(const f32x4*)(smem + (5 * lw + i) * 1024 + lane * 16) + sk[i];
#pragma unroll
      for (int s = 0; s < 3; ++s) v += (f32x4){bflo(pv[s][i][0]), bfhi(pv[s][i][0]), bflo(pv[s][i][1]), bfhi(pv[s][i][1])};
      *(f32x4*)(a.out + orow + 16 * i) = v;
    }
  } else {
    if (wave == 3) {
      unsigned long long pc; unsigned back;
      asm volatile("s_getpc_b64 %0\n1:\n\ts_mov_b32 %1, 1b-_Z5fused4Args" : "=s"(pc), "=s"(back));
      touch((const char*)(pc - back) + (idx * 12 + (lane < 12 ? lane : 0)) * 128, sbase + SP_EL);
      touch((const char*)a.w1 + (size_t)(b * 60 + (lane < 60 ? lane : 0)) * 128, sbase + SP_EL);
      touch((const char*)(lane < 32 ? a.Bre : a.Bim) + (size_t)(b * 25 + ((lane & 31) < 25 ? (lane & 31) : 0)) * 128, sbase + SP_EL);
    } else if (wave == 0) {
      touch((const char*)a.x + (size_t)(b * 80 + lane) * 128, sbase + SP_EL);
      touch((const char*)a.x + (size_t)(b * 80 + 64 + r16) * 128, sbase + SP_EL);
      touch((const char*)a.w2 + (size_t)(b * 60 + (lane < 60 ? lane : 0)) * 128, sbase + SP_EL);
      touch((const char*)(lane < 32 ? a.Cre : a.Cim) + (size_t)(b * 25 + ((lane & 31) < 25 ? (lane & 31) : 0)) * 128, sbase + SP_EL);
    }
    if (wave == 1) {
      const u64 ep = __hip_atomic_load((GAS u64*)(ws + OFF_EPOCH), __ATOMIC_RELAXED, __HIP_MEMORY_SCOPE_AGENT);
      const unsigned v = *(const unsigned*)(a.b1 + 96 * j + 2 * (lane < 48 ? lane : 0));
      const u32x4 g0 = *(const u32x4*)(a.gw + 8 * lane), g1 = *(const u32x4*)(a.gw + 8 * (64 + r16));
      if (lane < 48) *(f32x2*)(smem + SP_B1 + lane * 8) = (f32x2){bflo(v), bfhi(v)};
      *(u32x4*)(smem + SP_GW + lane * 16) = g0;
      if (lane < 16) *(u32x4*)(smem + SP_GW + 1024 + lane * 16) = g1;
      if (lane < 24 || lane >= 32) misc[lane] = lane == M_NEXT ? 7u : 0u;
      if (lane == 0) { const u64 tg = ep + 1; misc[M_TAG] = (unsigned)tg; misc[M_TAG + 1] = (unsigned)(tg >> 32); }
    } else if (wave == 2) {
      const int ch = 40 * j + (lane < 40 ? lane : 0);
      const float lre = bflo(a.Lre[ch]), lim = bflo(a.Lim[ch]), dt = expf(bflo(a.ldt[ch]));
      float e = expf(dt * lre), sn, cs;
      sincosf(dt * lim, &sn, &cs);
      f32x2 l1 = {e * cs, e * sn};
      const float nr = l1[0] - 1.f, ni = l1[1], den = lre * lre + lim * lim;
      f32x2 cf = {(nr * lre + ni * lim) / den, (ni * lre - nr * lim) / den};
      f32x2 l2 = cm2(l1, l1), l4 = cm2(l2, l2), l8 = cm2(l4, l4), l12 = cm2(l8, l4), l16 = cm2(l8, l8);
      f32x2 l32 = cm2(l16, l16), l64 = cm2(l32, l32), l128 = cm2(l64, l64), l96 = cm2(l64, l32);
      if (lane < 40) {
        float* pp = (float*)(smem + SP_PAR + lane * PARS);
        *(f32x4*)(pp) = (f32x4){l1[0], l1[1], l16[0], l16[1]};
        *(f32x4*)(pp + 4) = (f32x4){cf[0], cf[1], l32[0], l32[1]};
        *(f32x4*)(pp + 8) = (f32x4){l12[0], l12[1], l8[0], l8[1]};
        *(f32x4*)(pp + 12) = (f32x4){l4[0], l4[1], 1.f, 0.f};
        *(f32x4*)(pp + 16) = (f32x4){l64[0], l64[1], l96[0], l96[1]};
        *(f32x2*)(pp + 20) = l128;
      }
    }
    STAMP(1);
    const int th = wave & 1;
    const unsigned xo0 = ST_X + (64 * th + r16) * 128 + ((g ^ (r16 >> 1)) * 16);
    const unsigned xo1 = xo0 ^ 64;
    const int q4 = r16 >> 2, p4 = r16 & 3;

#define P1_PIPE(LOADF, STEP, MMA, INIT)                                             \
    CBAR;                                                                           \
    __builtin_amdgcn_s_setprio(3);                                                  \
    STAMP(2);                                                                       \
    INIT;                                                                           \
    LOADF(F0, 0u, 0);                                                               \
    STEP(F0, F1, 0u, 1);                                                            \
    _Pragma("unroll 1") for (int s = 1; s < 10; ++s) {                              \
      CBAR;                                                                         \
      STEP(F1, F0, (s & 3) * ST_SZ, 0);                                             \
      STEP(F0, F1, (s & 3) * ST_SZ, 1);                                             \
    }                                                                               \
    asm volatile("s_waitcnt lgkmcnt(0)" ::: "memory");                              \
    lds_inc(smem, M_P1D, lane);                                                     \
    MMA(F1); SCHED; __builtin_amdgcn_s_setprio(0);

    unsigned tlo, thi;
    if (wave < 2) {
      f32x4 acc[6][4];
      const unsigned wbase = ST_W1 + (8 * g + q4) * 192 + (p4 >> 1) * 16 + (p4 & 1) * 8;
      const unsigned wa = wbase + (g & 1) * 32;
      const unsigned wb = wbase + ((g & 1) ? 0 : 160);
      struct Frag { bf16x8 x[4], w[6]; };
      Frag F0, F1;
      auto rdw = [&](const char* sb, int ft, int kk) {
        unsigned ad = (ft < 5 ? wa + 32 * ft : wb) + kk * 32 * 192;
        s16x4 lo = __builtin_amdgcn_ds_read_tr16_b64_v4i16((LAS s16x4*)(sb + ad));
        s16x4 hi = __builtin_amdgcn_ds_read_tr16_b64_v4i16((LAS s16x4*)(sb + ad + 4 * 192));
        return __builtin_bit_cast(bf16x8, __builtin_shufflevector(lo, hi, 0, 1, 2, 3, 4, 5, 6, 7));
      };
      auto loadf = [&](Frag& F, unsigned boff, int kk) {
        const char* sb = smem + boff;
#pragma unroll
        for (int tt = 0; tt < 4; ++tt) F.x[tt] = *(const bf16x8*)(sb + (kk ? xo1 : xo0) + tt * 2048);
#pragma unroll
        for (int ft = 0; ft < 6; ++ft) F.w[ft] = rdw(sb, ft, kk);
      };
      auto mma = [&](Frag& F) {
#pragma unroll
        for (int m = 0; m < 24; ++m)
          acc[m >> 2][m & 3] = __builtin_amdgcn_mfma_f32_16x16x32_bf16(F.w[m >> 2], F.x[m & 3], acc[m >> 2][m & 3], 0, 0, 0);
      };
      auto step = [&](Frag& C, Frag& N, unsigned boff, const int kk) {
        const char* sb = smem + boff;
#pragma unroll
        for (int m = 0; m < 24; ++m) {
          SCHED;
          acc[m >> 2][m & 3] = __builtin_amdgcn_mfma_f32_16x16x32_bf16(C.w[m >> 2], C.x[m & 3], acc[m >> 2][m & 3], 0, 0, 0);
          SCHED;
          if (m < 4) N.x[m] = *(const bf16x8*)(sb + (kk ? xo1 : xo0) + m * 2048);
          if (m >= 4 && m < 10) N.w[m - 4] = rdw(sb, m - 4, kk);
        }
        SCHED;
      };
      auto init = [&]() {
#pragma unroll
        for (int ft = 0; ft < 6; ++ft) {
          f32x4 bv = *(const f32x4*)(smem + SP_B1 + (16 * ft + 4 * g) * 4);
#pragma unroll
          for (int tt = 0; tt < 4; ++tt) acc[ft][tt] = bv;
        }
      };
      P1_PIPE(loadf, step, mma, init());
      STAMP(3);
      tlo = misc[M_TAG]; thi = misc[M_TAG + 1];
      lds_wait_ge(misc + M_P1D, 4u);
#pragma unroll
      for (int t2 = 0; t2 < 2; ++t2)
#pragma unroll
        for (int i = 0; i < 6; ++i) *(f32x4*)(smem + (2 * th + t2) * SLOT + i * 1024 + lane * 16) = acc[i][2 + t2];
      asm volatile("s_waitcnt lgkmcnt(0)" ::: "memory");
      if (lane == 0) misc[M_OFF + th] = 1u;
#pragma unroll
      for (int tt = 0; tt < 2; ++tt)
#pragma unroll
        for (int ft = 0; ft < 6; ++ft) {
          const f32x2 m0 = mish2((f32x2){acc[ft][tt][0], acc[ft][tt][1]}), m1 = mish2((f32x2){acc[ft][tt][2], acc[ft][tt][3]});
          acc[ft][tt] = (f32x4){m0[0], m0[1], m1[0], m1[1]};
        }
      lds_wait_ge(misc + M_GRDY, 2u);
#pragma unroll
      for (int tt = 0; tt < 2; ++tt) {
        const float og = 1.f - 0.5f * *(const float*)(smem + SP_G + (64 * th + 16 * tt + r16) * 4);
#pragma unroll
        for (int fp = 0; fp < 3; ++fp) {
          const f32x4 a0 = acc[2 * fp][tt] * og, a1 = acc[2 * fp + 1][tt] * og;
          st128(rW, OFF_AF + (((rc * 4 + cg) * 22 + 3 * jl + fp) * 8 + 4 * th + tt) * 1024 + lane * 16,
                (u32x4){pack2(a0[0], a0[1]), pack2(a0[2], a0[3]), pack2(a1[0], a1[1]), pack2(a1[2], a1[3])});
        }
      }
      STAMP(4);
      WAITV(0);
      if (lds_inc(smem, M_ACT, lane) == 3u && lane == 0) set_flag(rW, OFF_FA + (rc * 4 + cg) * 128 + jl * 8, tlo, thi);
      STAMP(5);
    } else {
      f32x4 acc[4][5], gac[4];
      const unsigned bo0 = ST_BM + (40 * (r16 & 1) + (r16 >> 1)) * 128 + ((g ^ ((((r16 >> 2) & 3) << 1) | (r16 & 1))) * 16);
      const unsigned bo1 = bo0 ^ 64;
      struct Frag { bf16x8 x[4], w[5], gf; };
      Frag F0, F1;
      int kc = 0;
      auto loadf = [&](Frag& F, unsigned boff, int kk) {
        const char* sb = smem + boff;
#pragma unroll
        for (int tt = 0; tt < 4; ++tt) F.x[tt] = *(const bf16x8*)(sb + (kk ? xo1 : xo0) + tt * 2048);
#pragma unroll
        for (int nt = 0; nt < 5; ++nt) F.w[nt] = *(const bf16x8*)(sb + (kk ? bo1 : bo0) + nt * 1024);
        F.gf = *(const bf16x8*)(smem + SP_GW + kc * 64 + g * 16);
        ++kc;
      };
      auto mma = [&](Frag& F) {
#pragma unroll
        for (int tt = 0; tt < 4; ++tt) gac[tt] = __builtin_amdgcn_mfma_f32_16x16x32_bf16(F.x[tt], F.gf, gac[tt], 0, 0, 0);
#pragma unroll
        for (int m = 0; m < 20; ++m)
          acc[m & 3][m >> 2] = __builtin_amdgcn_mfma_f32_16x16x32_bf16(F.x[m & 3], F.w[m >> 2], acc[m & 3][m >> 2], 0, 0, 0);
      };
      auto step = [&](Frag& C, Frag& N, unsigned boff, const int kk) {
        const char* sb = smem + boff;
#pragma unroll
        for (int m = 0; m < 24; ++m) {
          SCHED;
          if (m < 20) acc[m & 3][m >> 2] = __builtin_amdgcn_mfma_f32_16x16x32_bf16(C.x[m & 3], C.w[m >> 2], acc[m & 3][m >> 2], 0, 0, 0);
          else gac[m & 3] = __builtin_amdgcn_mfma_f32_16x16x32_bf16(C.x[m & 3], C.gf, gac[m & 3], 0, 0, 0);
          SCHED;
          if (m < 4) N.x[m] = *(const bf16x8*)(sb + (kk ? xo1 : xo0) + m * 2048);
          if (m >= 4 && m < 9) N.w[m - 4] = *(const bf16x8*)(sb + (kk ? bo1 : bo0) + (m - 4) * 1024);
          if (m == 9) N.gf = *(const bf16x8*)(smem + SP_GW + kc * 64 + g * 16);
        }
        ++kc;
        SCHED;
      };
      auto init = [&]() {
#pragma unroll
        for (int tt = 0; tt < 4; ++tt) {
          gac[tt] = (f32x4){0.f, 0.f, 0.f, 0.f};
#pragma unroll
          for (int nt = 0; nt < 5; ++nt) acc[tt][nt] = (f32x4){0.f, 0.f, 0.f, 0.f};
        }
      };
      P1_PIPE(loadf, step, mma, init());
      STAMP(3);
      tlo = misc[M_TAG]; thi = misc[M_TAG + 1];
      {
        char* bw = smem + L_BU + r16 * BUS + (64 * th + 4 * g) * 4;
#pragma unroll
        for (int tt = 0; tt < 4; ++tt)
#pragma unroll
          for (int nt = 0; nt < 5; ++nt) *(f32x4*)(bw + nt * 16 * BUS + tt * 64) = acc[tt][nt];
        asm volatile("s_waitcnt lgkmcnt(0)" ::: "memory");
        lds_inc(smem, M_BUW, lane);
        f32x4 lg = r16 == 0 ? gac[0] : r16 == 1 ? gac[1] : r16 == 2 ? gac[2] : gac[3];
        f32x4 gg;
#pragma unroll
        for (int r = 0; r < 4; ++r) gg[r] = 2.f * __builtin_amdgcn_rcpf(1.f + __expf(-lg[r]));
        if (r16 < 4) *(f32x4*)(smem + SP_G + (64 * th + 16 * r16 + 4 * g) * 4) = gg;
        asm volatile("s_waitcnt lgkmcnt(0)" ::: "memory");
        lds_inc(smem, M_GRDY, lane);
      }
      STAMP(4);
      lds_wait_ge(misc + M_OFF + th, 1u);
#pragma unroll
      for (int t2 = 0; t2 < 2; ++t2) {
        const float og = 1.f - 0.5f * *(const float*)(smem + SP_G + (64 * th + 32 + 16 * t2 + r16) * 4);
#pragma unroll
        for (int fp = 0; fp < 3; ++fp) {
          const char* sp = smem + (2 * th + t2) * SLOT + fp * 2048 + lane * 16;
          st128(rW, OFF_AF + (((rc * 4 + cg) * 22 + 3 * jl + fp) * 8 + 4 * th + 2 + t2) * 1024 + lane * 16,
                mish_unit(*(const f32x4*)sp, *(const f32x4*)(sp + 1024), og));
        }
      }
      WAITV(0);
      if (lds_inc(smem, M_ACT, lane) == 3u && lane == 0) set_flag(rW, OFF_FA + (rc * 4 + cg) * 128 + jl * 8, tlo, thi);
      STAMP(5);
    }

    const int wn = wave & 1, wt = wave >> 1;
    f32x4 acc[5][4];
#pragma unroll
    for (int nt = 0; nt < 5; ++nt)
#pragma unroll
      for (int tt = 0; tt < 4; ++tt) acc[nt][tt] = (f32x4){0.f, 0.f, 0.f, 0.f};
    struct Frag2 { bf16x8 x[4], w[5]; };
    Frag2 G0, G1;
    const unsigned ao = 4 * wt * 1024 + lane * 16;
    const unsigned twb = 8192 + (4 * g + q4) * 320 + (p4 >> 1) * 16 + (p4 & 1) * 8;
    const unsigned twa = twb + (5 * wn + (g & 1)) * 32;
    const unsigned twl = (wn == 1 && (g & 1)) ? twb : twa + 128;
    const int f4 = (r16 >> 2) & 3, fx = (f4 == 0 ? 0 : f4 == 1 ? 2 : f4 == 2 ? 3 : 1);
    const unsigned cwo = 8192 + (80 * wn + r16) * 64 + ((g ^ fx) * 16);
    auto rd2 = [&](Frag2& N, const char* sb, int e, const bool ssm) {
      if (e < 4) N.x[e] = *(const bf16x8*)(sb + ao + e * 1024);
      else if (ssm) N.w[e - 4] = *(const bf16x8*)(sb + cwo + (e - 4) * 1024);
      else {
        const unsigned ad = e - 4 < 4 ? twa + 32 * (e - 4) : twl;
        s16x4 lo = __builtin_amdgcn_ds_read_tr16_b64_v4i16((LAS s16x4*)(sb + ad));
        s16x4 hi = __builtin_amdgcn_ds_read_tr16_b64_v4i16((LAS s16x4*)(sb + ad + 16 * 320));
        N.w[e - 4] = __builtin_bit_cast(bf16x8, __builtin_shufflevector(lo, hi, 0, 1, 2, 3, 4, 5, 6, 7));
      }
    };
    auto wait_full = [&](int st) { lds_wait_ge(misc + M_FULL + st, 1u); };
    int slot = 0;
    unsigned fpre = 0;
    auto step2 = [&](Frag2& C, Frag2& N, int n, const bool next_ssm, const bool last) {
      if (!last && fpre == 0u) wait_full(n + 1);
      asm volatile("s_waitcnt lgkmcnt(0)" ::: "memory");
      const char* sb = smem + slot * SLOT;
      slot = slot == NSLOT - 1 ? 0 : slot + 1;
#pragma unroll
      for (int m = 0; m < 20; ++m) {
        SCHED;
        acc[m >> 2][m & 3] = __builtin_amdgcn_mfma_f32_16x16x32_bf16(C.w[m >> 2], C.x[m & 3], acc[m >> 2][m & 3], 0, 0, 0);
        SCHED;
        if (!last && m < 9) rd2(N, sb, m, next_ssm);
        if (m == 9) { if (lane == 0) misc[M_CONS + wave] = n + 1; }
        if (m == 12) fpre = misc[M_FULL + n + 2];
      }
      SCHED;
    };
    wait_full(0);
    __builtin_amdgcn_s_setprio(1);
    STAMP(6);
#pragma unroll
    for (int e = 0; e < 9; ++e) rd2(G0, smem, e, false);
    slot = 1;
#pragma unroll 1
    for (int n = 0; n < 10; n += 2) { step2(G0, G1, n, false, false); step2(G1, G0, n + 1, false, false); }
    STAMP(7);
    step2(G0, G1, 10, false, false); step2(G1, G0, 11, true, false);
    STAMP(8);
#pragma unroll 1
    for (int n = 12; n < 20; n += 2) { step2(G0, G1, n, true, false); step2(G1, G0, n + 1, true, false); }
    step2(G0, G1, 20, true, false); step2(G1, G0, 21, true, true);
    __builtin_amdgcn_s_setprio(0);
    STAMP(9);
#pragma unroll
    for (int t2 = 0; t2 < 2; ++t2) {
      const int owner = 2 * wt + t2;
      if (owner != cg) {
        const unsigned pb = OFF_PB + (((rc * 4 + jl) * 4 + owner) * 4 + cg) * 10240 + wn * 5120 + lane * 16;
#pragma unroll
        for (int nt = 0; nt < 5; ++nt) {
          const f32x4 v0 = acc[nt][2 * t2], v1 = acc[nt][2 * t2 + 1];
          u32x4 o; o[0] = pack2(v0[0], v0[1]); o[1] = pack2(v0[2], v0[3]); o[2] = pack2(v1[0], v1[1]); o[3] = pack2(v1[2], v1[3]);
          st128(rW, pb + nt * 1024, o);
        }
      }
    }
    lds_wait_ge(misc + M_CONS, 22u); lds_wait_ge(misc + M_CONS + 1, 22u); lds_wait_ge(misc + M_CONS + 2, 22u); lds_wait_ge(misc + M_CONS + 3, 22u);
#pragma unroll
    for (int tt = 0; tt < 4; ++tt) {
      if (2 * wt + (tt >> 1) == cg) {
#pragma unroll
        for (int nt = 0; nt < 5; ++nt) *(f32x4*)(smem + (5 * (2 * wn + (tt & 1)) + nt) * 1024 + lane * 16) = acc[nt][tt];
      }
    }
    STAMP(10);
    asm volatile("s_waitcnt vmcnt(0) lgkmcnt(0)" ::: "memory");
    if (lds_inc(smem, M_RED + 1, lane) == 3u) {
      if (lane == 0) { set_flag(rW, OFF_FR + (rc * 4 + jl) * 128 + cg * 8, tlo, thi); misc[M_RED] = 4u; }
    }
    STAMP(11);
  }
  STAMP(25);
  STAMP(26);
  STAMP_OUT(0);
}

extern "C" void kernel_launch(void* const* d_in, const int* in_sizes, int n_in,
                              void* d_out, int out_size, void* d_ws, size_t ws_size,
                              hipStream_t stream) {
  Args a;
  a.x   = (const u16*)d_in[0];
  a.Lre = (const u16*)d_in[1];
  a.Lim = (const u16*)d_in[2];
  a.Bre = (const u16*)d_in[3];
  a.Bim = (const u16*)d_in[4];
  a.Cre = (const u16*)d_in[5];
  a.Cim = (const u16*)d_in[6];
  a.ldt = (const u16*)d_in[7];
  a.Dsk = (const u16*)d_in[8];
  a.w1  = (const u16*)d_in[10];
  a.b1  = (const u16*)d_in[11];
  a.w2  = (const u16*)d_in[12];
  a.gw  = (const u16*)d_in[13];
  a.ws  = (char*)d_ws;
  a.out = (float*)d_out;
  fused<<<256, 512, 0, stream>>>(a);
}
```

```cpp
#include <hip/hip_runtime.h>
#include <hip/hip_bf16.h>
#include <stdint.h>
#pragma clang diagnostic ignored "-Winline-asm"

typedef __attribute__((ext_vector_type(8))) __bf16 bf16x8;
typedef __attribute__((ext_vector_type(2))) __bf16 bf16x2;
typedef __attribute__((ext_vector_type(4))) float f32x4;
typedef __attribute__((ext_vector_type(2))) float f32x2;
typedef __attribute__((ext_vector_type(4))) unsigned u32x4;
typedef __attribute__((ext_vector_type(2))) unsigned u32x2;
typedef __attribute__((ext_vector_type(4))) short s16x4;
typedef unsigned short u16;
#define LAS __attribute__((address_space(3)))
#define GAS __attribute__((address_space(1)))
#define DI __device__ __forceinline__

constexpr int T_ = 2048, D_ = 640, P_ = 640, F_ = 1536;

constexpr unsigned OFF_AF = 0;
constexpr unsigned OFF_PB = OFF_AF + 64u * 22 * 8192;
constexpr unsigned OFF_E  = OFF_PB + 64u * 16 * 10240;
constexpr unsigned OFF_FA = OFF_E + 256u * 640;
constexpr unsigned OFF_FE = OFF_FA + 64 * 128;
constexpr unsigned OFF_FR = OFF_FE + 16 * 128;
constexpr unsigned OFF_EPOCH = OFF_FR + 64 * 128;
constexpr unsigned WS_BYTES = OFF_EPOCH + 256;

#define STAMP(i)
#define STAMP_DECL
#define STAMP_OUT(kid)

DI float bflo(unsigned v) { return __uint_as_float(v << 16); }
DI float bfhi(unsigned v) { return __uint_as_float(v & 0xffff0000u); }
DI unsigned pack2(float a, float b) {
  bf16x2 r; r[0] = (__bf16)a; r[1] = (__bf16)b; return __builtin_bit_cast(unsigned, r);
}
DI float dswap(float v) {
  return __int_as_float(__builtin_amdgcn_mov_dpp(__float_as_int(v), 0xB1, 0xf, 0xf, true));
}
DI float cmul(float lr, float ls, float v) { return fmaf(lr, v, ls * dswap(v)); }
DI float cmuladd(float lr, float ls, float v, float a) { return fmaf(lr, v, fmaf(ls, dswap(v), a)); }
DI f32x2 cm2(f32x2 a, f32x2 b) { return (f32x2){a[0] * b[0] - a[1] * b[1], a[0] * b[1] + a[1] * b[0]}; }
DI float sfma(float a, float b, float c) { float d; asm("v_fma_f32 %0, %1, %2, %3" : "=v"(d) : "v"(a), "v"(b), "v"(c)); return d; }
DI float smul(float a, float b) { float d; asm("v_mul_f32 %0, %1, %2" : "=v"(d) : "v"(a), "v"(b)); return d; }

DI f32x2 mish2(f32x2 h) {
  f32x2 z = h * 1.44269504088896341f;
  f32x2 w; w[0] = __builtin_amdgcn_exp2f(fminf(z[0], 43.f)); w[1] = __builtin_amdgcn_exp2f(fminf(z[1], 43.f));
  f32x2 n = w * (w + 2.f);
  f32x2 d = n + 2.f;
  f32x2 r; r[0] = __builtin_amdgcn_rcpf(d[0]); r[1] = __builtin_amdgcn_rcpf(d[1]);
  return (h * n) * r;
}

DI void glds16(const void* gsrc, unsigned lds_dst) {
  unsigned keep;
  asm volatile("s_mov_b32 %0, m0\n\ts_mov_b32 m0, %2\n\ts_nop 0\n\tglobal_load_lds_dwordx4 %1, off\n\ts_mov_b32 m0, %0"
               : "=&s"(keep) : "v"(gsrc), "s"(lds_dst) : "memory");
}
DI void touch(const void* gsrc, unsigned lds_dst) {
  unsigned keep;
  asm volatile("s_mov_b32 %0, m0\n\ts_mov_b32 m0, %2\n\ts_nop 0\n\tglobal_load_lds_dword %1, off\n\ts_mov_b32 m0, %0"
               : "=&s"(keep) : "v"(gsrc), "s"(lds_dst) : "memory");
}
DI float sum16(float v) {
  auto r = __builtin_amdgcn_permlane16_swap(__float_as_uint(v), __float_as_uint(v), false, false);
  return __uint_as_float(r[0]) + __uint_as_float(r[1]);
}
DI float sum32(float v) {
  auto r = __builtin_amdgcn_permlane32_swap(__float_as_uint(v), __float_as_uint(v), false, false);
  return __uint_as_float(r[0]) + __uint_as_float(r[1]);
}

typedef unsigned long long u64;
#define SC1 16
#define SCHED __builtin_amdgcn_sched_barrier(0)
#define RSRC __amdgpu_buffer_rsrc_t
DI void st128(RSRC r, unsigned off, u32x4 v) { __builtin_amdgcn_raw_buffer_store_b128(v, r, off, 0, SC1); }
DI void st128f(RSRC r, unsigned off, f32x4 v) { __builtin_amdgcn_raw_buffer_store_b128(__builtin_bit_cast(u32x4, v), r, off, 0, SC1); }
DI void st64(RSRC r, unsigned off, u32x2 v) { __builtin_amdgcn_raw_buffer_store_b64(v, r, off, 0, SC1); }
DI f32x4 ld128f(RSRC r, unsigned off) { return __builtin_bit_cast(f32x4, __builtin_amdgcn_raw_buffer_load_b128(r, off, 0, SC1)); }
DI f32x2 ld64f(RSRC r, unsigned off) { return __builtin_bit_cast(f32x2, __builtin_amdgcn_raw_buffer_load_b64(r, off, 0, SC1)); }
DI void glds16c(const void* gsrc, unsigned lds_dst) {
  unsigned keep;
  asm volatile("s_mov_b32 %0, m0\n\ts_mov_b32 m0, %2\n\ts_nop 0\n\tglobal_load_lds_dwordx4 %1, off sc1\n\ts_mov_b32 m0, %0"
               : "=&s"(keep) : "v"(gsrc), "s"(lds_dst) : "memory");
}

DI void gldss(unsigned voff, const void* sbase_, unsigned lds_dst) {
  asm volatile("s_mov_b32 m0, %2\n\ts_nop 0\n\tglobal_load_lds_dwordx4 %0, %1" :: "v"(voff), "s"(sbase_), "s"(lds_dst) : "memory", "m0");
}
DI void gldssc(unsigned voff, const void* sbase_, unsigned lds_dst) {
  asm volatile("s_mov_b32 m0, %2\n\ts_nop 0\n\tglobal_load_lds_dwordx4 %0, %1 sc1" :: "v"(voff), "s"(sbase_), "s"(lds_dst) : "memory", "m0");
}

constexpr int ST_X = 0, ST_BM = 16384, ST_W1 = 26624, ST_SZ = 38912, NSTG = 4;
constexpr int SP = NSTG * ST_SZ;
constexpr int PARS = 96;
constexpr int SP_PAR = SP, SP_B1 = SP + 3840, SP_EC = SP + 4224, SP_EL = SP + 5504, SP_GW = SP + 5824, SP_G = SP + 7104,
              SP_MISC = SP + 7616, LDS_BYTES = SP + 7872;
constexpr int BUS = 528, L_BU = SP - 80 * BUS;
constexpr int HIS = 176, L_HI = SP - 128 * HIS;
constexpr int SLOT = 18432, NSLOT = 7;
enum { M_CNTE = 0, M_GRDY, M_P1D, M_ACT, M_BURDY, M_SRD, M_SWR, M_PUB, M_RED, M_NEXT = 10, M_CONS = 12, M_OFF = 16, M_HOK = 18, M_BUW = 19, M_TAG = 24, M_FULL = 32 };
constexpr int SPIN_LDS = 1 << 22, SPIN_G = 1 << 19;

struct Args {
  const u16 *x, *Lre, *Lim, *Bre, *Bim, *Cre, *Cim, *ldt, *Dsk, *w1, *b1, *w2, *gw;
  char* ws;
  float* out;
};

#define CBAR do { SCHED; asm volatile("s_waitcnt lgkmcnt(0)\n\ts_barrier" ::: "memory"); SCHED; } while (0)
#define LBAR(n) asm volatile("s_waitcnt vmcnt(" #n ")\n\ts_barrier" ::: "memory")
#define WAITV(n) asm volatile("s_waitcnt vmcnt(" #n ")" ::: "memory")

DI void lds_wait_ge(volatile LAS unsigned* w, unsigned v) {
  for (int sp = 0; *w < v && sp < SPIN_LDS; ++sp) __builtin_amdgcn_s_sleep(1);
  asm volatile("" ::: "memory");
}
DI unsigned lds_inc(char* smem, int word, int lane) {
  unsigned old = 0;
  asm volatile("; wave converged" ::: "memory");
  if (lane == 0) old = __hip_atomic_fetch_add((LAS unsigned*)(smem + SP_MISC) + word, 1u, __ATOMIC_RELAXED, __HIP_MEMORY_SCOPE_WORKGROUP);
  asm volatile("; wave converged" : "+v"(old) :: "memory");
  return __builtin_amdgcn_readfirstlane(old);
}
DI void poll_flags(RSRC rW, unsigned off, int n, unsigned tlo, unsigned thi, int lane) {
  const unsigned o = off + (lane < n ? lane : 0) * 8;
  for (int sp = 0; n > 0 && sp < SPIN_G; ++sp) {
    const u32x2 f = __builtin_amdgcn_raw_buffer_load_b64(rW, o, 0, SC1);
    if (__all(f[0] == tlo && f[1] == thi)) break;
    __builtin_amdgcn_s_sleep(1);
    asm volatile("" ::: "memory");
  }
  asm volatile("" ::: "memory");
}
DI void set_flag(RSRC rW, unsigned off, unsigned tlo, unsigned thi) { u32x2 t; t[0] = tlo; t[1] = thi; st64(rW, off, t); }

DI u32x4 mish_unit(f32x4 a0, f32x4 a1, float og) {
  f32x2 m0 = mish2((f32x2){a0[0], a0[1]}) * og, m1 = mish2((f32x2){a0[2], a0[3]}) * og;
  f32x2 m2 = mish2((f32x2){a1[0], a1[1]}) * og, m3 = mish2((f32x2){a1[2], a1[3]}) * og;
  u32x4 o;
  o[0] = pack2(m0[0], m0[1]); o[1] = pack2(m1[0], m1[1]); o[2] = pack2(m2[0], m2[1]); o[3] = pack2(m3[0], m3[1]);
  return o;
}

DI void scan_task(RSRC rW, char* smem, volatile LAS unsigned* misc, int u, int lane, int rc, int cg, int jl, unsigned tlo, unsigned thi) {
  const int j = 4 * cg + jl;
  int L = 64 * u + lane; const bool act = L < 160; L = act ? L : 159;
  const int seg = L / 40, chl = L - 40 * seg;
  const float* pp = (const float*)(smem + SP_PAR + chl * PARS);
  const f32x2 l1 = *(const f32x2*)pp, l128 = *(const f32x2*)(pp + 20);
  const f32x4 cl = *(const f32x4*)(pp + 4);
  f32x2 pw = seg == 1 ? (f32x2){cl[2], cl[3]} : *(const f32x2*)(pp + 16 + (seg == 3 ? 2 : 0));
  if (seg == 0) pw = (f32x2){1.f, 0.f};
  const float lr = l1[0], li = l1[1], nli = -l1[1];
  lds_wait_ge(misc + M_BUW, 2u);
  __builtin_amdgcn_s_setprio(3);
  f32x4 ar[8], ai[8];
  {
    const char* bp = smem + L_BU + 2 * chl * BUS + seg * 128;
#pragma unroll
    for (int i = 0; i < 8; ++i) { ar[i] = *(const f32x4*)(bp + i * 16); ai[i] = *(const f32x4*)(bp + BUS + i * 16); }
  }
  float sr = 0.f, si = 0.f;
#pragma unroll
  for (int t = 0; t < 32; ++t) {
    const float tr = fmaf(nli, si, ar[t >> 2][t & 3]), ti = fmaf(li, sr, ai[t >> 2][t & 3]);
    sr = fmaf(lr, sr, tr); si = fmaf(lr, si, ti);
  }
  if (act) *(f32x2*)(smem + SP_EC + (seg * 40 + chl) * 8) = (f32x2){sr, si};
  asm volatile("s_waitcnt lgkmcnt(0)" ::: "memory");
  lds_inc(smem, M_SRD, lane);
  lds_wait_ge(misc + M_SRD, 3u);
  float qr = 0.f, qi = 0.f;
#pragma unroll
  for (int i = 0; i < 3; ++i) {
    const f32x2 e = *(const f32x2*)(smem + SP_EC + (i * 40 + chl) * 8);
    const float tr = fmaf(-cl[3], qi, e[0]), ti = fmaf(cl[3], qr, e[1]);
    const float nr = fmaf(cl[2], qr, tr), ni = fmaf(cl[2], qi, ti);
    qr = i < seg ? nr : qr; qi = i < seg ? ni : qi;
  }
  if (act && seg == 3) {
    const float er = fmaf(cl[2], qr, fmaf(-cl[3], qi, sr)), ei = fmaf(cl[2], qi, fmaf(cl[3], qr, si));
    st128(rW, OFF_E + ((j * 16 + rc) * 40 + chl) * 16, (u32x4){__float_as_uint(er), __float_as_uint(ei), tlo, thi});
  }
  __builtin_amdgcn_s_setprio(0);
#pragma unroll
  for (int i = 0; i < 8; ++i)
#pragma unroll
    for (int r = 0; r < 4; ++r) {
      const float xr = fmaf(cl[0], ar[i][r], -cl[1] * ai[i][r]), xi = fmaf(cl[0], ai[i][r], cl[1] * ar[i][r]);
      ar[i][r] = xr; ai[i][r] = xi;
    }
  lds_wait_ge(misc + M_GRDY, 2u);
  f32x4 gv[8];
#pragma unroll
  for (int i = 0; i < 8; ++i) gv[i] = *(const f32x4*)(smem + SP_G + seg * 128 + i * 16);
  {
    u32x4 ev[15];
    for (int sp = 0; sp < SPIN_G; ++sp) {
      bool ok = true;
#pragma unroll
      for (int r = 0; r < 15; ++r) {
        ev[r] = (u32x4){0u, 0u, tlo, thi};
        if (r < rc) ev[r] = __builtin_amdgcn_raw_buffer_load_b128(rW, OFF_E + ((j * 16 + r) * 40 + chl) * 16, 0, SC1);
      }
#pragma unroll
      for (int r = 0; r < 15; ++r) ok = ok && ev[r][2] == tlo && ev[r][3] == thi;
      if (__all(ok)) break;
      __builtin_amdgcn_s_sleep(1);
      asm volatile("" ::: "memory");
    }
    __builtin_amdgcn_s_setprio(3);
    float cr = 0.f, ci = 0.f;
    const float er = l128[0], ei = l128[1], nei = -l128[1];
#pragma unroll
    for (int r = 0; r < 15; ++r)
      if (r < rc) {
        const float tr = sfma(nei, ci, __uint_as_float(ev[r][0])), ti = sfma(ei, cr, __uint_as_float(ev[r][1]));
        cr = sfma(er, cr, tr); ci = sfma(er, ci, ti);
      }
    const float tr = fmaf(-pw[1], ci, qr), ti = fmaf(pw[1], cr, qi);
    cr = fmaf(pw[0], cr, tr); ci = fmaf(pw[0], ci, ti);
    sr = fmaf(cl[0], cr, -cl[1] * ci); si = fmaf(cl[0], ci, cl[1] * cr);
  }
  char* wp = smem + L_HI + seg * 32 * HIS + chl * 2;
#pragma unroll
  for (int t = 0; t < 32; ++t) {
    const float tr = sfma(nli, si, ar[t >> 2][t & 3]), ti = sfma(li, sr, ai[t >> 2][t & 3]);
    sr = sfma(lr, sr, tr); si = sfma(lr, si, ti);
    const float g2 = gv[t >> 2][t & 3];
    const unsigned pk = pack2(smul(sr, g2), smul(-si, g2));
    *(u16*)(wp + t * HIS) = (u16)pk;
    *(u16*)(wp + t * HIS + 80) = (u16)(pk >> 16);
  }
  asm volatile("s_waitcnt lgkmcnt(0)" ::: "memory");
  lds_inc(smem, M_SWR, lane);
  lds_wait_ge(misc + M_SWR, 3u);
#pragma unroll
  for (int k = 0; k < 7; ++k) {
    const int f = u + 3 * k;
    if (f < 20) {
      const int pe = f >> 1, part = pe / 5, e = pe % 5;
      const int t = 64 * (f & 1) + lane, pi = 5 * jl + e, step = 12 + 2 * (pi >> 2) + part;
      const u32x4 v = *(const u32x4*)(smem + L_HI + t * HIS + part * 80 + e * 16);
      st128(rW, OFF_AF + (((rc * 4 + cg) * 22 + step) * 8 + (t >> 4)) * 1024 + ((pi & 3) * 16 + (t & 15)) * 16, v);
    }
  }
  WAITV(0);
  if (lds_inc(smem, M_PUB, lane) == 2u) {
    if (lane == 0) set_flag(rW, OFF_FA + (rc * 4 + cg) * 128 + 32 + jl * 8, tlo, thi);
    poll_flags(rW, OFF_FA + (rc * 4 + cg) * 128 + 32, 4, tlo, thi, lane);
    if (lane == 0) misc[M_HOK] = 1u;
  }
  __builtin_amdgcn_s_setprio(0);
}

__global__ __launch_bounds__(512, 2) void fused(Args a) {
  __shared__ __attribute__((aligned(16))) char smem[LDS_BYTES];
  const int tid = threadIdx.x, lane = tid & 63;
  const int wave = __builtin_amdgcn_readfirstlane(tid >> 6);
  const int b = blockIdx.x, grp = b & 7, idx = b >> 3;
  const int cg = grp & 3, jl = idx & 3;
  const int rc = 8 * (grp >> 2) + (idx >> 2), j = 4 * cg + jl;
  const int r16 = lane & 15, g = lane >> 4;
  char* ws = a.ws;
  const RSRC rW = __builtin_amdgcn_make_buffer_rsrc((void*)ws, 0, WS_BYTES, 0x00020000);
  volatile LAS unsigned* misc = (volatile LAS unsigned*)(smem + SP_MISC);
  const unsigned sbase = (unsigned)(size_t)(LAS char*)smem;
  STAMP_DECL
  STAMP(0);

  if (wave >= 4) {
    const int lw = wave - 4;
    {
      unsigned voff[10]; const char* sb_[10]; int inc[10]; unsigned dsto[10];
#pragma unroll
      for (int i = 0; i < 10; ++i) {
        int p = lw + 4 * i; p = p >= 38 ? p - 38 : p;
        dsto[i] = sbase + p * 1024;
        const int rw = lane >> 3;
        const unsigned ox = (8 * p + rw) * (D_ * 2) + 16 * ((lane & 7) ^ (((8 * p + rw) >> 1) & 7));
        const int pb = p < 21 ? p - 16 : p - 21;
        const unsigned ob = (8 * pb + rw) * (D_ * 2) + 16 * ((lane & 7) ^ ((((rw >> 1) & 3) << 1) | (p >= 21 ? 1 : 0)));
        const int ci = 64 * (p >= 26 ? p - 26 : 0) + lane, k = ci / 12, cp = ci % 12;
        int cc = cp - 2 * ((k >> 3) & 1); cc = cc < 0 ? cc + 12 : cc;
        const unsigned ow = k * (F_ * 2) + 16 * cc;
        voff[i] = p < 16 ? ox : p < 26 ? ob : ow;
        inc[i] = p < 26 ? 128 : 64 * F_ * 2;
        sb_[i] = (const char*)(p < 16 ? a.x + (size_t)128 * rc * D_ : p < 21 ? a.Bre + (size_t)40 * j * D_ : p < 26 ? a.Bim + (size_t)40 * j * D_ : a.w1 + 96 * j);
      }
      auto stage = [&](unsigned boff) {
#pragma unroll
        for (int i = 0; i < 10; ++i) { gldss(voff[i], sb_[i], dsto[i] + boff); sb_[i] += inc[i]; }
      };
      stage(0); stage(ST_SZ);
      STAMP(1);
      LBAR(10);
      STAMP(2);
      stage(2 * ST_SZ);
      LBAR(10);
      stage(3 * ST_SZ); stage(0);
#pragma unroll 1
      for (int s = 2; s <= 6; ++s) {
        LBAR(20);
        stage(((s + 3) & 3) * ST_SZ);
      }
      LBAR(20); LBAR(10); LBAR(0);
    }
    STAMP(3);
    const unsigned tlo = misc[M_TAG], thi = misc[M_TAG + 1];

    unsigned woff[10], coff[10];
#pragma unroll
    for (int i = 0; i < 10; ++i) {
      const int ci = 64 * i + lane;
      const int k = ci / 20, cp = ci % 20;
      int cc = cp - 2 * ((k >> 2) & 1); cc = cc < 0 ? cc + 20 : cc;
      woff[i] = k * (D_ * 2) + 16 * cc;
      const int row = ci >> 2, f4 = (row >> 2) & 3, fx = (f4 == 0 ? 0 : f4 == 1 ? 2 : f4 == 2 ? 3 : 1);
      coff[i] = row * (P_ * 2) + 16 * ((ci & 3) ^ fx);
    }
    const char* wbase2 = (const char*)(a.w2 + (size_t)384 * cg * D_ + 160 * jl);
    const char* abase = ws + OFF_AF + (size_t)(rc * 4 + cg) * 22 * 8192;
    const unsigned aoff = lane * 16;
    auto issueB = [&](int n, unsigned sl) {
      if (n < 12) {
        const char* p = wbase2 + (size_t)n * (32 * D_ * 2);
#pragma unroll
        for (int i = 0; i < 10; ++i) gldss(woff[i], p, sl + 8192 + i * 1024);
      } else {
        const char* p = (const char*)(((n & 1) ? a.Cim : a.Cre) + (size_t)160 * jl * P_ + 160 * cg) + ((n - 12) >> 1) * 64;
#pragma unroll
        for (int i = 0; i < 10; ++i) gldss(coff[i], p, sl + 8192 + i * 1024);
      }
    };
    auto issueA = [&](int n, unsigned sl) {
#pragma unroll
      for (int i = 0; i < 8; ++i) gldssc(aoff, abase + (size_t)n * 8192 + i * 1024, sl + i * 1024);
    };
    int p2 = -1;
    if (lw != 1) {
      scan_task(rW, smem, misc, lw == 0 ? 2 : lw - 2, lane, rc, cg, jl, tlo, thi);
      lds_wait_ge(misc + M_FULL, 1u);
      STAMP(6);
    } else {
      lds_wait_ge(misc + M_P1D, 4u);
#pragma unroll 1
      for (int k = 0; k < 6; ++k) issueB(k, sbase + k * SLOT);
      STAMP(4);
      poll_flags(rW, OFF_FA + (rc * 4 + cg) * 128, 4, tlo, thi, lane);
      STAMP(5);
#pragma unroll 1
      for (int k = 0; k < 6; ++k) issueA(k, sbase + k * SLOT);
      WAITV(40); if (lane == 0) misc[M_FULL] = 1u;
      WAITV(32); if (lane == 0) misc[M_FULL + 1] = 1u;
      WAITV(24); if (lane == 0) misc[M_FULL + 2] = 1u;
      WAITV(16); if (lane == 0) misc[M_FULL + 3] = 1u;
      WAITV(8);  if (lane == 0) misc[M_FULL + 4] = 1u;
      p2 = 5;
      lds_wait_ge(misc + M_SRD, 3u);
    }
#pragma unroll 1
    for (;;) {
      const int n = (int)lds_inc(smem, M_NEXT, lane);
      if (n >= 22) break;
      const unsigned need = n - NSLOT + 1;
      bool ok = n < NSLOT || (misc[M_CONS] >= need && misc[M_CONS + 1] >= need && misc[M_CONS + 2] >= need && misc[M_CONS + 3] >= need);
      ok = ok && (n < 12 || misc[M_HOK] != 0u);
      if (!ok) {
        WAITV(0);
        if (lane == 0 && p2 >= 0) misc[M_FULL + p2] = 1u;
        p2 = -1;
        if (n >= NSLOT) {
          lds_wait_ge(misc + M_CONS, need); lds_wait_ge(misc + M_CONS + 1, need); lds_wait_ge(misc + M_CONS + 2, need); lds_wait_ge(misc + M_CONS + 3, need);
        }
      }
      asm volatile("" ::: "memory");
      const unsigned sl = sbase + (n % NSLOT) * SLOT;
      issueB(n, sl);
      if (n >= 12) lds_wait_ge(misc + M_HOK, 1u);
      issueA(n, sl);
      WAITV(18);
      if (lane == 0 && p2 >= 0) misc[M_FULL + p2] = 1u;
      p2 = n;
    }
    WAITV(0);  if (lane == 0 && p2 >= 0) misc[M_FULL + p2] = 1u;
    STAMP(8);
    const int tl = 16 * (lw & 1) + r16, n0 = 160 * jl + 80 * (lw >> 1) + 4 * g;
    const size_t orow = (size_t)(128 * rc + 32 * cg + tl) * D_ + n0;
    f32x4 sk[5];
    {
      const float gt = 0.5f * *(const float*)(smem + SP_G + (32 * cg + tl) * 4);
#pragma unroll
      for (int i = 0; i < 5; ++i) {
        u32x2 xv = *(const u32x2*)(a.x + orow + 16 * i);
        u32x2 dv = *(const u32x2*)(a.Dsk + n0 + 16 * i);
        sk[i][0] = gt * bflo(xv[0]) * bflo(dv[0]); sk[i][1] = gt * bfhi(xv[0]) * bfhi(dv[0]);
        sk[i][2] = gt * bflo(xv[1]) * bflo(dv[1]); sk[i][3] = gt * bfhi(xv[1]) * bfhi(dv[1]);
      }
    }
    STAMP(22);
    for (int sp = 0; misc[M_RED] < 4u && sp < SPIN_LDS; ++sp) __builtin_amdgcn_s_sleep(4);
    asm volatile("" ::: "memory");
    STAMP(23);
    poll_flags(rW, OFF_FR + (rc * 4 + jl) * 128, 4, tlo, thi, lane);
    STAMP(24);
    if (rc == 15 && j == 0 && tid == 256) set_flag(rW, OFF_EPOCH, tlo, thi);
    u32x2 pv[3][5];
#pragma unroll
    for (int s = 0; s < 3; ++s) {
      const int sc = (cg + 1 + s) & 3;
#pragma unroll
      for (int i = 0; i < 5; ++i)
        pv[s][i] = __builtin_amdgcn_raw_buffer_load_b64(rW, OFF_PB + (((rc * 4 + jl) * 4 + cg) * 4 + sc) * 10240 + (lw >> 1) * 5120 + i * 1024 + lane * 16 + (lw & 1) * 8, 0, SC1);
    }
#pragma unroll
    for (int i = 0; i < 5; ++i) {
      f32x4 v = *(const f32x4*)(smem + (5 * lw + i) * 1024 + lane * 16) + sk[i];
#pragma unroll
      for (int s = 0; s < 3; ++s) v += (f32x4){bflo(pv[s][i][0]), bfhi(pv[s][i][0]), bflo(pv[s][i][1]), bfhi(pv[s][i][1])};
      *(f32x4*)(a.out + orow + 16 * i) = v;
    }
  } else {
    if (wave == 3) {
      unsigned long long pc; unsigned back;
      asm volatile("s_getpc_b64 %0\n1:\n\ts_mov_b32 %1, 1b-_Z5fused4Args" : "=s"(pc), "=s"(back));
      touch((const char*)(pc - back) + (idx * 12 + (lane < 12 ? lane : 0)) * 128, sbase + SP_EL);
      touch((const char*)a.w1 + (size_t)(b * 60 + (lane < 60 ? lane : 0)) * 128, sbase + SP_EL);
      touch((const char*)(lane < 32 ? a.Bre : a.Bim) + (size_t)(b * 25 + ((lane & 31) < 25 ? (lane & 31) : 0)) * 128, sbase + SP_EL);
    } else if (wave == 0) {
      touch((const char*)a.x + (size_t)(b * 80 + lane) * 128, sbase + SP_EL);
      touch((const char*)a.x + (size_t)(b * 80 + 64 + r16) * 128, sbase + SP_EL);
      touch((const char*)a.w2 + (size_t)(b * 60 + (lane < 60 ? lane : 0)) * 128, sbase + SP_EL);
      touch((const char*)(lane < 32 ? a.Cre : a.Cim) + (size_t)(b * 25 + ((lane & 31) < 25 ? (lane & 31) : 0)) * 128, sbase + SP_EL);
    }
    if (wave == 1) {
      const u64 ep = __hip_atomic_load((GAS u64*)(ws + OFF_EPOCH), __ATOMIC_RELAXED, __HIP_MEMORY_SCOPE_AGENT);
      const unsigned v = *(const unsigned*)(a.b1 + 96 * j + 2 * (lane < 48 ? lane : 0));
      const u32x4 g0 = *(const u32x4*)(a.gw + 8 * lane), g1 = *(const u32x4*)(a.gw + 8 * (64 + r16));
      if (lane < 48) *(f32x2*)(smem + SP_B1 + lane * 8) = (f32x2){bflo(v), bfhi(v)};
      *(u32x4*)(smem + SP_GW + lane * 16) = g0;
      if (lane < 16) *(u32x4*)(smem + SP_GW + 1024 + lane * 16) = g1;
      if (lane < 24 || lane >= 32) misc[lane] = lane == M_NEXT ? 6u : 0u;
      if (lane == 0) { const u64 tg = ep + 1; misc[M_TAG] = (unsigned)tg; misc[M_TAG + 1] = (unsigned)(tg >> 32); }
    } else if (wave == 2) {
      const int ch = 40 * j + (lane < 40 ? lane : 0);
      const float lre = bflo(a.Lre[ch]), lim = bflo(a.Lim[ch]), dt = expf(bflo(a.ldt[ch]));
      float e = expf(dt * lre), sn, cs;
      sincosf(dt * lim, &sn, &cs);
      f32x2 l1 = {e * cs, e * sn};
      const float nr = l1[0] - 1.f, ni = l1[1], den = lre * lre + lim * lim;
      f32x2 cf = {(nr * lre + ni * lim) / den, (ni * lre - nr * lim) / den};
      f32x2 l2 = cm2(l1, l1), l4 = cm2(l2, l2), l8 = cm2(l4, l4), l12 = cm2(l8, l4), l16 = cm2(l8, l8);
      f32x2 l32 = cm2(l16, l16), l64 = cm2(l32, l32), l128 = cm2(l64, l64), l96 = cm2(l64, l32);
      if (lane < 40) {
        float* pp = (float*)(smem + SP_PAR + lane * PARS);
        *(f32x4*)(pp) = (f32x4){l1[0], l1[1], l16[0], l16[1]};
        *(f32x4*)(pp + 4) = (f32x4){cf[0], cf[1], l32[0], l32[1]};
        *(f32x4*)(pp + 8) = (f32x4){l12[0], l12[1], l8[0], l8[1]};
        *(f32x4*)(pp + 12) = (f32x4){l4[0], l4[1], 1.f, 0.f};
        *(f32x4*)(pp + 16) = (f32x4){l64[0], l64[1], l96[0], l96[1]};
        *(f32x2*)(pp + 20) = l128;
      }
    }
    STAMP(1);
    const int th = wave & 1;
    const unsigned xo0 = ST_X + (64 * th + r16) * 128 + ((g ^ (r16 >> 1)) * 16);
    const unsigned xo1 = xo0 ^ 64;
    const int q4 = r16 >> 2, p4 = r16 & 3;

#define P1_PIPE(LOADF, STEP, MMA, INIT)                                             \
    CBAR;                                                                           \
    __builtin_amdgcn_s_setprio(3);                                                  \
    STAMP(2);                                                                       \
    INIT;                                                                           \
    LOADF(F0, 0u, 0);                                                               \
    STEP(F0, F1, 0u, 1);                                                            \
    _Pragma("unroll 1") for (int s = 1; s < 10; ++s) {                              \
      CBAR;                                                                         \
      STEP(F1, F0, (s & 3) * ST_SZ, 0);                                             \
      STEP(F0, F1, (s & 3) * ST_SZ, 1);                                             \
    }                                                                               \
    asm volatile("s_waitcnt lgkmcnt(0)" ::: "memory");                              \
    lds_inc(smem, M_P1D, lane);                                                     \
    MMA(F1); SCHED; __builtin_amdgcn_s_setprio(0);

    unsigned tlo, thi;
    if (wave < 2) {
      f32x4 acc[6][4];
      const unsigned wbase = ST_W1 + (8 * g + q4) * 192 + (p4 >> 1) * 16 + (p4 & 1) * 8;
      const unsigned wa = wbase + (g & 1) * 32;
      const unsigned wb = wbase + ((g & 1) ? 0 : 160);
      struct Frag { bf16x8 x[4], w[6]; };
      Frag F0, F1;
      auto rdw = [&](const char* sb, int ft, int kk) {
        unsigned ad = (ft < 5 ? wa + 32 * ft : wb) + kk * 32 * 192;
        s16x4 lo = __builtin_amdgcn_ds_read_tr16_b64_v4i16((LAS s16x4*)(sb + ad));
        s16x4 hi = __builtin_amdgcn_ds_read_tr16_b64_v4i16((LAS s16x4*)(sb + ad + 4 * 192));
        return __builtin_bit_cast(bf16x8, __builtin_shufflevector(lo, hi, 0, 1, 2, 3, 4, 5, 6, 7));
      };
      auto loadf = [&](Frag& F, unsigned boff, int kk) {
        const char* sb = smem + boff;
#pragma unroll
        for (int tt = 0; tt < 4; ++tt) F.x[tt] = *(const bf16x8*)(sb + (kk ? xo1 : xo0) + tt * 2048);
#pragma unroll
        for (int ft = 0; ft < 6; ++ft) F.w[ft] = rdw(sb, ft, kk);
      };
      auto mma = [&](Frag& F) {
#pragma unroll
        for (int m = 0; m < 24; ++m)
          acc[m >> 2][m & 3] = __builtin_amdgcn_mfma_f32_16x16x32_bf16(F.w[m >> 2], F.x[m & 3], acc[m >> 2][m & 3], 0, 0, 0);
      };
      auto step = [&](Frag& C, Frag& N, unsigned boff, const int kk) {
        const char* sb = smem + boff;
#pragma unroll
        for (int m = 0; m < 24; ++m) {
          SCHED;
          acc[m >> 2][m & 3] = __builtin_amdgcn_mfma_f32_16x16x32_bf16(C.w[m >> 2], C.x[m & 3], acc[m >> 2][m & 3], 0, 0, 0);
          SCHED;
          if (m < 4) N.x[m] = *(const bf16x8*)(sb + (kk ? xo1 : xo0) + m * 2048);
          if (m >= 4 && m < 10) N.w[m - 4] = rdw(sb, m - 4, kk);
        }
        SCHED;
      };
      auto init = [&]() {
#pragma unroll
        for (int ft = 0; ft < 6; ++ft) {
          f32x4 bv = *(const f32x4*)(smem + SP_B1 + (16 * ft + 4 * g) * 4);
#pragma unroll
          for (int tt = 0; tt < 4; ++tt) acc[ft][tt] = bv;
        }
      };
      P1_PIPE(loadf, step, mma, init());
      STAMP(3);
      tlo = misc[M_TAG]; thi = misc[M_TAG + 1];
      lds_wait_ge(misc + M_P1D, 4u);
#pragma unroll
      for (int t2 = 0; t2 < 2; ++t2)
#pragma unroll
        for (int i = 0; i < 6; ++i) *(f32x4*)(smem + (2 * th + t2) * SLOT + i * 1024 + lane * 16) = acc[i][2 + t2];
      asm volatile("s_waitcnt lgkmcnt(0)" ::: "memory");
      if (lane == 0) misc[M_OFF + th] = 1u;
      lds_wait_ge(misc + M_GRDY, 2u);
#pragma unroll
      for (int tt = 0; tt < 2; ++tt) {
        const float og = 1.f - 0.5f * *(const float*)(smem + SP_G + (64 * th + 16 * tt + r16) * 4);
#pragma unroll
        for (int fp = 0; fp < 3; ++fp)
          st128(rW, OFF_AF + (((rc * 4 + cg) * 22 + 3 * jl + fp) * 8 + 4 * th + tt) * 1024 + lane * 16,
                mish_unit(acc[2 * fp][tt], acc[2 * fp + 1][tt], og));
      }
      STAMP(4);
      WAITV(0);
      if (lds_inc(smem, M_ACT, lane) == 3u && lane == 0) set_flag(rW, OFF_FA + (rc * 4 + cg) * 128 + jl * 8, tlo, thi);
      STAMP(5);
    } else {
      f32x4 acc[4][5], gac[4];
      const unsigned bo0 = ST_BM + (40 * (r16 & 1) + (r16 >> 1)) * 128 + ((g ^ ((((r16 >> 2) & 3) << 1) | (r16 & 1))) * 16);
      const unsigned bo1 = bo0 ^ 64;
      struct Frag { bf16x8 x[4], w[5], gf; };
      Frag F0, F1;
      int kc = 0;
      auto loadf = [&](Frag& F, unsigned boff, int kk) {
        const char* sb = smem + boff;
#pragma unroll
        for (int tt = 0; tt < 4; ++tt) F.x[tt] = *(const bf16x8*)(sb + (kk ? xo1 : xo0) + tt * 2048);
#pragma unroll
        for (int nt = 0; nt < 5; ++nt) F.w[nt] = *(const bf16x8*)(sb + (kk ? bo1 : bo0) + nt * 1024);
        F.gf = *(const bf16x8*)(smem + SP_GW + kc * 64 + g * 16);
        ++kc;
      };
      auto mma = [&](Frag& F) {
#pragma unroll
        for (int tt = 0; tt < 4; ++tt) gac[tt] = __builtin_amdgcn_mfma_f32_16x16x32_bf16(F.x[tt], F.gf, gac[tt], 0, 0, 0);
#pragma unroll
        for (int m = 0; m < 20; ++m)
          acc[m & 3][m >> 2] = __builtin_amdgcn_mfma_f32_16x16x32_bf16(F.x[m & 3], F.w[m >> 2], acc[m & 3][m >> 2], 0, 0, 0);
      };
      auto step = [&](Frag& C, Frag& N, unsigned boff, const int kk) {
        const char* sb = smem + boff;
#pragma unroll
        for (int m = 0; m < 24; ++m) {
          SCHED;
          if (m < 20) acc[m & 3][m >> 2] = __builtin_amdgcn_mfma_f32_16x16x32_bf16(C.x[m & 3], C.w[m >> 2], acc[m & 3][m >> 2], 0, 0, 0);
          else gac[m & 3] = __builtin_amdgcn_mfma_f32_16x16x32_bf16(C.x[m & 3], C.gf, gac[m & 3], 0, 0, 0);
          SCHED;
          if (m < 4) N.x[m] = *(const bf16x8*)(sb + (kk ? xo1 : xo0) + m * 2048);
          if (m >= 4 && m < 9) N.w[m - 4] = *(const bf16x8*)(sb + (kk ? bo1 : bo0) + (m - 4) * 1024);
          if (m == 9) N.gf = *(const bf16x8*)(smem + SP_GW + kc * 64 + g * 16);
        }
        ++kc;
        SCHED;
      };
      auto init = [&]() {
#pragma unroll
        for (int tt = 0; tt < 4; ++tt) {
          gac[tt] = (f32x4){0.f, 0.f, 0.f, 0.f};
#pragma unroll
          for (int nt = 0; nt < 5; ++nt) acc[tt][nt] = (f32x4){0.f, 0.f, 0.f, 0.f};
        }
      };
      P1_PIPE(loadf, step, mma, init());
      STAMP(3);
      tlo = misc[M_TAG]; thi = misc[M_TAG + 1];
      {
        char* bw = smem + L_BU + r16 * BUS + (64 * th + 4 * g) * 4;
#pragma unroll
        for (int tt = 0; tt < 4; ++tt)
#pragma unroll
          for (int nt = 0; nt < 5; ++nt) *(f32x4*)(bw + nt * 16 * BUS + tt * 64) = acc[tt][nt];
        asm volatile("s_waitcnt lgkmcnt(0)" ::: "memory");
        lds_inc(smem, M_BUW, lane);
        f32x4 lg = r16 == 0 ? gac[0] : r16 == 1 ? gac[1] : r16 == 2 ? gac[2] : gac[3];
        f32x4 gg;
#pragma unroll
        for (int r = 0; r < 4; ++r) gg[r] = 2.f * __builtin_amdgcn_rcpf(1.f + __expf(-lg[r]));
        if (r16 < 4) *(f32x4*)(smem + SP_G + (64 * th + 16 * r16 + 4 * g) * 4) = gg;
        asm volatile("s_waitcnt lgkmcnt(0)" ::: "memory");
        lds_inc(smem, M_GRDY, lane);
      }
      STAMP(4);
      lds_wait_ge(misc + M_OFF + th, 1u);
#pragma unroll
      for (int t2 = 0; t2 < 2; ++t2) {
        const float og = 1.f - 0.5f * *(const float*)(smem + SP_G + (64 * th + 32 + 16 * t2 + r16) * 4);
#pragma unroll
        for (int fp = 0; fp < 3; ++fp) {
          const char* sp = smem + (2 * th + t2) * SLOT + fp * 2048 + lane * 16;
          st128(rW, OFF_AF + (((rc * 4 + cg) * 22 + 3 * jl + fp) * 8 + 4 * th + 2 + t2) * 1024 + lane * 16,
                mish_unit(*(const f32x4*)sp, *(const f32x4*)(sp + 1024), og));
        }
      }
      WAITV(0);
      if (lds_inc(smem, M_ACT, lane) == 3u && lane == 0) set_flag(rW, OFF_FA + (rc * 4 + cg) * 128 + jl * 8, tlo, thi);
      STAMP(5);
    }

    const int wn = wave & 1, wt = wave >> 1;
    f32x4 acc[5][4];
#pragma unroll
    for (int nt = 0; nt < 5; ++nt)
#pragma unroll
      for (int tt = 0; tt < 4; ++tt) acc[nt][tt] = (f32x4){0.f, 0.f, 0.f, 0.f};
    struct Frag2 { bf16x8 x[4], w[5]; };
    Frag2 G0, G1;
    const unsigned ao = 4 * wt * 1024 + lane * 16;
    const unsigned twb = 8192 + (4 * g + q4) * 320 + (p4 >> 1) * 16 + (p4 & 1) * 8;
    const unsigned twa = twb + (5 * wn + (g & 1)) * 32;
    const unsigned twl = (wn == 1 && (g & 1)) ? twb : twa + 128;
    const int f4 = (r16 >> 2) & 3, fx = (f4 == 0 ? 0 : f4 == 1 ? 2 : f4 == 2 ? 3 : 1);
    const unsigned cwo = 8192 + (80 * wn + r16) * 64 + ((g ^ fx) * 16);
    auto rd2 = [&](Frag2& N, const char* sb, int e, const bool ssm) {
      if (e < 4) N.x[e] = *(const bf16x8*)(sb + ao + e * 1024);
      else if (ssm) N.w[e - 4] = *(const bf16x8*)(sb + cwo + (e - 4) * 1024);
      else {
        const unsigned ad = e - 4 < 4 ? twa + 32 * (e - 4) : twl;
        s16x4 lo = __builtin_amdgcn_ds_read_tr16_b64_v4i16((LAS s16x4*)(sb + ad));
        s16x4 hi = __builtin_amdgcn_ds_read_tr16_b64_v4i16((LAS s16x4*)(sb + ad + 16 * 320));
        N.w[e - 4] = __builtin_bit_cast(bf16x8, __builtin_shufflevector(lo, hi, 0, 1, 2, 3, 4, 5, 6, 7));
      }
    };
    auto wait_full = [&](int st) { lds_wait_ge(misc + M_FULL + st, 1u); };
    int slot = 0;
    unsigned fpre = 0;
    auto step2 = [&](Frag2& C, Frag2& N, int n, const bool next_ssm, const bool last) {
      if (!last && fpre == 0u) wait_full(n + 1);
      asm volatile("s_waitcnt lgkmcnt(0)" ::: "memory");
      const char* sb = smem + slot * SLOT;
      slot = slot == NSLOT - 1 ? 0 : slot + 1;
#pragma unroll
      for (int m = 0; m < 20; ++m) {
        SCHED;
        acc[m >> 2][m & 3] = __builtin_amdgcn_mfma_f32_16x16x32_bf16(C.w[m >> 2], C.x[m & 3], acc[m >> 2][m & 3], 0, 0, 0);
        SCHED;
        if (!last && m < 9) rd2(N, sb, m, next_ssm);
        if (m == 9) { if (lane == 0) misc[M_CONS + wave] = n + 1; }
        if (m == 12) fpre = misc[M_FULL + n + 2];
      }
      SCHED;
    };
    wait_full(0);
    __builtin_amdgcn_s_setprio(1);
    STAMP(6);
#pragma unroll
    for (int e = 0; e < 9; ++e) rd2(G0, smem, e, false);
    slot = 1;
#pragma unroll 1
    for (int n = 0; n < 10; n += 2) { step2(G0, G1, n, false, false); step2(G1, G0, n + 1, false, false); }
    STAMP(7);
    step2(G0, G1, 10, false, false); step2(G1, G0, 11, true, false);
    STAMP(8);
#pragma unroll 1
    for (int n = 12; n < 20; n += 2) { step2(G0, G1, n, true, false); step2(G1, G0, n + 1, true, false); }
    step2(G0, G1, 20, true, false); step2(G1, G0, 21, true, true);
    __builtin_amdgcn_s_setprio(0);
    STAMP(9);
#pragma unroll
    for (int t2 = 0; t2 < 2; ++t2) {
      const int owner = 2 * wt + t2;
      if (owner != cg) {
        const unsigned pb = OFF_PB + (((rc * 4 + jl) * 4 + owner) * 4 + cg) * 10240 + wn * 5120 + lane * 16;
#pragma unroll
        for (int nt = 0; nt < 5; ++nt) {
          const f32x4 v0 = acc[nt][2 * t2], v1 = acc[nt][2 * t2 + 1];
          u32x4 o; o[0] = pack2(v0[0], v0[1]); o[1] = pack2(v0[2], v0[3]); o[2] = pack2(v1[0], v1[1]); o[3] = pack2(v1[2], v1[3]);
          st128(rW, pb + nt * 1024, o);
        }
      }
    }
    lds_wait_ge(misc + M_CONS, 22u); lds_wait_ge(misc + M_CONS + 1, 22u); lds_wait_ge(misc + M_CONS + 2, 22u); lds_wait_ge(misc + M_CONS + 3, 22u);
#pragma unroll
    for (int tt = 0; tt < 4; ++tt) {
      if (2 * wt + (tt >> 1) == cg) {
#pragma unroll
        for (int nt = 0; nt < 5; ++nt) *(f32x4*)(smem + (5 * (2 * wn + (tt & 1)) + nt) * 1024 + lane * 16) = acc[nt][tt];
      }
    }
    STAMP(10);
    asm volatile("s_waitcnt vmcnt(0) lgkmcnt(0)" ::: "memory");
    if (lds_inc(smem, M_RED + 1, lane) == 3u) {
      if (lane == 0) { set_flag(rW, OFF_FR + (rc * 4 + jl) * 128 + cg * 8, tlo, thi); misc[M_RED] = 4u; }
    }
    STAMP(11);
  }
  STAMP(25);
  STAMP(26);
  STAMP_OUT(0);
}

extern "C" void kernel_launch(void* const* d_in, const int* in_sizes, int n_in,
                              void* d_out, int out_size, void* d_ws, size_t ws_size,
                              hipStream_t stream) {
  Args a;
  a.x   = (const u16*)d_in[0];
  a.Lre = (const u16*)d_in[1];
  a.Lim = (const u16*)d_in[2];
  a.Bre = (const u16*)d_in[3];
  a.Bim = (const u16*)d_in[4];
  a.Cre = (const u16*)d_in[5];
  a.Cim = (const u16*)d_in[6];
  a.ldt = (const u16*)d_in[7];
  a.Dsk = (const u16*)d_in[8];
  a.w1  = (const u16*)d_in[10];
  a.b1  = (const u16*)d_in[11];
  a.w2  = (const u16*)d_in[12];
  a.gw  = (const u16*)d_in[13];
  a.ws  = (char*)d_ws;
  a.out = (float*)d_out;
  fused<<<256, 512, 0, stream>>>(a);
}
```

```cpp
#include <hip/hip_runtime.h>
#include <hip/hip_bf16.h>
#include <stdint.h>
#pragma clang diagnostic ignored "-Winline-asm"

typedef __attribute__((ext_vector_type(8))) __bf16 bf16x8;
typedef __attribute__((ext_vector_type(2))) __bf16 bf16x2;
typedef __attribute__((ext_vector_type(4))) float f32x4;
typedef __attribute__((ext_vector_type(2))) float f32x2;
typedef __attribute__((ext_vector_type(4))) unsigned u32x4;
typedef __attribute__((ext_vector_type(2))) unsigned u32x2;
typedef __attribute__((ext_vector_type(4))) short s16x4;
typedef unsigned short u16;
#define LAS __attribute__((address_space(3)))
#define GAS __attribute__((address_space(1)))
#define DI __device__ __forceinline__

constexpr int T_ = 2048, D_ = 640, P_ = 640, F_ = 1536;

constexpr unsigned OFF_AF = 0;
constexpr unsigned OFF_PB = OFF_AF + 64u * 22 * 8192;
constexpr unsigned OFF_E  = OFF_PB + 64u * 16 * 10240;
constexpr unsigned OFF_FA = OFF_E + 256u * 640;
constexpr unsigned OFF_FE = OFF_FA + 64 * 128;
constexpr unsigned OFF_FR = OFF_FE + 16 * 128;
constexpr unsigned OFF_EPOCH = OFF_FR + 64 * 128;
constexpr unsigned WS_BYTES = OFF_EPOCH + 256;

#define STAMP(i)
#define STAMP_DECL
#define STAMP_OUT(kid)

DI float bflo(unsigned v) { return __uint_as_float(v << 16); }
DI float bfhi(unsigned v) { return __uint_as_float(v & 0xffff0000u); }
DI unsigned pack2(float a, float b) {
  bf16x2 r; r[0] = (__bf16)a; r[1] = (__bf16)b; return __builtin_bit_cast(unsigned, r);
}
DI float dswap(float v) {
  return __int_as_float(__builtin_amdgcn_mov_dpp(__float_as_int(v), 0xB1, 0xf, 0xf, true));
}
DI float cmul(float lr, float ls, float v) { return fmaf(lr, v, ls * dswap(v)); }
DI float cmuladd(float lr, float ls, float v, float a) { return fmaf(lr, v, fmaf(ls, dswap(v), a)); }
DI f32x2 cm2(f32x2 a, f32x2 b) { return (f32x2){a[0] * b[0] - a[1] * b[1], a[0] * b[1] + a[1] * b[0]}; }
DI float sfma(float a, float b, float c) { float d; asm("v_fma_f32 %0, %1, %2, %3" : "=v"(d) : "v"(a), "v"(b), "v"(c)); return d; }
DI float smul(float a, float b) { float d; asm("v_mul_f32 %0, %1, %2" : "=v"(d) : "v"(a), "v"(b)); return d; }

DI f32x2 mish2(f32x2 h) {
  f32x2 z = h * 1.44269504088896341f;
  f32x2 w; w[0] = __builtin_amdgcn_exp2f(fminf(z[0], 43.f)); w[1] = __builtin_amdgcn_exp2f(fminf(z[1], 43.f));
  f32x2 n = w * (w + 2.f);
  f32x2 d = n + 2.f;
  f32x2 r; r[0] = __builtin_amdgcn_rcpf(d[0]); r[1] = __builtin_amdgcn_rcpf(d[1]);
  return (h * n) * r;
}

DI void glds16(const void* gsrc, unsigned lds_dst) {
  unsigned keep;
  asm volatile("s_mov_b32 %0, m0\n\ts_mov_b32 m0, %2\n\ts_nop 0\n\tglobal_load_lds_dwordx4 %1, off\n\ts_mov_b32 m0, %0"
               : "=&s"(keep) : "v"(gsrc), "s"(lds_dst) : "memory");
}
DI void touch(const void* gsrc, unsigned lds_dst) {
  unsigned keep;
  asm volatile("s_mov_b32 %0, m0\n\ts_mov_b32 m0, %2\n\ts_nop 0\n\tglobal_load_lds_dword %1, off\n\ts_mov_b32 m0, %0"
               : "=&s"(keep) : "v"(gsrc), "s"(lds_dst) : "memory");
}
DI float sum16(float v) {
  auto r = __builtin_amdgcn_permlane16_swap(__float_as_uint(v), __float_as_uint(v), false, false);
  return __uint_as_float(r[0]) + __uint_as_float(r[1]);
}
DI float sum32(float v) {
  auto r = __builtin_amdgcn_permlane32_swap(__float_as_uint(v), __float_as_uint(v), false, false);
  return __uint_as_float(r[0]) + __uint_as_float(r[1]);
}

typedef unsigned long long u64;
#define SC1 16
#define SCHED __builtin_amdgcn_sched_barrier(0)
#define RSRC __amdgpu_buffer_rsrc_t
DI void st128(RSRC r, unsigned off, u32x4 v) { __builtin_amdgcn_raw_buffer_store_b128(v, r, off, 0, SC1); }
DI void st128f(RSRC r, unsigned off, f32x4 v) { __builtin_amdgcn_raw_buffer_store_b128(__builtin_bit_cast(u32x4, v), r, off, 0, SC1); }
DI void st64(RSRC r, unsigned off, u32x2 v) { __builtin_amdgcn_raw_buffer_store_b64(v, r, off, 0, SC1); }
DI f32x4 ld128f(RSRC r, unsigned off) { return __builtin_bit_cast(f32x4, __builtin_amdgcn_raw_buffer_load_b128(r, off, 0, SC1)); }
DI f32x2 ld64f(RSRC r, unsigned off) { return __builtin_bit_cast(f32x2, __builtin_amdgcn_raw_buffer_load_b64(r, off, 0, SC1)); }
DI void glds16c(const void* gsrc, unsigned lds_dst) {
  unsigned keep;
  asm volatile("s_mov_b32 %0, m0\n\ts_mov_b32 m0, %2\n\ts_nop 0\n\tglobal_load_lds_dwordx4 %1, off sc1\n\ts_mov_b32 m0, %0"
               : "=&s"(keep) : "v"(gsrc), "s"(lds_dst) : "memory");
}

DI void gldss(unsigned voff, const void* sbase_, unsigned lds_dst) {
  asm volatile("s_mov_b32 m0, %2\n\ts_nop 0\n\tglobal_load_lds_dwordx4 %0, %1" :: "v"(voff), "s"(sbase_), "s"(lds_dst) : "memory", "m0");
}
DI void gldssc(unsigned voff, const void* sbase_, unsigned lds_dst) {
  asm volatile("s_mov_b32 m0, %2\n\ts_nop 0\n\tglobal_load_lds_dwordx4 %0, %1 sc1" :: "v"(voff), "s"(sbase_), "s"(lds_dst) : "memory", "m0");
}

constexpr int ST_X = 0, ST_BM = 16384, ST_W1 = 26624, ST_SZ = 38912, NSTG = 4;
constexpr int SP = NSTG * ST_SZ;
constexpr int PARS = 96;
constexpr int SP_PAR = SP, SP_B1 = SP + 3840, SP_EC = SP + 4224, SP_EL = SP + 5504, SP_GW = SP + 5824, SP_G = SP + 7104,
              SP_MISC = SP + 7616, LDS_BYTES = SP + 7872;
constexpr int BUS = 528, L_BU = SP - 80 * BUS;
constexpr int HIS = 176, L_HI = SP - 128 * HIS;
constexpr int SLOT = 18432, NSLOT = 7;
enum { M_CNTE = 0, M_GRDY, M_P1D, M_ACT, M_BURDY, M_SRD, M_SWR, M_PUB, M_RED, M_NEXT = 10, M_CONS = 12, M_OFF = 16, M_HOK = 18, M_BUW = 19, M_TAG = 24, M_FULL = 32 };
constexpr int SPIN_LDS = 1 << 22, SPIN_G = 1 << 19;

struct Args {
  const u16 *x, *Lre, *Lim, *Bre, *Bim, *Cre, *Cim, *ldt, *Dsk, *w1, *b1, *w2, *gw;
  char* ws;
  float* out;
};

#define CBAR do { SCHED; asm volatile("s_waitcnt lgkmcnt(0)\n\ts_barrier" ::: "memory"); SCHED; } while (0)
#define LBAR(n) asm volatile("s_waitcnt vmcnt(" #n ")\n\ts_barrier" ::: "memory")
#define WAITV(n) asm volatile("s_waitcnt vmcnt(" #n ")" ::: "memory")

DI void lds_wait_ge(volatile LAS unsigned* w, unsigned v) {
  for (int sp = 0; *w < v && sp < SPIN_LDS; ++sp) __builtin_amdgcn_s_sleep(1);
  asm volatile("" ::: "memory");
}
DI unsigned lds_inc(char* smem, int word, int lane) {
  unsigned old = 0;
  asm volatile("; wave converged" ::: "memory");
  if (lane == 0) old = __hip_atomic_fetch_add((LAS unsigned*)(smem + SP_MISC) + word, 1u, __ATOMIC_RELAXED, __HIP_MEMORY_SCOPE_WORKGROUP);
  asm volatile("; wave converged" : "+v"(old) :: "memory");
  return __builtin_amdgcn_readfirstlane(old);
}
DI void poll_flags(RSRC rW, unsigned off, int n, unsigned tlo, unsigned thi, int lane) {
  const unsigned o = off + (lane < n ? lane : 0) * 8;
  for (int sp = 0; n > 0 && sp < SPIN_G; ++sp) {
    const u32x2 f = __builtin_amdgcn_raw_buffer_load_b64(rW, o, 0, SC1);
    if (__all(f[0] == tlo && f[1] == thi)) break;
    __builtin_amdgcn_s_sleep(1);
    asm volatile("" ::: "memory");
  }
  asm volatile("" ::: "memory");
}
DI void set_flag(RSRC rW, unsigned off, unsigned tlo, unsigned thi) { u32x2 t; t[0] = tlo; t[1] = thi; st64(rW, off, t); }

DI u32x4 mish_unit(f32x4 a0, f32x4 a1, float og) {
  f32x2 m0 = mish2((f32x2){a0[0], a0[1]}) * og, m1 = mish2((f32x2){a0[2], a0[3]}) * og;
  f32x2 m2 = mish2((f32x2){a1[0], a1[1]}) * og, m3 = mish2((f32x2){a1[2], a1[3]}) * og;
  u32x4 o;
  o[0] = pack2(m0[0], m0[1]); o[1] = pack2(m1[0], m1[1]); o[2] = pack2(m2[0], m2[1]); o[3] = pack2(m3[0], m3[1]);
  return o;
}

DI void scan_task(RSRC rW, char* smem, volatile LAS unsigned* misc, int u, int lane, int rc, int cg, int jl, unsigned tlo, unsigned thi) {
  const int j = 4 * cg + jl;
  int L = 64 * u + lane; const bool act = L < 160; L = act ? L : 159;
  const int seg = L / 40, chl = L - 40 * seg;
  const float* pp = (const float*)(smem + SP_PAR + chl * PARS);
  const f32x2 l1 = *(const f32x2*)pp, l128 = *(const f32x2*)(pp + 20);
  const f32x4 cl = *(const f32x4*)(pp + 4);
  f32x2 pw = seg == 1 ? (f32x2){cl[2], cl[3]} : *(const f32x2*)(pp + 16 + (seg == 3 ? 2 : 0));
  if (seg == 0) pw = (f32x2){1.f, 0.f};
  const float lr = l1[0], li = l1[1], nli = -l1[1];
  lds_wait_ge(misc + M_BUW, 2u);
  __builtin_amdgcn_s_setprio(3);
  f32x4 ar[8], ai[8];
  {
    const char* bp = smem + L_BU + 2 * chl * BUS + seg * 128;
#pragma unroll
    for (int i = 0; i < 8; ++i) { ar[i] = *(const f32x4*)(bp + i * 16); ai[i] = *(const f32x4*)(bp + BUS + i * 16); }
  }
  float sr = 0.f, si = 0.f;
#pragma unroll
  for (int t = 0; t < 32; ++t) {
    const float tr = fmaf(nli, si, ar[t >> 2][t & 3]), ti = fmaf(li, sr, ai[t >> 2][t & 3]);
    sr = fmaf(lr, sr, tr); si = fmaf(lr, si, ti);
  }
  if (act) *(f32x2*)(smem + SP_EC + (seg * 40 + chl) * 8) = (f32x2){sr, si};
  asm volatile("s_waitcnt lgkmcnt(0)" ::: "memory");
  lds_inc(smem, M_SRD, lane);
  lds_wait_ge(misc + M_SRD, 3u);
  float qr = 0.f, qi = 0.f;
#pragma unroll
  for (int i = 0; i < 3; ++i) {
    const f32x2 e = *(const f32x2*)(smem + SP_EC + (i * 40 + chl) * 8);
    const float tr = fmaf(-cl[3], qi, e[0]), ti = fmaf(cl[3], qr, e[1]);
    const float nr = fmaf(cl[2], qr, tr), ni = fmaf(cl[2], qi, ti);
    qr = i < seg ? nr : qr; qi = i < seg ? ni : qi;
  }
  if (act && seg == 3) {
    const float er = fmaf(cl[2], qr, fmaf(-cl[3], qi, sr)), ei = fmaf(cl[2], qi, fmaf(cl[3], qr, si));
    st128(rW, OFF_E + ((j * 16 + rc) * 40 + chl) * 16, (u32x4){__float_as_uint(er), __float_as_uint(ei), tlo, thi});
  }
  __builtin_amdgcn_s_setprio(0);
#pragma unroll
  for (int i = 0; i < 8; ++i)
#pragma unroll
    for (int r = 0; r < 4; ++r) {
      const float xr = fmaf(cl[0], ar[i][r], -cl[1] * ai[i][r]), xi = fmaf(cl[0], ai[i][r], cl[1] * ar[i][r]);
      ar[i][r] = xr; ai[i][r] = xi;
    }
  lds_wait_ge(misc + M_GRDY, 2u);
  f32x4 gv[8];
#pragma unroll
  for (int i = 0; i < 8; ++i) gv[i] = *(const f32x4*)(smem + SP_G + seg * 128 + i * 16);
  {
    u32x4 ev[15];
    for (int sp = 0; sp < SPIN_G; ++sp) {
      bool ok = true;
#pragma unroll
      for (int r = 0; r < 15; ++r) {
        ev[r] = (u32x4){0u, 0u, tlo, thi};
        if (r < rc) ev[r] = __builtin_amdgcn_raw_buffer_load_b128(rW, OFF_E + ((j * 16 + r) * 40 + chl) * 16, 0, SC1);
      }
#pragma unroll
      for (int r = 0; r < 15; ++r) ok = ok && ev[r][2] == tlo && ev[r][3] == thi;
      if (__all(ok)) break;
      __builtin_amdgcn_s_sleep(1);
      asm volatile("" ::: "memory");
    }
    __builtin_amdgcn_s_setprio(3);
    float cr = 0.f, ci = 0.f;
    const float er = l128[0], ei = l128[1], nei = -l128[1];
#pragma unroll
    for (int r = 0; r < 15; ++r)
      if (r < rc) {
        const float tr = sfma(nei, ci, __uint_as_float(ev[r][0])), ti = sfma(ei, cr, __uint_as_float(ev[r][1]));
        cr = sfma(er, cr, tr); ci = sfma(er, ci, ti);
      }
    const float tr = fmaf(-pw[1], ci, qr), ti = fmaf(pw[1], cr, qi);
    cr = fmaf(pw[0], cr, tr); ci = fmaf(pw[0], ci, ti);
    sr = fmaf(cl[0], cr, -cl[1] * ci); si = fmaf(cl[0], ci, cl[1] * cr);
  }
  char* wp = smem + L_HI + seg * 32 * HIS + chl * 2;
#pragma unroll
  for (int t = 0; t < 32; ++t) {
    const float tr = sfma(nli, si, ar[t >> 2][t & 3]), ti = sfma(li, sr, ai[t >> 2][t & 3]);
    sr = sfma(lr, sr, tr); si = sfma(lr, si, ti);
    const float g2 = gv[t >> 2][t & 3];
    const unsigned pk = pack2(smul(sr, g2), smul(-si, g2));
    *(u16*)(wp + t * HIS) = (u16)pk;
    *(u16*)(wp + t * HIS + 80) = (u16)(pk >> 16);
  }
  asm volatile("s_waitcnt lgkmcnt(0)" ::: "memory");
  lds_inc(smem, M_SWR, lane);
  lds_wait_ge(misc + M_SWR, 3u);
#pragma unroll
  for (int k = 0; k < 7; ++k) {
    const int f = u + 3 * k;
    if (f < 20) {
      const int pe = f >> 1, part = pe / 5, e = pe % 5;
      const int t = 64 * (f & 1) + lane, pi = 5 * jl + e, step = 12 + 2 * (pi >> 2) + part;
      const u32x4 v = *(const u32x4*)(smem + L_HI + t * HIS + part * 80 + e * 16);
      st128(rW, OFF_AF + (((rc * 4 + cg) * 22 + step) * 8 + (t >> 4)) * 1024 + ((pi & 3) * 16 + (t & 15)) * 16, v);
    }
  }
  WAITV(0);
  if (lds_inc(smem, M_PUB, lane) == 2u) {
    if (lane == 0) set_flag(rW, OFF_FA + (rc * 4 + cg) * 128 + 32 + jl * 8, tlo, thi);
    poll_flags(rW, OFF_FA + (rc * 4 + cg) * 128 + 32, 4, tlo, thi, lane);
    if (lane == 0) misc[M_HOK] = 1u;
  }
  __builtin_amdgcn_s_setprio(0);
}

__global__ __launch_bounds__(512, 2) void fused(Args a) {
  __shared__ __attribute__((aligned(16))) char smem[LDS_BYTES];
  const int tid = threadIdx.x, lane = tid & 63;
  const int wave = __builtin_amdgcn_readfirstlane(tid >> 6);
  const int b = blockIdx.x, grp = b & 7, idx = b >> 3;
  const int cg = grp & 3, jl = idx & 3;
  const int rc = 8 * (grp >> 2) + (idx >> 2), j = 4 * cg + jl;
  const int r16 = lane & 15, g = lane >> 4;
  char* ws = a.ws;
  const RSRC rW = __builtin_amdgcn_make_buffer_rsrc((void*)ws, 0, WS_BYTES, 0x00020000);
  volatile LAS unsigned* misc = (volatile LAS unsigned*)(smem + SP_MISC);
  const unsigned sbase = (unsigned)(size_t)(LAS char*)smem;
  STAMP_DECL
  STAMP(0);

  if (wave >= 4) {
    const int lw = wave - 4;
    {
      unsigned voff[10]; const char* sb_[10]; int inc[10]; unsigned dsto[10];
#pragma unroll
      for (int i = 0; i < 10; ++i) {
        int p = lw + 4 * i; p = p >= 38 ? p - 38 : p;
        dsto[i] = sbase + p * 1024;
        const int rw = lane >> 3;
        const unsigned ox = (8 * p + rw) * (D_ * 2) + 16 * ((lane & 7) ^ (((8 * p + rw) >> 1) & 7));
        const int pb = p < 21 ? p - 16 : p - 21;
        const unsigned ob = (8 * pb + rw) * (D_ * 2) + 16 * ((lane & 7) ^ ((((rw >> 1) & 3) << 1) | (p >= 21 ? 1 : 0)));
        const int ci = 64 * (p >= 26 ? p - 26 : 0) + lane, k = ci / 12, cp = ci % 12;
        int cc = cp - 2 * ((k >> 3) & 1); cc = cc < 0 ? cc + 12 : cc;
        const unsigned ow = k * (F_ * 2) + 16 * cc;
        voff[i] = p < 16 ? ox : p < 26 ? ob : ow;
        inc[i] = p < 26 ? 128 : 64 * F_ * 2;
        sb_[i] = (const char*)(p < 16 ? a.x + (size_t)128 * rc * D_ : p < 21 ? a.Bre + (size_t)40 * j * D_ : p < 26 ? a.Bim + (size_t)40 * j * D_ : a.w1 + 96 * j);
      }
      auto stage = [&](unsigned boff) {
#pragma unroll
        for (int i = 0; i < 10; ++i) { gldss(voff[i], sb_[i], dsto[i] + boff); sb_[i] += inc[i]; }
      };
      stage(0); stage(ST_SZ);
      STAMP(1);
      LBAR(10);
      STAMP(2);
      stage(2 * ST_SZ);
      LBAR(10);
      stage(3 * ST_SZ); stage(0);
#pragma unroll 1
      for (int s = 2; s <= 6; ++s) {
        LBAR(20);
        stage(((s + 3) & 3) * ST_SZ);
      }
      LBAR(20); LBAR(10); LBAR(0);
    }
    STAMP(3);
    const unsigned tlo = misc[M_TAG], thi = misc[M_TAG + 1];

    unsigned woff[10], coff[10];
#pragma unroll
    for (int i = 0; i < 10; ++i) {
      const int ci = 64 * i + lane;
      const int k = ci / 20, cp = ci % 20;
      int cc = cp - 2 * ((k >> 2) & 1); cc = cc < 0 ? cc + 20 : cc;
      woff[i] = k * (D_ * 2) + 16 * cc;
      const int row = ci >> 2, f4 = (row >> 2) & 3, fx = (f4 == 0 ? 0 : f4 == 1 ? 2 : f4 == 2 ? 3 : 1);
      coff[i] = row * (P_ * 2) + 16 * ((ci & 3) ^ fx);
    }
    const char* wbase2 = (const char*)(a.w2 + (size_t)384 * cg * D_ + 160 * jl);
    const char* abase = ws + OFF_AF + (size_t)(rc * 4 + cg) * 22 * 8192;
    const unsigned aoff = lane * 16;
    auto issueB = [&](int n, unsigned sl) {
      if (n < 12) {
        const char* p = wbase2 + (size_t)n * (32 * D_ * 2);
#pragma unroll
        for (int i = 0; i < 10; ++i) gldss(woff[i], p, sl + 8192 + i * 1024);
      } else {
        const char* p = (const char*)(((n & 1) ? a.Cim : a.Cre) + (size_t)160 * jl * P_ + 160 * cg) + ((n - 12) >> 1) * 64;
#pragma unroll
        for (int i = 0; i < 10; ++i) gldss(coff[i], p, sl + 8192 + i * 1024);
      }
    };
    auto issueA = [&](int n, unsigned sl) {
#pragma unroll
      for (int i = 0; i < 8; ++i) gldssc(aoff, abase + (size_t)n * 8192 + i * 1024, sl + i * 1024);
    };
    int p2 = -1;
    if (lw != 1) {
      scan_task(rW, smem, misc, lw == 0 ? 2 : lw - 2, lane, rc, cg, jl, tlo, thi);
      lds_wait_ge(misc + M_FULL, 1u);
      STAMP(6);
    } else {
      lds_wait_ge(misc + M_P1D, 4u);
#pragma unroll 1
      for (int k = 0; k < 6; ++k) issueB(k, sbase + k * SLOT);
      STAMP(4);
      poll_flags(rW, OFF_FA + (rc * 4 + cg) * 128, 4, tlo, thi, lane);
      STAMP(5);
#pragma unroll 1
      for (int k = 0; k < 6; ++k) issueA(k, sbase + k * SLOT);
      WAITV(40); if (lane == 0) misc[M_FULL] = 1u;
      WAITV(32); if (lane == 0) misc[M_FULL + 1] = 1u;
      WAITV(24); if (lane == 0) misc[M_FULL + 2] = 1u;
      WAITV(16); if (lane == 0) misc[M_FULL + 3] = 1u;
      WAITV(8);  if (lane == 0) misc[M_FULL + 4] = 1u;
      p2 = 5;
      lds_wait_ge(misc + M_SRD, 3u);
    }
#pragma unroll 1
    for (;;) {
      const int n = (int)lds_inc(smem, M_NEXT, lane);
      if (n >= 22) break;
      const unsigned need = n - NSLOT + 1;
      bool ok = n < NSLOT || (misc[M_CONS] >= need && misc[M_CONS + 1] >= need && misc[M_CONS + 2] >= need && misc[M_CONS + 3] >= need);
      ok = ok && (n < 12 || misc[M_HOK] != 0u);
      if (!ok) {
        WAITV(0);
        if (lane == 0 && p2 >= 0) misc[M_FULL + p2] = 1u;
        p2 = -1;
        if (n >= NSLOT) {
          lds_wait_ge(misc + M_CONS, need); lds_wait_ge(misc + M_CONS + 1, need); lds_wait_ge(misc + M_CONS + 2, need); lds_wait_ge(misc + M_CONS + 3, need);
        }
      }
      asm volatile("" ::: "memory");
      const unsigned sl = sbase + (n % NSLOT) * SLOT;
      issueB(n, sl);
      if (n >= 12) lds_wait_ge(misc + M_HOK, 1u);
      issueA(n, sl);
      WAITV(18);
      if (lane == 0 && p2 >= 0) misc[M_FULL + p2] = 1u;
      p2 = n;
    }
    WAITV(0);  if (lane == 0 && p2 >= 0) misc[M_FULL + p2] = 1u;
    STAMP(8);
    const int tl = 16 * (lw & 1) + r16, n0 = 160 * jl + 80 * (lw >> 1) + 4 * g;
    const size_t orow = (size_t)(128 * rc + 32 * cg + tl) * D_ + n0;
    f32x4 sk[5];
    {
      const float gt = 0.5f * *(const float*)(smem + SP_G + (32 * cg + tl) * 4);
#pragma unroll
      for (int i = 0; i < 5; ++i) {
        u32x2 xv = *(const u32x2*)(a.x + orow + 16 * i);
        u32x2 dv = *(const u32x2*)(a.Dsk + n0 + 16 * i);
        sk[i][0] = gt * bflo(xv[0]) * bflo(dv[0]); sk[i][1] = gt * bfhi(xv[0]) * bfhi(dv[0]);
        sk[i][2] = gt * bflo(xv[1]) * bflo(dv[1]); sk[i][3] = gt * bfhi(xv[1]) * bfhi(dv[1]);
      }
    }
    STAMP(22);
    for (int sp = 0; misc[M_RED] < 4u && sp < SPIN_LDS; ++sp) __builtin_amdgcn_s_sleep(4);
    asm volatile("" ::: "memory");
    STAMP(23);
    poll_flags(rW, OFF_FR + (rc * 4 + jl) * 128, 4, tlo, thi, lane);
    STAMP(24);
    if (rc == 15 && j == 0 && tid == 256) set_flag(rW, OFF_EPOCH, tlo, thi);
    const RSRC rO = __builtin_amdgcn_make_buffer_rsrc((void*)a.out, 0, T_ * D_ * 4, 0x00020000);
    u32x2 pv[3][5];
#pragma unroll
    for (int s = 0; s < 3; ++s) {
      const int sc = (cg + 1 + s) & 3;
#pragma unroll
      for (int i = 0; i < 5; ++i)
        pv[s][i] = __builtin_amdgcn_raw_buffer_load_b64(rW, OFF_PB + (((rc * 4 + jl) * 4 + cg) * 4 + sc) * 10240 + (lw >> 1) * 5120 + i * 1024 + lane * 16 + (lw & 1) * 8, 0, SC1);
    }
#pragma unroll
    for (int i = 0; i < 5; ++i) {
      f32x4 v = *(const f32x4*)(smem + (5 * lw + i) * 1024 + lane * 16) + sk[i];
#pragma unroll
      for (int s = 0; s < 3; ++s) v += (f32x4){bflo(pv[s][i][0]), bfhi(pv[s][i][0]), bflo(pv[s][i][1]), bfhi(pv[s][i][1])};
      __builtin_amdgcn_raw_buffer_store_b128(__builtin_bit_cast(u32x4, v), rO, (unsigned)(orow + 16 * i) * 4, 0, 17);
    }
  } else {
    if (wave == 3) {
      unsigned long long pc; unsigned back, csz;
      asm volatile("s_getpc_b64 %0\n1:\n\ts_mov_b32 %1, 1b-_Z5fused4Args\n\ts_mov_b32 %2, _Z8code_endv-_Z5fused4Args" : "=s"(pc), "=s"(back), "=s"(csz));
      const unsigned co = (idx * 14 + (lane < 14 ? lane : 0)) * 128;
      touch((const char*)(pc - back) + (co + 4 <= csz ? co : 0u), sbase + SP_EL);
      touch((const char*)a.w1 + (size_t)(b * 60 + (lane < 60 ? lane : 0)) * 128, sbase + SP_EL);
      touch((const char*)(lane < 32 ? a.Bre : a.Bim) + (size_t)(b * 25 + ((lane & 31) < 25 ? (lane & 31) : 0)) * 128, sbase + SP_EL);
    } else if (wave == 0) {
      touch((const char*)a.x + (size_t)(b * 80 + lane) * 128, sbase + SP_EL);
      touch((const char*)a.x + (size_t)(b * 80 + 64 + r16) * 128, sbase + SP_EL);
      touch((const char*)a.w2 + (size_t)(b * 60 + (lane < 60 ? lane : 0)) * 128, sbase + SP_EL);
      touch((const char*)(lane < 32 ? a.Cre : a.Cim) + (size_t)(b * 25 + ((lane & 31) < 25 ? (lane & 31) : 0)) * 128, sbase + SP_EL);
    }
    if (wave == 1) {
      const u64 ep = __hip_atomic_load((GAS u64*)(ws + OFF_EPOCH), __ATOMIC_RELAXED, __HIP_MEMORY_SCOPE_AGENT);
      const unsigned v = *(const unsigned*)(a.b1 + 96 * j + 2 * (lane < 48 ? lane : 0));
      const u32x4 g0 = *(const u32x4*)(a.gw + 8 * lane), g1 = *(const u32x4*)(a.gw + 8 * (64 + r16));
      if (lane < 48) *(f32x2*)(smem + SP_B1 + lane * 8) = (f32x2){bflo(v), bfhi(v)};
      *(u32x4*)(smem + SP_GW + lane * 16) = g0;
      if (lane < 16) *(u32x4*)(smem + SP_GW + 1024 + lane * 16) = g1;
      if (lane < 24 || lane >= 32) misc[lane] = lane == M_NEXT ? 6u : 0u;
      if (lane == 0) { const u64 tg = ep + 1; misc[M_TAG] = (unsigned)tg; misc[M_TAG + 1] = (unsigned)(tg >> 32); }
    } else if (wave == 2) {
      const int ch = 40 * j + (lane < 40 ? lane : 0);
      const float lre = bflo(a.Lre[ch]), lim = bflo(a.Lim[ch]), dt = expf(bflo(a.ldt[ch]));
      float e = expf(dt * lre), sn, cs;
      sincosf(dt * lim, &sn, &cs);
      f32x2 l1 = {e * cs, e * sn};
      const float nr = l1[0] - 1.f, ni = l1[1], den = lre * lre + lim * lim;
      f32x2 cf = {(nr * lre + ni * lim) / den, (ni * lre - nr * lim) / den};
      f32x2 l2 = cm2(l1, l1), l4 = cm2(l2, l2), l8 = cm2(l4, l4), l12 = cm2(l8, l4), l16 = cm2(l8, l8);
      f32x2 l32 = cm2(l16, l16), l64 = cm2(l32, l32), l128 = cm2(l64, l64), l96 = cm2(l64, l32);
      if (lane < 40) {
        float* pp = (float*)(smem + SP_PAR + lane * PARS);
        *(f32x4*)(pp) = (f32x4){l1[0], l1[1], l16[0], l16[1]};
        *(f32x4*)(pp + 4) = (f32x4){cf[0], cf[1], l32[0], l32[1]};
        *(f32x4*)(pp + 8) = (f32x4){l12[0], l12[1], l8[0], l8[1]};
        *(f32x4*)(pp + 12) = (f32x4){l4[0], l4[1], 1.f, 0.f};
        *(f32x4*)(pp + 16) = (f32x4){l64[0], l64[1], l96[0], l96[1]};
        *(f32x2*)(pp + 20) = l128;
      }
    }
    STAMP(1);
    const int th = wave & 1;
    const unsigned xo0 = ST_X + (64 * th + r16) * 128 + ((g ^ (r16 >> 1)) * 16);
    const unsigned xo1 = xo0 ^ 64;
    const int q4 = r16 >> 2, p4 = r16 & 3;

#define P1_PIPE(LOADF, STEP, MMA, INIT)                                             \
    CBAR;                                                                           \
    __builtin_amdgcn_s_setprio(3);                                                  \
    STAMP(2);                                                                       \
    INIT;                                                                           \
    LOADF(F0, 0u, 0);                                                               \
    STEP(F0, F1, 0u, 1);                                                            \
    _Pragma("unroll 1") for (int s = 1; s < 10; ++s) {                              \
      CBAR;                                                                         \
      STEP(F1, F0, (s & 3) * ST_SZ, 0);                                             \
      STEP(F0, F1, (s & 3) * ST_SZ, 1);                                             \
    }                                                                               \
    asm volatile("s_waitcnt lgkmcnt(0)" ::: "memory");                              \
    lds_inc(smem, M_P1D, lane);                                                     \
    MMA(F1); SCHED; __builtin_amdgcn_s_setprio(0);

    unsigned tlo, thi;
    if (wave < 2) {
      f32x4 acc[6][4];
      const unsigned wbase = ST_W1 + (8 * g + q4) * 192 + (p4 >> 1) * 16 + (p4 & 1) * 8;
      const unsigned wa = wbase + (g & 1) * 32;
      const unsigned wb = wbase + ((g & 1) ? 0 : 160);
      struct Frag { bf16x8 x[4], w[6]; };
      Frag F0, F1;
      auto rdw = [&](const char* sb, int ft, int kk) {
        unsigned ad = (ft < 5 ? wa + 32 * ft : wb) + kk * 32 * 192;
        s16x4 lo = __builtin_amdgcn_ds_read_tr16_b64_v4i16((LAS s16x4*)(sb + ad));
        s16x4 hi = __builtin_amdgcn_ds_read_tr16_b64_v4i16((LAS s16x4*)(sb + ad + 4 * 192));
        return __builtin_bit_cast(bf16x8, __builtin_shufflevector(lo, hi, 0, 1, 2, 3, 4, 5, 6, 7));
      };
      auto loadf = [&](Frag& F, unsigned boff, int kk) {
        const char* sb = smem + boff;
#pragma unroll
        for (int tt = 0; tt < 4; ++tt) F.x[tt] = *(const bf16x8*)(sb + (kk ? xo1 : xo0) + tt * 2048);
#pragma unroll
        for (int ft = 0; ft < 6; ++ft) F.w[ft] = rdw(sb, ft, kk);
      };
      auto mma = [&](Frag& F) {
#pragma unroll
        for (int m = 0; m < 24; ++m)
          acc[m >> 2][m & 3] = __builtin_amdgcn_mfma_f32_16x16x32_bf16(F.w[m >> 2], F.x[m & 3], acc[m >> 2][m & 3], 0, 0, 0);
      };
      auto step = [&](Frag& C, Frag& N, unsigned boff, const int kk) {
        const char* sb = smem + boff;
#pragma unroll
        for (int m = 0; m < 24; ++m) {
          SCHED;
          acc[m >> 2][m & 3] = __builtin_amdgcn_mfma_f32_16x16x32_bf16(C.w[m >> 2], C.x[m & 3], acc[m >> 2][m & 3], 0, 0, 0);
          SCHED;
          if (m < 4) N.x[m] = *(const bf16x8*)(sb + (kk ? xo1 : xo0) + m * 2048);
          if (m >= 4 && m < 10) N.w[m - 4] = rdw(sb, m - 4, kk);
        }
        SCHED;
      };
      auto init = [&]() {
#pragma unroll
        for (int ft = 0; ft < 6; ++ft) {
          f32x4 bv = *(const f32x4*)(smem + SP_B1 + (16 * ft + 4 * g) * 4);
#pragma unroll
          for (int tt = 0; tt < 4; ++tt) acc[ft][tt] = bv;
        }
      };
      P1_PIPE(loadf, step, mma, init());
      STAMP(3);
      tlo = misc[M_TAG]; thi = misc[M_TAG + 1];
      lds_wait_ge(misc + M_P1D, 4u);
#pragma unroll
      for (int t2 = 0; t2 < 2; ++t2)
#pragma unroll
        for (int i = 0; i < 6; ++i) *(f32x4*)(smem + (2 * th + t2) * SLOT + i * 1024 + lane * 16) = acc[i][2 + t2];
      asm volatile("s_waitcnt lgkmcnt(0)" ::: "memory");
      if (lane == 0) misc[M_OFF + th] = 1u;
      lds_wait_ge(misc + M_GRDY, 2u);
#pragma unroll
      for (int tt = 0; tt < 2; ++tt) {
        const float og = 1.f - 0.5f * *(const float*)(smem + SP_G + (64 * th + 16 * tt + r16) * 4);
#pragma unroll
        for (int fp = 0; fp < 3; ++fp)
          st128(rW, OFF_AF + (((rc * 4 + cg) * 22 + 3 * jl + fp) * 8 + 4 * th + tt) * 1024 + lane * 16,
                mish_unit(acc[2 * fp][tt], acc[2 * fp + 1][tt], og));
      }
      STAMP(4);
      WAITV(0);
      if (lds_inc(smem, M_ACT, lane) == 3u && lane == 0) set_flag(rW, OFF_FA + (rc * 4 + cg) * 128 + jl * 8, tlo, thi);
      STAMP(5);
    } else {
      f32x4 acc[4][5], gac[4];
      const unsigned bo0 = ST_BM + (40 * (r16 & 1) + (r16 >> 1)) * 128 + ((g ^ ((((r16 >> 2) & 3) << 1) | (r16 & 1))) * 16);
      const unsigned bo1 = bo0 ^ 64;
      struct Frag { bf16x8 x[4], w[5], gf; };
      Frag F0, F1;
      int kc = 0;
      auto loadf = [&](Frag& F, unsigned boff, int kk) {
        const char* sb = smem + boff;
#pragma unroll
        for (int tt = 0; tt < 4; ++tt) F.x[tt] = *(const bf16x8*)(sb + (kk ? xo1 : xo0) + tt * 2048);
#pragma unroll
        for (int nt = 0; nt < 5; ++nt) F.w[nt] = *(const bf16x8*)(sb + (kk ? bo1 : bo0) + nt * 1024);
        F.gf = *(const bf16x8*)(smem + SP_GW + kc * 64 + g * 16);
        ++kc;
      };
      auto mma = [&](Frag& F) {
#pragma unroll
        for (int tt = 0; tt < 4; ++tt) gac[tt] = __builtin_amdgcn_mfma_f32_16x16x32_bf16(F.x[tt], F.gf, gac[tt], 0, 0, 0);
#pragma unroll
        for (int m = 0; m < 20; ++m)
          acc[m & 3][m >> 2] = __builtin_amdgcn_mfma_f32_16x16x32_bf16(F.x[m & 3], F.w[m >> 2], acc[m & 3][m >> 2], 0, 0, 0);
      };
      auto step = [&](Frag& C, Frag& N, unsigned boff, const int kk) {
        const char* sb = smem + boff;
#pragma unroll
        for (int m = 0; m < 24; ++m) {
          SCHED;
          if (m < 20) acc[m & 3][m >> 2] = __builtin_amdgcn_mfma_f32_16x16x32_bf16(C.x[m & 3], C.w[m >> 2], acc[m & 3][m >> 2], 0, 0, 0);
          else gac[m & 3] = __builtin_amdgcn_mfma_f32_16x16x32_bf16(C.x[m & 3], C.gf, gac[m & 3], 0, 0, 0);
          SCHED;
          if (m < 4) N.x[m] = *(const bf16x8*)(sb + (kk ? xo1 : xo0) + m * 2048);
          if (m >= 4 && m < 9) N.w[m - 4] = *(const bf16x8*)(sb + (kk ? bo1 : bo0) + (m - 4) * 1024);
          if (m == 9) N.gf = *(const bf16x8*)(smem + SP_GW + kc * 64 + g * 16);
        }
        ++kc;
        SCHED;
      };
      auto init = [&]() {
#pragma unroll
        for (int tt = 0; tt < 4; ++tt) {
          gac[tt] = (f32x4){0.f, 0.f, 0.f, 0.f};
#pragma unroll
          for (int nt = 0; nt < 5; ++nt) acc[tt][nt] = (f32x4){0.f, 0.f, 0.f, 0.f};
        }
      };
      P1_PIPE(loadf, step, mma, init());
      STAMP(3);
      tlo = misc[M_TAG]; thi = misc[M_TAG + 1];
      {
        char* bw = smem + L_BU + r16 * BUS + (64 * th + 4 * g) * 4;
#pragma unroll
        for (int tt = 0; tt < 4; ++tt)
#pragma unroll
          for (int nt = 0; nt < 5; ++nt) *(f32x4*)(bw + nt * 16 * BUS + tt * 64) = acc[tt][nt];
        asm volatile("s_waitcnt lgkmcnt(0)" ::: "memory");
        lds_inc(smem, M_BUW, lane);
        f32x4 lg = r16 == 0 ? gac[0] : r16 == 1 ? gac[1] : r16 == 2 ? gac[2] : gac[3];
        f32x4 gg;
#pragma unroll
        for (int r = 0; r < 4; ++r) gg[r] = 2.f * __builtin_amdgcn_rcpf(1.f + __expf(-lg[r]));
        if (r16 < 4) *(f32x4*)(smem + SP_G + (64 * th + 16 * r16 + 4 * g) * 4) = gg;
        asm volatile("s_waitcnt lgkmcnt(0)" ::: "memory");
        lds_inc(smem, M_GRDY, lane);
      }
      STAMP(4);
      lds_wait_ge(misc + M_OFF + th, 1u);
#pragma unroll
      for (int t2 = 0; t2 < 2; ++t2) {
        const float og = 1.f - 0.5f * *(const float*)(smem + SP_G + (64 * th + 32 + 16 * t2 + r16) * 4);
#pragma unroll
        for (int fp = 0; fp < 3; ++fp) {
          const char* sp = smem + (2 * th + t2) * SLOT + fp * 2048 + lane * 16;
          st128(rW, OFF_AF + (((rc * 4 + cg) * 22 + 3 * jl + fp) * 8 + 4 * th + 2 + t2) * 1024 + lane * 16,
                mish_unit(*(const f32x4*)sp, *(const f32x4*)(sp + 1024), og));
        }
      }
      WAITV(0);
      if (lds_inc(smem, M_ACT, lane) == 3u && lane == 0) set_flag(rW, OFF_FA + (rc * 4 + cg) * 128 + jl * 8, tlo, thi);
      STAMP(5);
    }

    const int wn = wave & 1, wt = wave >> 1;
    f32x4 acc[5][4];
#pragma unroll
    for (int nt = 0; nt < 5; ++nt)
#pragma unroll
      for (int tt = 0; tt < 4; ++tt) acc[nt][tt] = (f32x4){0.f, 0.f, 0.f, 0.f};
    struct Frag2 { bf16x8 x[4], w[5]; };
    Frag2 G0, G1;
    const unsigned ao = 4 * wt * 1024 + lane * 16;
    const unsigned twb = 8192 + (4 * g + q4) * 320 + (p4 >> 1) * 16 + (p4 & 1) * 8;
    const unsigned twa = twb + (5 * wn + (g & 1)) * 32;
    const unsigned twl = (wn == 1 && (g & 1)) ? twb : twa + 128;
    const int f4 = (r16 >> 2) & 3, fx = (f4 == 0 ? 0 : f4 == 1 ? 2 : f4 == 2 ? 3 : 1);
    const unsigned cwo = 8192 + (80 * wn + r16) * 64 + ((g ^ fx) * 16);
    auto rd2 = [&](Frag2& N, const char* sb, int e, const bool ssm) {
      if (e < 4) N.x[e] = *(const bf16x8*)(sb + ao + e * 1024);
      else if (ssm) N.w[e - 4] = *(const bf16x8*)(sb + cwo + (e - 4) * 1024);
      else {
        const unsigned ad = e - 4 < 4 ? twa + 32 * (e - 4) : twl;
        s16x4 lo = __builtin_amdgcn_ds_read_tr16_b64_v4i16((LAS s16x4*)(sb + ad));
        s16x4 hi = __builtin_amdgcn_ds_read_tr16_b64_v4i16((LAS s16x4*)(sb + ad + 16 * 320));
        N.w[e - 4] = __builtin_bit_cast(bf16x8, __builtin_shufflevector(lo, hi, 0, 1, 2, 3, 4, 5, 6, 7));
      }
    };
    auto wait_full = [&](int st) { lds_wait_ge(misc + M_FULL + st, 1u); };
    int slot = 0;
    unsigned fpre = 0;
    auto step2 = [&](Frag2& C, Frag2& N, int n, const bool next_ssm, const bool last) {
      if (!last && fpre == 0u) wait_full(n + 1);
      asm volatile("s_waitcnt lgkmcnt(0)" ::: "memory");
      const char* sb = smem + slot * SLOT;
      slot = slot == NSLOT - 1 ? 0 : slot + 1;
#pragma unroll
      for (int m = 0; m < 20; ++m) {
        SCHED;
        acc[m >> 2][m & 3] = __builtin_amdgcn_mfma_f32_16x16x32_bf16(C.w[m >> 2], C.x[m & 3], acc[m >> 2][m & 3], 0, 0, 0);
        SCHED;
        if (!last && m < 9) rd2(N, sb, m, next_ssm);
        if (m == 9) { if (lane == 0) misc[M_CONS + wave] = n + 1; }
        if (m == 12) fpre = misc[M_FULL + n + 2];
      }
      SCHED;
    };
    wait_full(0);
    __builtin_amdgcn_s_setprio(1);
    STAMP(6);
#pragma unroll
    for (int e = 0; e < 9; ++e) rd2(G0, smem, e, false);
    slot = 1;
#pragma unroll 1
    for (int n = 0; n < 10; n += 2) { step2(G0, G1, n, false, false); step2(G1, G0, n + 1, false, false); }
    STAMP(7);
    step2(G0, G1, 10, false, false); step2(G1, G0, 11, true, false);
    STAMP(8);
#pragma unroll 1
    for (int n = 12; n < 20; n += 2) { step2(G0, G1, n, true, false); step2(G1, G0, n + 1, true, false); }
    step2(G0, G1, 20, true, false); step2(G1, G0, 21, true, true);
    __builtin_amdgcn_s_setprio(0);
    STAMP(9);
#pragma unroll
    for (int t2 = 0; t2 < 2; ++t2) {
      const int owner = 2 * wt + t2;
      if (owner != cg) {
        const unsigned pb = OFF_PB + (((rc * 4 + jl) * 4 + owner) * 4 + cg) * 10240 + wn * 5120 + lane * 16;
#pragma unroll
        for (int nt = 0; nt < 5; ++nt) {
          const f32x4 v0 = acc[nt][2 * t2], v1 = acc[nt][2 * t2 + 1];
          u32x4 o; o[0] = pack2(v0[0], v0[1]); o[1] = pack2(v0[2], v0[3]); o[2] = pack2(v1[0], v1[1]); o[3] = pack2(v1[2], v1[3]);
          st128(rW, pb + nt * 1024, o);
        }
      }
    }
    lds_wait_ge(misc + M_CONS, 22u); lds_wait_ge(misc + M_CONS + 1, 22u); lds_wait_ge(misc + M_CONS + 2, 22u); lds_wait_ge(misc + M_CONS + 3, 22u);
#pragma unroll
    for (int tt = 0; tt < 4; ++tt) {
      if (2 * wt + (tt >> 1) == cg) {
#pragma unroll
        for (int nt = 0; nt < 5; ++nt) *(f32x4*)(smem + (5 * (2 * wn + (tt & 1)) + nt) * 1024 + lane * 16) = acc[nt][tt];
      }
    }
    STAMP(10);
    asm volatile("s_waitcnt vmcnt(0) lgkmcnt(0)" ::: "memory");
    if (lds_inc(smem, M_RED + 1, lane) == 3u) {
      if (lane == 0) { set_flag(rW, OFF_FR + (rc * 4 + jl) * 128 + cg * 8, tlo, thi); misc[M_RED] = 4u; }
    }
    STAMP(11);
  }
  STAMP(25);
  STAMP(26);
  STAMP_OUT(0);
}

__global__ void code_end() {}

extern "C" void kernel_launch(void* const* d_in, const int* in_sizes, int n_in,
                              void* d_out, int out_size, void* d_ws, size_t ws_size,
                              hipStream_t stream) {
  Args a;
  a.x   = (const u16*)d_in[0];
  a.Lre = (const u16*)d_in[1];
  a.Lim = (const u16*)d_in[2];
  a.Bre = (const u16*)d_in[3];
  a.Bim = (const u16*)d_in[4];
  a.Cre = (const u16*)d_in[5];
  a.Cim = (const u16*)d_in[6];
  a.ldt = (const u16*)d_in[7];
  a.Dsk = (const u16*)d_in[8];
  a.w1  = (const u16*)d_in[10];
  a.b1  = (const u16*)d_in[11];
  a.w2  = (const u16*)d_in[12];
  a.gw  = (const u16*)d_in[13];
  a.ws  = (char*)d_ws;
  a.out = (float*)d_out;
  fused<<<256, 512, 0, stream>>>(a);
}
```

```cpp
#include <hip/hip_runtime.h>
#include <hip/hip_bf16.h>
#include <stdint.h>
#pragma clang diagnostic ignored "-Winline-asm"

typedef __attribute__((ext_vector_type(8))) __bf16 bf16x8;
typedef __attribute__((ext_vector_type(2))) __bf16 bf16x2;
typedef __attribute__((ext_vector_type(4))) float f32x4;
typedef __attribute__((ext_vector_type(2))) float f32x2;
typedef __attribute__((ext_vector_type(4))) unsigned u32x4;
typedef __attribute__((ext_vector_type(2))) unsigned u32x2;
typedef __attribute__((ext_vector_type(4))) short s16x4;
typedef unsigned short u16;
#define LAS __attribute__((address_space(3)))
#define GAS __attribute__((address_space(1)))
#define DI __device__ __forceinline__

constexpr int T_ = 2048, D_ = 640, P_ = 640, F_ = 1536;

constexpr unsigned OFF_AF = 0;
constexpr unsigned OFF_PB = OFF_AF + 64u * 22 * 8192;
constexpr unsigned OFF_E  = OFF_PB + 64u * 16 * 10240;
constexpr unsigned OFF_FA = OFF_E + 256u * 640;
constexpr unsigned OFF_FE = OFF_FA + 64 * 128;
constexpr unsigned OFF_FR = OFF_FE + 16 * 128;
constexpr unsigned OFF_EPOCH = OFF_FR + 64 * 128;
constexpr unsigned WS_BYTES = OFF_EPOCH + 256;

#define STAMP(i)
#define STAMP_DECL
#define SCAN_ARG
#define SCAN_PASS
#define STAMP_OUT(kid)

DI float bflo(unsigned v) { return __uint_as_float(v << 16); }
DI float bfhi(unsigned v) { return __uint_as_float(v & 0xffff0000u); }
DI unsigned pack2(float a, float b) {
  bf16x2 r; r[0] = (__bf16)a; r[1] = (__bf16)b; return __builtin_bit_cast(unsigned, r);
}
DI float dswap(float v) {
  return __int_as_float(__builtin_amdgcn_mov_dpp(__float_as_int(v), 0xB1, 0xf, 0xf, true));
}
DI float cmul(float lr, float ls, float v) { return fmaf(lr, v, ls * dswap(v)); }
DI float cmuladd(float lr, float ls, float v, float a) { return fmaf(lr, v, fmaf(ls, dswap(v), a)); }
DI f32x2 cm2(f32x2 a, f32x2 b) { return (f32x2){a[0] * b[0] - a[1] * b[1], a[0] * b[1] + a[1] * b[0]}; }
DI float sfma(float a, float b, float c) { float d; asm("v_fma_f32 %0, %1, %2, %3" : "=v"(d) : "v"(a), "v"(b), "v"(c)); return d; }
DI float smul(float a, float b) { float d; asm("v_mul_f32 %0, %1, %2" : "=v"(d) : "v"(a), "v"(b)); return d; }

DI f32x2 mish2(f32x2 h) {
  f32x2 z = h * 1.44269504088896341f;
  f32x2 w; w[0] = __builtin_amdgcn_exp2f(fminf(z[0], 43.f)); w[1] = __builtin_amdgcn_exp2f(fminf(z[1], 43.f));
  f32x2 n = w * (w + 2.f);
  f32x2 d = n + 2.f;
  f32x2 r; r[0] = __builtin_amdgcn_rcpf(d[0]); r[1] = __builtin_amdgcn_rcpf(d[1]);
  return (h * n) * r;
}

DI void glds16(const void* gsrc, unsigned lds_dst) {
  unsigned keep;
  asm volatile("s_mov_b32 %0, m0\n\ts_mov_b32 m0, %2\n\ts_nop 0\n\tglobal_load_lds_dwordx4 %1, off\n\ts_mov_b32 m0, %0"
               : "=&s"(keep) : "v"(gsrc), "s"(lds_dst) : "memory");
}
DI void touch(const void* gsrc, unsigned lds_dst) {
  unsigned keep;
  asm volatile("s_mov_b32 %0, m0\n\ts_mov_b32 m0, %2\n\ts_nop 0\n\tglobal_load_lds_dword %1, off\n\ts_mov_b32 m0, %0"
               : "=&s"(keep) : "v"(gsrc), "s"(lds_dst) : "memory");
}
DI float sum16(float v) {
  auto r = __builtin_amdgcn_permlane16_swap(__float_as_uint(v), __float_as_uint(v), false, false);
  return __uint_as_float(r[0]) + __uint_as_float(r[1]);
}
DI float sum32(float v) {
  auto r = __builtin_amdgcn_permlane32_swap(__float_as_uint(v), __float_as_uint(v), false, false);
  return __uint_as_float(r[0]) + __uint_as_float(r[1]);
}

typedef unsigned long long u64;
#define SC1 16
#define SCHED __builtin_amdgcn_sched_barrier(0)
#define RSRC __amdgpu_buffer_rsrc_t
DI void st128(RSRC r, unsigned off, u32x4 v) { __builtin_amdgcn_raw_buffer_store_b128(v, r, off, 0, SC1); }
DI void st128f(RSRC r, unsigned off, f32x4 v) { __builtin_amdgcn_raw_buffer_store_b128(__builtin_bit_cast(u32x4, v), r, off, 0, SC1); }
DI void st64(RSRC r, unsigned off, u32x2 v) { __builtin_amdgcn_raw_buffer_store_b64(v, r, off, 0, SC1); }
DI f32x4 ld128f(RSRC r, unsigned off) { return __builtin_bit_cast(f32x4, __builtin_amdgcn_raw_buffer_load_b128(r, off, 0, SC1)); }
DI f32x2 ld64f(RSRC r, unsigned off) { return __builtin_bit_cast(f32x2, __builtin_amdgcn_raw_buffer_load_b64(r, off, 0, SC1)); }
DI void glds16c(const void* gsrc, unsigned lds_dst) {
  unsigned keep;
  asm volatile("s_mov_b32 %0, m0\n\ts_mov_b32 m0, %2\n\ts_nop 0\n\tglobal_load_lds_dwordx4 %1, off sc1\n\ts_mov_b32 m0, %0"
               : "=&s"(keep) : "v"(gsrc), "s"(lds_dst) : "memory");
}

DI void gldss(unsigned voff, const void* sbase_, unsigned lds_dst) {
  asm volatile("s_mov_b32 m0, %2\n\ts_nop 0\n\tglobal_load_lds_dwordx4 %0, %1" :: "v"(voff), "s"(sbase_), "s"(lds_dst) : "memory", "m0");
}
DI void gldssc(unsigned voff, const void* sbase_, unsigned lds_dst) {
  asm volatile("s_mov_b32 m0, %2\n\ts_nop 0\n\tglobal_load_lds_dwordx4 %0, %1 sc1" :: "v"(voff), "s"(sbase_), "s"(lds_dst) : "memory", "m0");
}

DI void gld4c(unsigned voff, const void* sbase_, unsigned lds_dst) {
  asm volatile("s_mov_b32 m0, %2\n\ts_nop 0\n\tglobal_load_lds_dwordx4 %0, %1 sc1\n\tglobal_load_lds_dwordx4 %0, %1 offset:1024 sc1\n\t"
               "global_load_lds_dwordx4 %0, %1 offset:2048 sc1\n\tglobal_load_lds_dwordx4 %0, %1 offset:3072 sc1"
               :: "v"(voff), "s"(sbase_), "s"(lds_dst) : "memory", "m0");
}
DI void gld4(unsigned v0, unsigned v1, unsigned v2, unsigned v3, const void* sbase_, unsigned lds_dst) {
  asm volatile("s_mov_b32 m0, %5\n\ts_nop 0\n\tglobal_load_lds_dwordx4 %0, %4\n\tglobal_load_lds_dwordx4 %1, %4 offset:1024\n\t"
               "global_load_lds_dwordx4 %2, %4 offset:2048\n\tglobal_load_lds_dwordx4 %3, %4 offset:3072"
               :: "v"(v0), "v"(v1), "v"(v2), "v"(v3), "s"(sbase_), "s"(lds_dst) : "memory", "m0");
}

constexpr int ST_X = 0, ST_BM = 16384, ST_W1 = 26624, ST_SZ = 38912, NSTG = 4;
constexpr int SP = NSTG * ST_SZ;
constexpr int PARS = 48;
constexpr int SP_PAR = SP, SP_CONS = SP + 1920, SP_FULL = SP + 2944, SP_B1 = SP + 3840, SP_EC = SP + 4224, SP_EL = SP + 5504, SP_GW = SP + 5824,
              SP_G = SP + 7104, SP_MISC = SP + 7616, LDS_BYTES = SP + 7872;
constexpr int BUS = 560, L_BU = SP - 80 * BUS;
constexpr int SLOT = 18432, NSLOT = 8;
enum { M_CNTE = 0, M_GRDY, M_P1D, M_ACT, M_BURDY, M_SRD, M_SWR, M_PUB, M_RED, M_NEXT = 10, M_CONS = 12, M_OFF = 16, M_AOK = 18, M_BUW = 19, M_TAG = 24, M_FULL = 32 };
constexpr int LAG = 3;
constexpr int SPIN_LDS = 1 << 22, SPIN_G = 1 << 19;

struct Args {
  const u16 *x, *Lre, *Lim, *Bre, *Bim, *Cre, *Cim, *ldt, *Dsk, *w1, *b1, *w2, *gw;
  char* ws;
  float* out;
};

#define CBAR do { SCHED; asm volatile("s_waitcnt lgkmcnt(0)\n\ts_barrier" ::: "memory"); SCHED; } while (0)
#define LBAR(n) asm volatile("s_waitcnt vmcnt(" #n ")\n\ts_barrier" ::: "memory")
#define WAITV(n) asm volatile("s_waitcnt vmcnt(" #n ")" ::: "memory")

DI void lds_wait_ge(volatile LAS unsigned* w, unsigned v) {
  for (int sp = 0; *w < v && sp < SPIN_LDS; ++sp) __builtin_amdgcn_s_sleep(1);
  asm volatile("" ::: "memory");
}
DI void cons_wait(volatile LAS unsigned* w, unsigned v) {
  for (int sp = 0; !__all(*w >= v) && sp < SPIN_LDS; ++sp) __builtin_amdgcn_s_sleep(1);
  asm volatile("" ::: "memory");
}
DI unsigned lds_inc(char* smem, int word, int lane) {
  unsigned old = 0;
  asm volatile("; wave converged" ::: "memory");
  if (lane == 0) old = __hip_atomic_fetch_add((LAS unsigned*)(smem + SP_MISC) + word, 1u, __ATOMIC_RELAXED, __HIP_MEMORY_SCOPE_WORKGROUP);
  asm volatile("; wave converged" : "+v"(old) :: "memory");
  return __builtin_amdgcn_readfirstlane(old);
}
DI void poll_flags(RSRC rW, unsigned off, int n, unsigned tlo, unsigned thi, int lane) {
  const unsigned o = off + (lane < n ? lane : 0) * 8;
#pragma unroll 1
  for (int sp = 0; n > 0 && sp < SPIN_G; ++sp) {
    const u32x2 f = __builtin_amdgcn_raw_buffer_load_b64(rW, o, 0, SC1);
    if (__all(f[0] == tlo && f[1] == thi)) break;
    __builtin_amdgcn_s_sleep(1);
    asm volatile("" ::: "memory");
  }
  asm volatile("" ::: "memory");
}
DI void set_flag(RSRC rW, unsigned off, unsigned tlo, unsigned thi) { u32x2 t; t[0] = tlo; t[1] = thi; st64(rW, off, t); }

DI u32x4 mish_unit(f32x4 a0, f32x4 a1, float og) {
  f32x2 m0 = mish2((f32x2){a0[0], a0[1]}) * og, m1 = mish2((f32x2){a0[2], a0[3]}) * og;
  f32x2 m2 = mish2((f32x2){a1[0], a1[1]}) * og, m3 = mish2((f32x2){a1[2], a1[3]}) * og;
  u32x4 o;
  o[0] = pack2(m0[0], m0[1]); o[1] = pack2(m1[0], m1[1]); o[2] = pack2(m2[0], m2[1]); o[3] = pack2(m3[0], m3[1]);
  return o;
}

template <int I> DI float qb(float v) {
  return __int_as_float(__builtin_amdgcn_mov_dpp(__float_as_int(v), I * 0x55, 0xf, 0xf, true));
}
template <class F> DI void scan_task(F&& early, RSRC rW, char* smem, volatile LAS unsigned* misc, int u, int lane, int rc, int cg, int jl, unsigned tlo, unsigned thi SCAN_ARG) {
  const int j = 4 * cg + jl, seg = lane & 3;
  const bool act = 16 * u + (lane >> 2) < 40;
  const int chl = act ? 16 * u + (lane >> 2) : 39;
  const unsigned OOB = 0x7fffffffu;
  const float* pp = (const float*)(smem + SP_PAR + chl * PARS);
  const f32x4 la = *(const f32x4*)pp;
  const f32x4 cl = *(const f32x4*)(pp + 4);
  f32x2 pw = seg == 1 ? (f32x2){cl[2], cl[3]} : *(const f32x2*)(pp + 8 + (seg == 3 ? 2 : 0));
  if (seg == 0) pw = (f32x2){1.f, 0.f};
  const float lr = la[0], li = la[1], nli = -la[1];
  const f32x2 l256 = cm2((f32x2){la[2], la[3]}, (f32x2){la[2], la[3]}), l512 = cm2(l256, l256);
  const int r0 = rc - 16 + 4 * seg;
  unsigned eo[4];
#pragma unroll
  for (int i = 0; i < 4; ++i) eo[i] = r0 + i >= 0 ? OFF_E + ((j * 16 + r0 + i) * 40 + chl) * 16 : OOB;
  const int c = 40 * jl + chl, k = c & 31;
  const unsigned ho = act ? OFF_AF + ((rc * 4 + cg) * 22 + 12 + (c >> 5)) * 8192 + k * 64 + ((seg ^ ((k >> 2) & 2)) * 16) : OOB - 65536;
  STAMP(9);
  lds_wait_ge(misc + M_BUW, 2u);
  STAMP(10);
  __builtin_amdgcn_s_setprio(3);
  f32x4 ar[8], ai[8];
  {
    const char* bp = smem + L_BU + 2 * chl * BUS + seg * 144;
#pragma unroll
    for (int i = 0; i < 8; ++i) { ar[i] = *(const f32x4*)(bp + i * 16); ai[i] = *(const f32x4*)(bp + BUS + i * 16); }
  }
  float sr = 0.f, si = 0.f;
#pragma unroll
  for (int t = 0; t < 32; ++t) {
    const float tr = fmaf(nli, si, ar[t >> 2][t & 3]), ti = fmaf(li, sr, ai[t >> 2][t & 3]);
    sr = fmaf(lr, sr, tr); si = fmaf(lr, si, ti);
  }
  STAMP(11);
  float qr = qb<0>(sr), qi = qb<0>(si);
  qr = seg > 0 ? qr : 0.f; qi = seg > 0 ? qi : 0.f;
  {
    float nr = fmaf(cl[2], qr, fmaf(-cl[3], qi, qb<1>(sr))), ni = fmaf(cl[2], qi, fmaf(cl[3], qr, qb<1>(si)));
    qr = seg > 1 ? nr : qr; qi = seg > 1 ? ni : qi;
    nr = fmaf(cl[2], qr, fmaf(-cl[3], qi, qb<2>(sr))); ni = fmaf(cl[2], qi, fmaf(cl[3], qr, qb<2>(si)));
    qr = seg > 2 ? nr : qr; qi = seg > 2 ? ni : qi;
    const float er = fmaf(cl[2], qr, fmaf(-cl[3], qi, sr)), ei = fmaf(cl[2], qi, fmaf(cl[3], qr, si));
    st128(rW, act && seg == 3 ? OFF_E + ((j * 16 + rc) * 40 + chl) * 16 : OOB, (u32x4){__float_as_uint(er), __float_as_uint(ei), tlo, thi});
  }
  STAMP(13);
  __builtin_amdgcn_s_setprio(0);
  lds_inc(smem, M_SRD, lane);
#pragma unroll
  for (int i = 0; i < 8; ++i)
#pragma unroll
    for (int r = 0; r < 4; ++r) {
      const float xr = fmaf(cl[0], ar[i][r], -cl[1] * ai[i][r]), xi = fmaf(cl[0], ai[i][r], cl[1] * ar[i][r]);
      ar[i][r] = xr; ai[i][r] = xi;
    }
  lds_wait_ge(misc + M_GRDY, 2u);
  f32x4 gv[8];
#pragma unroll
  for (int i = 0; i < 8; ++i) gv[i] = *(const f32x4*)(smem + SP_G + seg * 128 + i * 16);
  if (u == 2) { lds_wait_ge(misc + M_SRD, 3u); early(); }
  STAMP(14);
  {
    u32x4 ev[4];
#pragma unroll 1
    for (int sp = 0; sp < SPIN_G; ++sp) {
      bool ok = true;
#pragma unroll
      for (int i = 0; i < 4; ++i) ev[i] = __builtin_amdgcn_raw_buffer_load_b128(rW, eo[i], 0, SC1);
#pragma unroll
      for (int i = 0; i < 4; ++i) ok = ok && (r0 + i < 0 || (ev[i][2] == tlo && ev[i][3] == thi));
      if (__all(ok)) break;
      __builtin_amdgcn_s_sleep(1);
      asm volatile("" ::: "memory");
    }
    STAMP(15);
    __builtin_amdgcn_s_setprio(3);
    WAITV(0);
    ((volatile LAS unsigned*)(smem + SP_FULL))[u == 2 ? 0 : u + 2] = 8u;
    float cr = __uint_as_float(ev[0][0]), ci = __uint_as_float(ev[0][1]);
#pragma unroll
    for (int i = 1; i < 4; ++i) {
      const float tr = sfma(-la[3], ci, __uint_as_float(ev[i][0])), ti = sfma(la[3], cr, __uint_as_float(ev[i][1]));
      cr = sfma(la[2], cr, tr); ci = sfma(la[2], ci, ti);
    }
    float dr = qb<0>(cr), di = qb<0>(ci), tr, ti;
    tr = sfma(-l512[1], di, qb<1>(cr)); ti = sfma(l512[1], dr, qb<1>(ci)); dr = sfma(l512[0], dr, tr); di = sfma(l512[0], di, ti);
    tr = sfma(-l512[1], di, qb<2>(cr)); ti = sfma(l512[1], dr, qb<2>(ci)); dr = sfma(l512[0], dr, tr); di = sfma(l512[0], di, ti);
    tr = sfma(-l512[1], di, qb<3>(cr)); ti = sfma(l512[1], dr, qb<3>(ci)); dr = sfma(l512[0], dr, tr); di = sfma(l512[0], di, ti);
    tr = sfma(-pw[1], di, qr); ti = sfma(pw[1], dr, qi);
    cr = sfma(pw[0], dr, tr); ci = sfma(pw[0], di, ti);
    sr = sfma(cl[0], cr, smul(-cl[1], ci)); si = sfma(cl[0], ci, smul(cl[1], cr));
  }
  STAMP(16);
#pragma unroll
  for (int i = 0; i < 4; ++i) {
    u32x4 vr, vi;
#pragma unroll
    for (int t2 = 0; t2 < 4; ++t2) {
      float hr[2], hi[2];
#pragma unroll
      for (int e = 0; e < 2; ++e) {
        const int t = 8 * i + 2 * t2 + e;
        const float tr = sfma(nli, si, ar[t >> 2][t & 3]), ti = sfma(li, sr, ai[t >> 2][t & 3]);
        sr = sfma(lr, sr, tr); si = sfma(lr, si, ti);
        hr[e] = smul(sr, gv[t >> 2][t & 3]); hi[e] = smul(-si, gv[t >> 2][t & 3]);
      }
      vr[t2] = pack2(hr[0], hr[1]); vi[t2] = pack2(hi[0], hi[1]);
    }
    __builtin_amdgcn_raw_buffer_store_b128(vr, rW, ho ^ ((i & 1) * 16), i * 2048, SC1);
    __builtin_amdgcn_raw_buffer_store_b128(vi, rW, ho ^ ((i & 1) * 16), i * 2048 + 5 * 8192, SC1);
  }
  STAMP(19);
  WAITV(0);
  STAMP(20);
  if (lds_inc(smem, M_PUB, lane) == 2u && lane == 0) set_flag(rW, OFF_FA + (rc * 4 + cg) * 128 + 32 + jl * 8, tlo, thi);
  __builtin_amdgcn_s_setprio(0);
}

__global__ __launch_bounds__(512, 2) void fused(Args a) {
  __shared__ __attribute__((aligned(16))) char smem[LDS_BYTES];
  const int tid = threadIdx.x, lane = tid & 63;
  const int wave = __builtin_amdgcn_readfirstlane(tid >> 6);
  const int b = blockIdx.x, grp = b & 7, idx = b >> 3;
  const int cg = grp & 3, jl = idx & 3;
  const int rc = 8 * (grp >> 2) + (idx >> 2), j = 4 * cg + jl;
  const int r16 = lane & 15, g = lane >> 4;
  char* ws = a.ws;
  const RSRC rW = __builtin_amdgcn_make_buffer_rsrc((void*)ws, 0, WS_BYTES, 0x00020000);
  volatile LAS unsigned* misc = (volatile LAS unsigned*)(smem + SP_MISC);
  const unsigned sbase = (unsigned)(size_t)(LAS char*)smem;
  STAMP_DECL
  STAMP(0);

  if (wave >= 4) {
    const int lw = wave - 4;
    unsigned ow[5], oc[5];
    const char* wbase2 = (const char*)(a.w2 + (size_t)384 * cg * D_ + 160 * jl);
    const unsigned slb = sbase + 8192 + (lw & 1) * 5120;
    auto issueW = [&](int n) {
      const char* p = wbase2 + (size_t)n * (32 * D_ * 2);
      const unsigned sl = slb + (n & (NSLOT - 1)) * SLOT;
      gld4(ow[0], ow[1], ow[2], ow[3], p, sl); gldss(ow[4], p, sl + 4096);
    };
    {
      const int rw = lane >> 3, c8 = lane & 7;
      const unsigned vx = rw * (D_ * 2) + 16 * (c8 ^ (rw >> 1) ^ (4 * (lw & 1)));
      const unsigned vb = rw * (D_ * 2) + 16 * (c8 ^ ((rw >> 1) << 1));
      const char* px = (const char*)(a.x + (size_t)(128 * rc + 8 * lw) * D_);
      const char* pre = (const char*)(a.Bre + (size_t)(40 * j + 8 * lw) * D_);
      const char* pim = (const char*)(a.Bim + (size_t)(40 * j + 8 * lw) * D_);
      const char* pex = (const char*)((lw & 1 ? a.Bim : a.Bre) + (size_t)(40 * j + 32) * D_);
      const char* pw = (const char*)(a.w1 + (size_t)16 * lw * F_ + 96 * j);
      const unsigned dl = sbase + lw * 1024, vex = vb ^ ((lw & 1) * 16);
      unsigned vw[3];
      auto stage1 = [&](unsigned boff) {
        if (lw < 2) gldss(vex, pex, sbase + ST_BM + (4 + 5 * lw) * 1024 + boff);
#pragma unroll
        for (int i = 0; i < 4; ++i) gldss(vx + i * (32 * D_ * 2), px, dl + ST_X + i * 4096 + boff);
        gldss(vb, pre, dl + ST_BM + boff);
        gldss(vb ^ 16, pim, dl + ST_BM + 5120 + boff);
        px += 128; pre += 128; pim += 128; pex += 128;
      };
      auto stage2 = [&](unsigned boff) {
#pragma unroll
        for (int i = 0; i < 3; ++i) gldss(vw[i], pw, sbase + ST_W1 + (3 * lw + i) * 1024 + boff);
        pw += 64 * F_ * 2;
      };
      auto stage = [&](unsigned boff) { stage1(boff); stage2(boff); };
      stage1(0);
#pragma unroll
      for (int i = 0; i < 3; ++i) {
        const unsigned v = 64 * i + lane, k = (v * 171u) >> 11, cc = v - 12 * k - ((k >> 2) & 2);
        vw[i] = k * (F_ * 2) + 16 * min(cc, cc + 12u);
      }
      stage2(0); stage(ST_SZ);
      STAMP(1);
      LBAR(9);
      STAMP(2);
      stage(2 * ST_SZ);
      LBAR(9);
      stage(3 * ST_SZ); stage(0);
#pragma unroll
      for (int i5 = 0; i5 < 5; ++i5) {
        const unsigned i = 5 * (lw & 1) + i5;
        const unsigned v = 4 * i + lane, kq = (v * 205u) >> 12, k = 3 * i + kq, cc = v - 20 * kq - ((k >> 1) & 2);
        ow[i5] = k * (D_ * 2) + 16 * min(cc, cc + 20u) - 1024 * (i5 & 3);
        oc[i5] = (16 * i + (lane >> 2)) * (P_ * 2) + 16 * ((lane & 3) ^ ((0x78 >> (2 * g)) & 3)) - 1024 * (i5 & 3);
      }
#pragma unroll 1
      for (int s = 2; s <= 6; ++s) {
        LBAR(18);
        stage(((s + 3) & 3) * ST_SZ);
      }
      LBAR(18); LBAR(9);
      issueW(4 + (lw >> 1));
      LBAR(5);
      issueW(lw >> 1);
      lds_wait_ge(misc + M_P1D, 4u);
      issueW(2 + (lw >> 1));
    }
    STAMP(3);
    const unsigned tlo = misc[M_TAG], thi = misc[M_TAG + 1];

    volatile LAS unsigned* land = (volatile LAS unsigned*)(smem + SP_FULL);
    const char* abase = ws + OFF_AF + (size_t)(rc * 4 + cg) * 22 * 8192;
    const unsigned aoff = lane * 16;
    auto issueA4 = [&](int n, int h) {
      gld4c(aoff, abase + (size_t)n * 8192 + h * 4096, sbase + (n & (NSLOT - 1)) * SLOT + h * 4096);
    };
    if (lw != 1) {
      scan_task([&]() { issueW(6); issueW(7); }, rW, smem, misc, lw == 0 ? 2 : lw - 2, lane, rc, cg, jl, tlo, thi SCAN_PASS);
      STAMP(21);
    } else {
      lds_wait_ge(misc + M_SRD, 3u);
      issueW(6); issueW(7);
      lds_wait_ge(misc + M_ACT, 4u);
      STAMP(4);
      poll_flags(rW, OFF_FA + (rc * 4 + cg) * 128, 4, tlo, thi, lane);
      STAMP(5);
#pragma unroll
      for (int k = 0; k < 8; ++k) {
        if (k >= 4) { WAITV(24); land[1] = k - 3; }
        issueA4(k, 0); issueA4(k, 1);
      }
      WAITV(24); land[1] = 5u;
    }
    STAMP(6);
    {
      volatile LAS unsigned* cons = (volatile LAS unsigned*)(smem + SP_CONS) + (lane & 3);
      unsigned cv = *cons;
      auto slot = [&](int n) {
        if (__builtin_expect(__builtin_amdgcn_ballot_w64(cv < (unsigned)(n - NSLOT + 1)) != 0ull, 0)) cons_wait(cons, n - NSLOT + 1);
        asm volatile("" ::: "memory");
      };
      if (lw < 2) {
        const unsigned fo = OFF_FA + (rc * 4 + cg) * 128 + 32 + (lane & 3) * 8;
        int np = lw == 0 ? 8 : 5;
        auto body = [&](int n) {
          slot(n);
          issueA4(n, lw);
          cv = *cons;
          WAITV(12);
          np = max(np, n - 2); land[lw] = np;
        };
        slot(8); issueA4(8, lw); cv = *cons; WAITV(20); np = max(np, 6); land[lw] = np;
        slot(9); issueA4(9, lw); cv = *cons; WAITV(16); np = max(np, 7); land[lw] = np;
        u32x2 hf;
        asm volatile("buffer_load_dwordx2 %0, %1, %2, 0 offen sc1" : "=v"(hf) : "v"(fo), "s"(rW) : "memory");
        body(10); body(11);
        asm volatile("s_waitcnt vmcnt(8)" : "+v"(hf) :: "memory");
        if (!__all(hf[0] == tlo && hf[1] == thi)) {
          land[lw] = 10u; WAITV(0); land[lw] = 12u; np = 12;
          poll_flags(rW, OFF_FA + (rc * 4 + cg) * 128 + 32, 4, tlo, thi, lane);
        }
#pragma unroll 1
        for (int n = 12; n < 22; ++n) body(n);
        WAITV(8); land[lw] = 20u; WAITV(4); land[lw] = 21u; WAITV(0); land[lw] = 22u;
      } else {
        const char* p = wbase2 + (size_t)8 * (32 * D_ * 2);
        const char* cre = (const char*)(a.Cre + (size_t)160 * jl * P_ + 160 * cg);
        const char* cim = (const char*)(a.Cim + (size_t)160 * jl * P_ + 160 * cg);
#pragma unroll 1
        for (int n = 8; n < 22; ++n) {
          if (n == 12) {
#pragma unroll
            for (int i = 0; i < 5; ++i) ow[i] = oc[i];
          }
          p = n == 12 ? cre : n == 17 ? cim : p;
          slot(n);
          const unsigned sl = slb + (n & (NSLOT - 1)) * SLOT;
          gld4(ow[0], ow[1], ow[2], ow[3], p, sl); gldss(ow[4], p, sl + 4096);
          cv = *cons;
          p += n < 12 ? 32 * D_ * 2 : 64;
          asm volatile("s_waitcnt vmcnt(%0)" :: "n"(5 * LAG) : "memory");
          land[lw] = max(8, n - LAG + 1);
        }
#pragma unroll
        for (int d = LAG - 1; d >= 0; --d) { asm volatile("s_waitcnt vmcnt(%0)" :: "n"(5 * d) : "memory"); land[lw] = 22 - d; }
      }
    }
    STAMP(8);
    const int tl = 16 * (lw & 1) + r16, n0 = 160 * jl + 80 * (lw >> 1) + 4 * g;
    const size_t orow = (size_t)(128 * rc + 32 * cg + tl) * D_ + n0;
    f32x4 sk[5];
    {
      const float gt = 0.5f * *(const float*)(smem + SP_G + (32 * cg + tl) * 4);
#pragma unroll
      for (int i = 0; i < 5; ++i) {
        u32x2 xv = *(const u32x2*)(a.x + orow + 16 * i);
        u32x2 dv = *(const u32x2*)(a.Dsk + n0 + 16 * i);
        sk[i][0] = gt * bflo(xv[0]) * bflo(dv[0]); sk[i][1] = gt * bfhi(xv[0]) * bfhi(dv[0]);
        sk[i][2] = gt * bflo(xv[1]) * bflo(dv[1]); sk[i][3] = gt * bfhi(xv[1]) * bfhi(dv[1]);
      }
    }
    STAMP(22);
    for (int sp = 0; misc[M_RED] < 4u && sp < SPIN_LDS; ++sp) __builtin_amdgcn_s_sleep(4);
    asm volatile("" ::: "memory");
    STAMP(23);
    poll_flags(rW, OFF_FR + (rc * 4 + jl) * 128, 4, tlo, thi, lane);
    STAMP(24);
    if (rc == 15 && j == 0 && tid == 256) set_flag(rW, OFF_EPOCH, tlo, thi);
    u32x2 pv[3][5];
#pragma unroll
    for (int s = 0; s < 3; ++s) {
      const int sc = (cg + 1 + s) & 3;
#pragma unroll
      for (int i = 0; i < 5; ++i)
        pv[s][i] = __builtin_amdgcn_raw_buffer_load_b64(rW, OFF_PB + (((rc * 4 + jl) * 4 + cg) * 4 + sc) * 10240 + (lw >> 1) * 5120 + i * 1024 + lane * 16 + (lw & 1) * 8, 0, SC1);
    }
#pragma unroll
    for (int i = 0; i < 5; ++i) {
      f32x4 v = *(const f32x4*)(smem + (5 * lw + i) * 1024 + lane * 16) + sk[i];
#pragma unroll
      for (int s = 0; s < 3; ++s) v += (f32x4){bflo(pv[s][i][0]), bfhi(pv[s][i][0]), bflo(pv[s][i][1]), bfhi(pv[s][i][1])};
      *(f32x4*)(a.out + orow + 16 * i) = v;
    }
  } else {
    if (wave == 3) {
      unsigned long long pc; unsigned back, csz;
      asm volatile("s_getpc_b64 %0\n1:\n\ts_mov_b32 %1, 1b-_Z5fused4Args\n\ts_mov_b32 %2, _Z8code_endv-_Z5fused4Args" : "=s"(pc), "=s"(back), "=s"(csz));
      const unsigned co = (idx * 14 + (lane < 14 ? lane : 0)) * 128;
      touch((const char*)(pc - back) + (co + 4 <= csz ? co : 0u), sbase + SP_EL);
      touch((const char*)a.w1 + (size_t)(b * 60 + (lane < 60 ? lane : 0)) * 128, sbase + SP_EL);
      touch((const char*)(lane < 32 ? a.Bre : a.Bim) + (size_t)(b * 25 + ((lane & 31) < 25 ? (lane & 31) : 0)) * 128, sbase + SP_EL);
    } else if (wave == 0) {
      touch((const char*)a.x + (size_t)(b * 80 + lane) * 128, sbase + SP_EL);
      touch((const char*)a.x + (size_t)(b * 80 + 64 + r16) * 128, sbase + SP_EL);
      touch((const char*)a.w2 + (size_t)(b * 60 + (lane < 60 ? lane : 0)) * 128, sbase + SP_EL);
      touch((const char*)(lane < 32 ? a.Cre : a.Cim) + (size_t)(b * 25 + ((lane & 31) < 25 ? (lane & 31) : 0)) * 128, sbase + SP_EL);
    }
    if (wave == 1) {
      const u64 ep = __hip_atomic_load((GAS u64*)(ws + OFF_EPOCH), __ATOMIC_RELAXED, __HIP_MEMORY_SCOPE_AGENT);
      const unsigned v = *(const unsigned*)(a.b1 + 96 * j + 2 * (lane < 48 ? lane : 0));
      const u32x4 g0 = *(const u32x4*)(a.gw + 8 * lane), g1 = *(const u32x4*)(a.gw + 8 * (64 + r16));
      if (lane < 48) *(f32x2*)(smem + SP_B1 + lane * 8) = (f32x2){bflo(v), bfhi(v)};
      *(u32x4*)(smem + SP_GW + lane * 16) = g0;
      if (lane < 16) *(u32x4*)(smem + SP_GW + 1024 + lane * 16) = g1;
      if (lane < 24 || lane >= 32) misc[lane] = 0u;
      *(unsigned*)(smem + SP_FULL + lane * 4) = 0u;
      *(u32x4*)(smem + SP_CONS + lane * 16) = (u32x4){0u, 0u, 0u, 0u};
      if (lane == 0) { const u64 tg = ep + 1; misc[M_TAG] = (unsigned)tg; misc[M_TAG + 1] = (unsigned)(tg >> 32); }
    } else if (wave == 2) {
      const int ch = 40 * j + (lane < 40 ? lane : 0);
      const float lre = bflo(a.Lre[ch]), lim = bflo(a.Lim[ch]), dt = expf(bflo(a.ldt[ch]));
      float e = expf(dt * lre), sn, cs;
      sincosf(dt * lim, &sn, &cs);
      f32x2 l1 = {e * cs, e * sn};
      const float nr = l1[0] - 1.f, ni = l1[1], den = lre * lre + lim * lim;
      f32x2 cf = {(nr * lre + ni * lim) / den, (ni * lre - nr * lim) / den};
      f32x2 l2 = cm2(l1, l1), l4 = cm2(l2, l2), l8 = cm2(l4, l4), l16 = cm2(l8, l8);
      f32x2 l32 = cm2(l16, l16), l64 = cm2(l32, l32), l128 = cm2(l64, l64), l96 = cm2(l64, l32);
      if (lane < 40) {
        float* pp = (float*)(smem + SP_PAR + lane * PARS);
        *(f32x4*)(pp) = (f32x4){l1[0], l1[1], l128[0], l128[1]};
        *(f32x4*)(pp + 4) = (f32x4){cf[0], cf[1], l32[0], l32[1]};
        *(f32x4*)(pp + 8) = (f32x4){l64[0], l64[1], l96[0], l96[1]};
      }
    }
    STAMP(1);
    const int th = wave & 1;
    const unsigned xo0 = ST_X + (64 * th + r16) * 128 + ((g ^ (r16 >> 1)) * 16);
    const unsigned xo1 = xo0 ^ 64;
    const int q4 = r16 >> 2, p4 = r16 & 3;

#define P1_PIPE(LOADF, STEP, MMA, INIT)                                             \
    CBAR;                                                                           \
    __builtin_amdgcn_s_setprio(3);                                                  \
    STAMP(2);                                                                       \
    INIT;                                                                           \
    LOADF(F0, 0u, 0);                                                               \
    STEP(F0, F1, 0u, 1);                                                            \
    _Pragma("unroll 1") for (int s = 1; s < 10; ++s) {                              \
      CBAR;                                                                         \
      STEP(F1, F0, (s & 3) * ST_SZ, 0);                                             \
      STEP(F0, F1, (s & 3) * ST_SZ, 1);                                             \
    }                                                                               \
    asm volatile("s_waitcnt lgkmcnt(0)" ::: "memory");                              \
    lds_inc(smem, M_P1D, lane);                                                     \
    MMA(F1); SCHED; __builtin_amdgcn_s_setprio(0);

    unsigned tlo, thi;
    if (wave < 2) {
      f32x4 acc[6][4];
      const unsigned wbase = ST_W1 + (8 * g + q4) * 192 + (p4 >> 1) * 16 + (p4 & 1) * 8;
      const unsigned wa = wbase + (g & 1) * 32;
      const unsigned wb = wbase + ((g & 1) ? 0 : 160);
      struct Frag { bf16x8 x[4], w[6]; };
      Frag F0, F1;
      auto rdw = [&](const char* sb, int ft, int kk) {
        unsigned ad = (ft < 5 ? wa + 32 * ft : wb) + kk * 32 * 192;
        s16x4 lo = __builtin_amdgcn_ds_read_tr16_b64_v4i16((LAS s16x4*)(sb + ad));
        s16x4 hi = __builtin_amdgcn_ds_read_tr16_b64_v4i16((LAS s16x4*)(sb + ad + 4 * 192));
        return __builtin_bit_cast(bf16x8, __builtin_shufflevector(lo, hi, 0, 1, 2, 3, 4, 5, 6, 7));
      };
      auto loadf = [&](Frag& F, unsigned boff, int kk) {
        const char* sb = smem + boff;
#pragma unroll
        for (int tt = 0; tt < 4; ++tt) F.x[tt] = *(const bf16x8*)(sb + (kk ? xo1 : xo0) + tt * 2048);
#pragma unroll
        for (int ft = 0; ft < 6; ++ft) F.w[ft] = rdw(sb, ft, kk);
      };
      auto mma = [&](Frag& F) {
#pragma unroll
        for (int m = 0; m < 24; ++m)
          acc[m >> 2][m & 3] = __builtin_amdgcn_mfma_f32_16x16x32_bf16(F.w[m >> 2], F.x[m & 3], acc[m >> 2][m & 3], 0, 0, 0);
      };
      auto step = [&](Frag& C, Frag& N, unsigned boff, const int kk) {
        const char* sb = smem + boff;
#pragma unroll
        for (int m = 0; m < 24; ++m) {
          SCHED;
          acc[m >> 2][m & 3] = __builtin_amdgcn_mfma_f32_16x16x32_bf16(C.w[m >> 2], C.x[m & 3], acc[m >> 2][m & 3], 0, 0, 0);
          SCHED;
          if (m < 4) N.x[m] = *(const bf16x8*)(sb + (kk ? xo1 : xo0) + m * 2048);
          if (m >= 4 && m < 10) N.w[m - 4] = rdw(sb, m - 4, kk);
        }
        SCHED;
      };
      auto init = [&]() {
#pragma unroll
        for (int ft = 0; ft < 6; ++ft) {
          f32x4 bv = *(const f32x4*)(smem + SP_B1 + (16 * ft + 4 * g) * 4);
#pragma unroll
          for (int tt = 0; tt < 4; ++tt) acc[ft][tt] = bv;
        }
      };
      P1_PIPE(loadf, step, mma, init());
      STAMP(3);
      tlo = misc[M_TAG]; thi = misc[M_TAG + 1];
      lds_wait_ge(misc + M_P1D, 4u);
#pragma unroll
      for (int t2 = 0; t2 < 2; ++t2)
#pragma unroll
        for (int i = 0; i < 6; ++i) *(f32x4*)(smem + (2 * th + t2) * SLOT + i * 1024 + lane * 16) = acc[i][2 + t2];
      asm volatile("s_waitcnt lgkmcnt(0)" ::: "memory");
      if (lane == 0) misc[M_OFF + th] = 1u;
      lds_wait_ge(misc + M_GRDY, 2u);
#pragma unroll
      for (int tt = 0; tt < 2; ++tt) {
        const float og = 1.f - 0.5f * *(const float*)(smem + SP_G + (64 * th + 16 * tt + r16) * 4);
#pragma unroll
        for (int fp = 0; fp < 3; ++fp)
          st128(rW, OFF_AF + (((rc * 4 + cg) * 22 + 3 * jl + fp) * 8 + 4 * th + tt) * 1024 + lane * 16,
                mish_unit(acc[2 * fp][tt], acc[2 * fp + 1][tt], og));
      }
      STAMP(4);
      WAITV(0);
      if (lds_inc(smem, M_ACT, lane) == 3u && lane == 0) set_flag(rW, OFF_FA + (rc * 4 + cg) * 128 + jl * 8, tlo, thi);
      STAMP(5);
    } else {
      f32x4 acc[4][5], gac[4];
      const unsigned bo0 = ST_BM + (40 * (r16 & 1) + (r16 >> 1)) * 128 + ((g ^ ((((r16 >> 2) & 3) << 1) | (r16 & 1))) * 16);
      const unsigned bo1 = bo0 ^ 64;
      struct Frag { bf16x8 x[4], w[5], gf; };
      Frag F0, F1;
      int kc = 0;
      auto loadf = [&](Frag& F, unsigned boff, int kk) {
        const char* sb = smem + boff;
#pragma unroll
        for (int tt = 0; tt < 4; ++tt) F.x[tt] = *(const bf16x8*)(sb + (kk ? xo1 : xo0) + tt * 2048);
#pragma unroll
        for (int nt = 0; nt < 5; ++nt) F.w[nt] = *(const bf16x8*)(sb + (kk ? bo1 : bo0) + nt * 1024);
        F.gf = *(const bf16x8*)(smem + SP_GW + kc * 64 + g * 16);
        ++kc;
      };
      auto mma = [&](Frag& F) {
#pragma unroll
        for (int tt = 0; tt < 4; ++tt) gac[tt] = __builtin_amdgcn_mfma_f32_16x16x32_bf16(F.x[tt], F.gf, gac[tt], 0, 0, 0);
#pragma unroll
        for (int m = 0; m < 20; ++m)
          acc[m & 3][m >> 2] = __builtin_amdgcn_mfma_f32_16x16x32_bf16(F.x[m & 3], F.w[m >> 2], acc[m & 3][m >> 2], 0, 0, 0);
      };
      auto step = [&](Frag& C, Frag& N, unsigned boff, const int kk) {
        const char* sb = smem + boff;
#pragma unroll
        for (int m = 0; m < 24; ++m) {
          SCHED;
          if (m < 20) acc[m & 3][m >> 2] = __builtin_amdgcn_mfma_f32_16x16x32_bf16(C.x[m & 3], C.w[m >> 2], acc[m & 3][m >> 2], 0, 0, 0);
          else gac[m & 3] = __builtin_amdgcn_mfma_f32_16x16x32_bf16(C.x[m & 3], C.gf, gac[m & 3], 0, 0, 0);
          SCHED;
          if (m < 4) N.x[m] = *(const bf16x8*)(sb + (kk ? xo1 : xo0) + m * 2048);
          if (m >= 4 && m < 9) N.w[m - 4] = *(const bf16x8*)(sb + (kk ? bo1 : bo0) + (m - 4) * 1024);
          if (m == 9) N.gf = *(const bf16x8*)(smem + SP_GW + kc * 64 + g * 16);
        }
        ++kc;
        SCHED;
      };
      auto init = [&]() {
#pragma unroll
        for (int tt = 0; tt < 4; ++tt) {
          gac[tt] = (f32x4){0.f, 0.f, 0.f, 0.f};
#pragma unroll
          for (int nt = 0; nt < 5; ++nt) acc[tt][nt] = (f32x4){0.f, 0.f, 0.f, 0.f};
        }
      };
      P1_PIPE(loadf, step, mma, init());
      STAMP(3);
      tlo = misc[M_TAG]; thi = misc[M_TAG + 1];
      {
        char* bw = smem + L_BU + r16 * BUS + th * 288 + g * 16;
#pragma unroll
        for (int tt = 0; tt < 4; ++tt)
#pragma unroll
          for (int nt = 0; nt < 5; ++nt) *(f32x4*)(bw + nt * 16 * BUS + (tt >> 1) * 144 + (tt & 1) * 64) = acc[tt][nt];
        asm volatile("s_waitcnt lgkmcnt(0)" ::: "memory");
        lds_inc(smem, M_BUW, lane);
        f32x4 lg = r16 == 0 ? gac[0] : r16 == 1 ? gac[1] : r16 == 2 ? gac[2] : gac[3];
        f32x4 gg;
#pragma unroll
        for (int r = 0; r < 4; ++r) gg[r] = 2.f * __builtin_amdgcn_rcpf(1.f + __expf(-lg[r]));
        if (r16 < 4) *(f32x4*)(smem + SP_G + (64 * th + 16 * r16 + 4 * g) * 4) = gg;
        asm volatile("s_waitcnt lgkmcnt(0)" ::: "memory");
        lds_inc(smem, M_GRDY, lane);
      }
      STAMP(4);
      lds_wait_ge(misc + M_OFF + th, 1u);
#pragma unroll
      for (int t2 = 0; t2 < 2; ++t2) {
        const float og = 1.f - 0.5f * *(const float*)(smem + SP_G + (64 * th + 32 + 16 * t2 + r16) * 4);
#pragma unroll
        for (int fp = 0; fp < 3; ++fp) {
          const char* sp = smem + (2 * th + t2) * SLOT + fp * 2048 + lane * 16;
          st128(rW, OFF_AF + (((rc * 4 + cg) * 22 + 3 * jl + fp) * 8 + 4 * th + 2 + t2) * 1024 + lane * 16,
                mish_unit(*(const f32x4*)sp, *(const f32x4*)(sp + 1024), og));
        }
      }
      WAITV(0);
      if (lds_inc(smem, M_ACT, lane) == 3u && lane == 0) set_flag(rW, OFF_FA + (rc * 4 + cg) * 128 + jl * 8, tlo, thi);
      STAMP(5);
    }

    const int wn = wave & 1, wt = wave >> 1;
    f32x4 acc[5][4];
#pragma unroll
    for (int nt = 0; nt < 5; ++nt)
#pragma unroll
      for (int tt = 0; tt < 4; ++tt) acc[nt][tt] = (f32x4){0.f, 0.f, 0.f, 0.f};
    struct Frag2 { bf16x8 x[4], w[5]; };
    Frag2 G0, G1;
    const unsigned ao = 4 * wt * 1024 + lane * 16;
    const unsigned twb = 8192 + (4 * g + q4) * 320 + (p4 >> 1) * 16 + (p4 & 1) * 8;
    const unsigned twa = twb + (5 * wn + (g & 1)) * 32;
    const unsigned twl = (wn == 1 && (g & 1)) ? twb : twa + 128;
    const int f4 = (r16 >> 2) & 3, fx = (f4 == 0 ? 0 : f4 == 1 ? 2 : f4 == 2 ? 3 : 1);
    const unsigned cwo = 8192 + (80 * wn + r16) * 64 + ((g ^ fx) * 16);
    unsigned ba[2] = {ao, ao + 4 * SLOT}, bta[2] = {twa, twa + 4 * SLOT}, btl[2] = {twl, twl + 4 * SLOT}, bc[2] = {cwo, cwo + 4 * SLOT};
    const unsigned hxo = (p4 >> 1) * 2048 + (8 * g + q4) * 64 + (((2 * wt) ^ (((g & 1) << 1) | (p4 >> 1))) * 16) + (p4 & 1) * 8;
    unsigned bh[4] = {hxo, hxo + 4 * SLOT, hxo ^ 16, (hxo ^ 16) + 4 * SLOT};
    asm volatile("" : "+v"(bh[0]), "+v"(bh[1]), "+v"(bh[2]), "+v"(bh[3]));
    unsigned mb = SP_FULL + (lane & 3) * 4, cw = SP_CONS + wave * 4 + lane * 16;
    asm volatile("" : "+v"(ba[0]), "+v"(ba[1]), "+v"(bta[0]), "+v"(bta[1]), "+v"(btl[0]), "+v"(btl[1]), "+v"(bc[0]), "+v"(bc[1]), "+v"(mb), "+v"(cw));
    auto rd2 = [&](Frag2& N, const int n, const int r) {
      const int sl = n % NSLOT, hb = sl >> 2, so = (sl & 3) * SLOT;
      if (n >= 12) {
        if (r < 8) {
          const int tt = r >> 1, h = r & 1;
          const s16x4 v = __builtin_amdgcn_ds_read_tr16_b64_v4i16((LAS s16x4*)(smem + bh[(tt >> 1) * 2 + hb] + so + (tt & 1) * 4096 + h * 256));
          u32x4 t = __builtin_bit_cast(u32x4, N.x[tt]); const u32x2 vv = __builtin_bit_cast(u32x2, v);
          t[2 * h] = vv[0]; t[2 * h + 1] = vv[1];
          N.x[tt] = __builtin_bit_cast(bf16x8, t);
        } else if (r < 13) N.w[r - 8] = *(const bf16x8*)(smem + bc[hb] + so + (r - 8) * 1024);
      }
      else if (r < 4) N.x[r] = *(const bf16x8*)(smem + ba[hb] + so + r * 1024);
      else if (r < 14) {
        const int nt = (r - 4) >> 1, h = (r - 4) & 1;
        const s16x4 v = __builtin_amdgcn_ds_read_tr16_b64_v4i16((LAS s16x4*)(smem + (nt < 4 ? bta[hb] + 32 * nt : btl[hb]) + so + h * 16 * 320));
        u32x4 t = __builtin_bit_cast(u32x4, N.w[nt]); const u32x2 vv = __builtin_bit_cast(u32x2, v);
        t[2 * h] = vv[0]; t[2 * h + 1] = vv[1];
        N.w[nt] = __builtin_bit_cast(bf16x8, t);
      }
    };
    auto wait_full = [&](int st) { cons_wait((volatile LAS unsigned*)(smem + mb), st + 1); };
    bool fok = false;
    auto step2 = [&](Frag2& C, Frag2& N, const int n) {
      if (n < 21 && __builtin_expect(!fok, 0)) wait_full(n + 1);
      unsigned fpre = 0u;
#pragma unroll
      for (int m = 0; m < 20; ++m) {
        SCHED;
        acc[m >> 2][m & 3] = __builtin_amdgcn_mfma_f32_16x16x32_bf16(C.w[m >> 2], C.x[m & 3], acc[m >> 2][m & 3], 0, 0, 0);
        SCHED;
        if (n < 21) {
          rd2(N, n + 1, m);
          if (m == 14) fpre = *(volatile LAS unsigned*)(smem + mb);
          if (m == 17) __builtin_amdgcn_s_waitcnt(0xC07F);
          if (m == 18) { *(volatile LAS unsigned*)(smem + cw) = n + 2; fok = __builtin_amdgcn_ballot_w64(fpre < n + 3u) == 0ull; }
        }
      }
      SCHED;
    };
    wait_full(0);
    __builtin_amdgcn_s_setprio(1);
    STAMP(6);
#pragma unroll
    for (int r = 0; r < 14; ++r) rd2(G0, 0, r);
    __builtin_amdgcn_s_waitcnt(0xC07F);
    *(volatile LAS unsigned*)(smem + cw) = 1u;
#pragma unroll
    for (int n = 0; n < 22; n += 2) {
      if (n == 10) { STAMP(7); }
      if (n == 12) { STAMP(8); }
      step2(G0, G1, n); step2(G1, G0, n + 1);
    }
    __builtin_amdgcn_s_setprio(0);
    STAMP(9);
#pragma unroll
    for (int t2 = 0; t2 < 2; ++t2) {
      const int owner = 2 * wt + t2;
      if (owner != cg) {
        const unsigned pb = OFF_PB + (((rc * 4 + jl) * 4 + owner) * 4 + cg) * 10240 + wn * 5120 + lane * 16;
#pragma unroll
        for (int nt = 0; nt < 5; ++nt) {
          const f32x4 v0 = acc[nt][2 * t2], v1 = acc[nt][2 * t2 + 1];
          u32x4 o; o[0] = pack2(v0[0], v0[1]); o[1] = pack2(v0[2], v0[3]); o[2] = pack2(v1[0], v1[1]); o[3] = pack2(v1[2], v1[3]);
          st128(rW, pb + nt * 1024, o);
        }
      }
    }
    cons_wait((volatile LAS unsigned*)(smem + SP_CONS) + (lane & 3), 22u);
#pragma unroll
    for (int tt = 0; tt < 4; ++tt) {
      if (2 * wt + (tt >> 1) == cg) {
#pragma unroll
        for (int nt = 0; nt < 5; ++nt) *(f32x4*)(smem + (5 * (2 * wn + (tt & 1)) + nt) * 1024 + lane * 16) = acc[nt][tt];
      }
    }
    STAMP(10);
    asm volatile("s_waitcnt vmcnt(0) lgkmcnt(0)" ::: "memory");
    if (lds_inc(smem, M_RED + 1, lane) == 3u) {
      if (lane == 0) { set_flag(rW, OFF_FR + (rc * 4 + jl) * 128 + cg * 8, tlo, thi); misc[M_RED] = 4u; }
    }
    STAMP(11);
  }
  STAMP(25);
  STAMP(26);
  STAMP_OUT(0);
}

__global__ void code_end() {}

extern "C" void kernel_launch(void* const* d_in, const int* in_sizes, int n_in,
                              void* d_out, int out_size, void* d_ws, size_t ws_size,
                              hipStream_t stream) {
  Args a;
  a.x   = (const u16*)d_in[0];
  a.Lre = (const u16*)d_in[1];
  a.Lim = (const u16*)d_in[2];
  a.Bre = (const u16*)d_in[3];
  a.Bim = (const u16*)d_in[4];
  a.Cre = (const u16*)d_in[5];
  a.Cim = (const u16*)d_in[6];
  a.ldt = (const u16*)d_in[7];
  a.Dsk = (const u16*)d_in[8];
  a.w1  = (const u16*)d_in[10];
  a.b1  = (const u16*)d_in[11];
  a.w2  = (const u16*)d_in[12];
  a.gw  = (const u16*)d_in[13];
  a.ws  = (char*)d_ws;
  a.out = (float*)d_out;
  fused<<<256, 512, 0, stream>>>(a);
}
```

```cpp
#include <hip/hip_runtime.h>
#include <hip/hip_bf16.h>
#include <stdint.h>
#pragma clang diagnostic ignored "-Winline-asm"

typedef __attribute__((ext_vector_type(8))) __bf16 bf16x8;
typedef __attribute__((ext_vector_type(2))) __bf16 bf16x2;
typedef __attribute__((ext_vector_type(4))) float f32x4;
typedef __attribute__((ext_vector_type(2))) float f32x2;
typedef __attribute__((ext_vector_type(4))) unsigned u32x4;
typedef __attribute__((ext_vector_type(2))) unsigned u32x2;
typedef __attribute__((ext_vector_type(4))) short s16x4;
typedef unsigned short u16;
typedef unsigned long long u64;
#define LAS __attribute__((address_space(3)))
#define GAS __attribute__((address_space(1)))
#define DI __device__ __forceinline__

constexpr int D_ = 640, P_ = 640, F_ = 1536;

constexpr unsigned OFF_AF = 0;
constexpr unsigned OFF_PB = OFF_AF + 64u * 22 * 8192;
constexpr unsigned OFF_E  = OFF_PB + 64u * 16 * 10240;
constexpr unsigned OFF_FA = OFF_E + 256u * 640;
constexpr unsigned OFF_FH = OFF_FA + 64 * 128;
constexpr unsigned OFF_MK = OFF_FH + 256 * 128;
constexpr unsigned OFF_FR = OFF_MK + 256 * 128;
constexpr unsigned OFF_EPOCH = OFF_FR + 64 * 128;
constexpr unsigned WS_BYTES = OFF_EPOCH + 256;

#define STAMP(i)
#define CSTAMP(i)
#define STAMP_DECL
#define SCAN_ARG
#define SCAN_PASS
#define STAMP_OUT(kid)

DI float bflo(unsigned v) { return __uint_as_float(v << 16); }
DI float bfhi(unsigned v) { return __uint_as_float(v & 0xffff0000u); }
DI unsigned pack2(float a, float b) {
  bf16x2 r; r[0] = (__bf16)a; r[1] = (__bf16)b; return __builtin_bit_cast(unsigned, r);
}
DI f32x2 cm2(f32x2 a, f32x2 b) { return (f32x2){a[0] * b[0] - a[1] * b[1], a[0] * b[1] + a[1] * b[0]}; }
DI float sfma(float a, float b, float c) { float d; asm("v_fma_f32 %0, %1, %2, %3" : "=v"(d) : "v"(a), "v"(b), "v"(c)); return d; }
DI float smul(float a, float b) { float d; asm("v_mul_f32 %0, %1, %2" : "=v"(d) : "v"(a), "v"(b)); return d; }
template <int I> DI float qb(float v) {
  return __int_as_float(__builtin_amdgcn_mov_dpp(__float_as_int(v), I * 0x55, 0xf, 0xf, true));
}

DI f32x2 mish2(f32x2 h) {
  const f32x2 z = h * 1.44269504088896341f;
  f32x2 w; w[0] = __builtin_amdgcn_exp2f(z[0]); w[1] = __builtin_amdgcn_exp2f(z[1]);
  const f32x2 t = __builtin_elementwise_fma(w, (f32x2){-0.5f, -0.5f}, (f32x2){-1.f, -1.f});
  const f32x2 d = __builtin_elementwise_fma(w, t, (f32x2){-1.f, -1.f});
  f32x2 r; r[0] = __builtin_amdgcn_rcpf(d[0]); r[1] = __builtin_amdgcn_rcpf(d[1]);
  return __builtin_elementwise_fma(h, r, h);
}
DI f32x4 mish4(f32x4 a) {
  const f32x2 m0 = mish2((f32x2){a[0], a[1]}), m1 = mish2((f32x2){a[2], a[3]});
  return (f32x4){m0[0], m0[1], m1[0], m1[1]};
}
DI u32x4 pack8(f32x4 a0, f32x4 a1, float og) {
  a0 *= og; a1 *= og;
  return (u32x4){pack2(a0[0], a0[1]), pack2(a0[2], a0[3]), pack2(a1[0], a1[1]), pack2(a1[2], a1[3])};
}

#define SC1 16
#define SCHED __builtin_amdgcn_sched_barrier(0)
#define RSRC __amdgpu_buffer_rsrc_t
DI void st128(RSRC r, unsigned off, u32x4 v) { __builtin_amdgcn_raw_buffer_store_b128(v, r, off, 0, SC1); }
DI void set_flag(RSRC r, unsigned off, unsigned tlo, unsigned thi) { __builtin_amdgcn_raw_buffer_store_b64((u32x2){tlo, thi}, r, off, 0, SC1); }

DI void touch(const void* gsrc, unsigned lds_dst) {
  asm volatile("s_mov_b32 m0, %1\n\ts_nop 0\n\tglobal_load_lds_dword %0, off" :: "v"(gsrc), "s"(lds_dst) : "memory", "m0");
}
DI void gldss(unsigned voff, const void* sbase_, unsigned lds_dst) {
  asm volatile("s_mov_b32 m0, %2\n\ts_nop 0\n\tglobal_load_lds_dwordx4 %0, %1" :: "v"(voff), "s"(sbase_), "s"(lds_dst) : "memory", "m0");
}
DI void gld4c(unsigned voff, const void* sbase_, unsigned lds_dst) {
  asm volatile("s_mov_b32 m0, %2\n\ts_nop 0\n\tglobal_load_lds_dwordx4 %0, %1 sc1\n\tglobal_load_lds_dwordx4 %0, %1 offset:1024 sc1\n\t"
               "global_load_lds_dwordx4 %0, %1 offset:2048 sc1\n\tglobal_load_lds_dwordx4 %0, %1 offset:3072 sc1"
               :: "v"(voff), "s"(sbase_), "s"(lds_dst) : "memory", "m0");
}
DI void gld5(const unsigned* v, const void* sbase_, unsigned lds_dst) {
  asm volatile("s_mov_b32 m0, %5\n\ts_nop 0\n\tglobal_load_lds_dwordx4 %0, %4\n\tglobal_load_lds_dwordx4 %1, %4 offset:1024\n\t"
               "global_load_lds_dwordx4 %2, %4 offset:2048\n\tglobal_load_lds_dwordx4 %3, %4 offset:3072"
               :: "v"(v[0]), "v"(v[1]), "v"(v[2]), "v"(v[3]), "s"(sbase_), "s"(lds_dst) : "memory", "m0");
  gldss(v[4], sbase_, lds_dst + 4096);
}
DI void gld1c(unsigned voff, const void* sbase_, unsigned lds_dst) {
  asm volatile("s_mov_b32 m0, %2\n\ts_nop 0\n\tglobal_load_lds_dword %0, %1 sc1" :: "v"(voff), "s"(sbase_), "s"(lds_dst) : "memory", "m0");
}

constexpr int ST_X = 0, ST_BM = 16384, ST_W1 = 26624, ST_SZ = 38912;
constexpr int SP = 4 * ST_SZ;
constexpr int PARS = 48;
constexpr int SP_PAR = SP, SP_DF = SP + 1920, SP_HF = SP + 3200, SP_FULL = SP + 3456, SP_B1 = SP + 3840, SP_EL = SP + 4224,
              SP_GW = SP + 4544, SP_G = SP + 5824, SP_MISC = SP + 6336, LDS_BYTES = SP + 6592;
constexpr int BUS = 560, L_BU = SP - 80 * BUS;
constexpr int L_SCR = 65536;
DI constexpr int atile(int n) { return 40960 + 8192 * (n & 7) + ((n & 4) ? 45056 : 0); }
DI constexpr int btile(int n) {
  return n >= 12 ? (n < 14 ? 20480 : n < 16 ? 98304 : n < 18 ? 77824 : n < 20 ? 0 : 20480)
       : (n & 7) < 2 ? 77824 + 10240 * (n & 7) : (n & 7) < 5 ? 10240 * ((n & 7) - 2) : n == 5 ? 98304 : n == 6 ? 30720 : 108544;
}
enum { M_GRDY = 0, M_P1D, M_ACT, M_SRD, M_RED, M_RED2, M_OFF, M_OFF2, M_OWN, M_BUW, M_TAG = 16 };
constexpr int K0 = 6;
constexpr int DA = 11 - K0, DB = DA + 1;
constexpr int SPIN_LDS = 1 << 22, SPIN_G = 1 << 19;

struct Args {
  const u16 *x, *Lre, *Lim, *Bre, *Bim, *Cre, *Cim, *ldt, *Dsk, *w1, *b1, *w2, *gw;
  char* ws;
  float* out;
};

#define CBAR do { SCHED; asm volatile("s_waitcnt lgkmcnt(0)\n\ts_barrier" ::: "memory"); SCHED; } while (0)
#define WBAR(n) asm volatile("s_waitcnt vmcnt(%0)\n\ts_barrier" :: "n"(n) : "memory")
#define WAITV(n) asm volatile("s_waitcnt vmcnt(%0)" :: "n"(n) : "memory")
#define WAITL asm volatile("s_waitcnt lgkmcnt(0)" ::: "memory")

DI void lds_wait_ge(volatile LAS unsigned* w, unsigned v) {
  for (int sp = 0; !__all(*w >= v) && sp < SPIN_LDS; ++sp) __builtin_amdgcn_s_sleep(1);
  asm volatile("" ::: "memory");
}
DI unsigned lds_inc(char* smem, int word, int lane) {
  unsigned old = 0;
  asm volatile("; wave converged" ::: "memory");
  if (lane == 0) old = __hip_atomic_fetch_add((LAS unsigned*)(smem + SP_MISC) + word, 1u, __ATOMIC_RELAXED, __HIP_MEMORY_SCOPE_WORKGROUP);
  asm volatile("; wave converged" : "+v"(old) :: "memory");
  return __builtin_amdgcn_readfirstlane(old);
}
DI void lds_add(char* smem, int word, int lane) {
  asm volatile("; wave converged" ::: "memory");
  if (lane == 0) __hip_atomic_fetch_add((LAS unsigned*)(smem + SP_MISC) + word, 1u, __ATOMIC_RELAXED, __HIP_MEMORY_SCOPE_WORKGROUP);
  asm volatile("; wave converged" ::: "memory");
}
DI void poll_flags(RSRC rW, unsigned off, unsigned tlo, unsigned thi, int lane) {
  const unsigned o = off + (lane & 3) * 8;
#pragma unroll 1
  for (int sp = 0; sp < SPIN_G; ++sp) {
    const u32x2 f = __builtin_amdgcn_raw_buffer_load_b64(rW, o, 0, SC1);
    if (__all(f[0] == tlo && f[1] == thi)) break;
    __builtin_amdgcn_s_sleep(1);
    asm volatile("" ::: "memory");
  }
  asm volatile("" ::: "memory");
}

DI void scan_task(RSRC rW, char* smem, volatile LAS unsigned* misc, int u, int lane, int rc, int cg, int jl, unsigned tlo, unsigned thi SCAN_ARG) {
  const int j = 4 * cg + jl, seg = lane & 3;
  const bool act = 16 * u + (lane >> 2) < 40;
  const int chl = act ? 16 * u + (lane >> 2) : 39;
  const unsigned OOB = 0x7fffffffu;
  const float* pp = (const float*)(smem + SP_PAR + chl * PARS);
  const f32x4 la = *(const f32x4*)pp;
  const f32x4 cl = *(const f32x4*)(pp + 4);
  f32x2 pw = seg == 1 ? (f32x2){cl[2], cl[3]} : *(const f32x2*)(pp + 8 + (seg == 3 ? 2 : 0));
  if (seg == 0) pw = (f32x2){1.f, 0.f};
  const float lr = la[0], li = la[1], nli = -la[1];
  const f32x2 l256 = cm2((f32x2){la[2], la[3]}, (f32x2){la[2], la[3]}), l512 = cm2(l256, l256);
  const int r0 = rc - 16 + 4 * seg;
  unsigned eo[4];
#pragma unroll
  for (int i = 0; i < 4; ++i) eo[i] = r0 + i >= 0 ? OFF_E + ((j * 16 + r0 + i) * 40 + chl) * 16 : OOB;
  const int c = 40 * jl + chl, k = c & 31;
  const unsigned ho = act ? OFF_AF + ((rc * 4 + cg) * 22 + 12 + (c >> 5)) * 8192 + k * 64 + ((seg ^ ((k >> 2) & 2)) * 16) : OOB - 65536;
  STAMP(9);
  lds_wait_ge(misc + M_BUW, 2u);
  STAMP(10);
  __builtin_amdgcn_s_setprio(3);
  f32x4 ar[8], ai[8];
  {
    const char* bp = smem + L_BU + 2 * chl * BUS + seg * 144;
#pragma unroll
    for (int i = 0; i < 8; ++i) { ar[i] = *(const f32x4*)(bp + i * 16); ai[i] = *(const f32x4*)(bp + BUS + i * 16); }
  }
  WAITV(0);
  float sr = 0.f, si = 0.f;
#pragma unroll
  for (int t = 0; t < 32; ++t) {
    const float tr = fmaf(nli, si, ar[t >> 2][t & 3]), ti = fmaf(li, sr, ai[t >> 2][t & 3]);
    sr = fmaf(lr, sr, tr); si = fmaf(lr, si, ti);
  }
  STAMP(11);
  float qr = qb<0>(sr), qi = qb<0>(si);
  qr = seg > 0 ? qr : 0.f; qi = seg > 0 ? qi : 0.f;
  {
    float nr = fmaf(cl[2], qr, fmaf(-cl[3], qi, qb<1>(sr))), ni = fmaf(cl[2], qi, fmaf(cl[3], qr, qb<1>(si)));
    qr = seg > 1 ? nr : qr; qi = seg > 1 ? ni : qi;
    nr = fmaf(cl[2], qr, fmaf(-cl[3], qi, qb<2>(sr))); ni = fmaf(cl[2], qi, fmaf(cl[3], qr, qb<2>(si)));
    qr = seg > 2 ? nr : qr; qi = seg > 2 ? ni : qi;
    const float er = fmaf(cl[2], qr, fmaf(-cl[3], qi, sr)), ei = fmaf(cl[2], qi, fmaf(cl[3], qr, si));
    st128(rW, act && seg == 3 ? OFF_E + ((j * 16 + rc) * 40 + chl) * 16 : OOB, (u32x4){__float_as_uint(er), __float_as_uint(ei), tlo, thi});
  }
  STAMP(13);
  __builtin_amdgcn_s_setprio(0);
  lds_add(smem, M_SRD, lane);
#pragma unroll
  for (int i = 0; i < 8; ++i)
#pragma unroll
    for (int r = 0; r < 4; ++r) {
      const float xr = fmaf(cl[0], ar[i][r], -cl[1] * ai[i][r]), xi = fmaf(cl[0], ai[i][r], cl[1] * ar[i][r]);
      ar[i][r] = xr; ai[i][r] = xi;
    }
  lds_wait_ge(misc + M_GRDY, 2u);
  f32x4 gv[8];
#pragma unroll
  for (int i = 0; i < 8; ++i) gv[i] = *(const f32x4*)(smem + SP_G + seg * 128 + i * 16);
  STAMP(14);
  {
    u32x4 ev[4];
#pragma unroll 1
    for (int sp = 0; sp < SPIN_G; ++sp) {
      bool ok = true;
#pragma unroll
      for (int i = 0; i < 4; ++i) ev[i] = __builtin_amdgcn_raw_buffer_load_b128(rW, eo[i], 0, SC1);
#pragma unroll
      for (int i = 0; i < 4; ++i) ok = ok && (r0 + i < 0 || (ev[i][2] == tlo && ev[i][3] == thi));
      if (__all(ok)) break;
      __builtin_amdgcn_s_sleep(1);
      asm volatile("" ::: "memory");
    }
    STAMP(15);
    __builtin_amdgcn_s_setprio(3);
    float cr = __uint_as_float(ev[0][0]), ci = __uint_as_float(ev[0][1]);
#pragma unroll
    for (int i = 1; i < 4; ++i) {
      const float tr = sfma(-la[3], ci, __uint_as_float(ev[i][0])), ti = sfma(la[3], cr, __uint_as_float(ev[i][1]));
      cr = sfma(la[2], cr, tr); ci = sfma(la[2], ci, ti);
    }
    float dr = qb<0>(cr), di = qb<0>(ci), tr, ti;
    tr = sfma(-l512[1], di, qb<1>(cr)); ti = sfma(l512[1], dr, qb<1>(ci)); dr = sfma(l512[0], dr, tr); di = sfma(l512[0], di, ti);
    tr = sfma(-l512[1], di, qb<2>(cr)); ti = sfma(l512[1], dr, qb<2>(ci)); dr = sfma(l512[0], dr, tr); di = sfma(l512[0], di, ti);
    tr = sfma(-l512[1], di, qb<3>(cr)); ti = sfma(l512[1], dr, qb<3>(ci)); dr = sfma(l512[0], dr, tr); di = sfma(l512[0], di, ti);
    tr = sfma(-pw[1], di, qr); ti = sfma(pw[1], dr, qi);
    cr = sfma(pw[0], dr, tr); ci = sfma(pw[0], di, ti);
    sr = sfma(cl[0], cr, smul(-cl[1], ci)); si = sfma(cl[0], ci, smul(cl[1], cr));
  }
  STAMP(16);
#pragma unroll
  for (int i = 0; i < 4; ++i) {
    u32x4 vr, vi;
#pragma unroll
    for (int t2 = 0; t2 < 4; ++t2) {
      float hr[2], hi[2];
#pragma unroll
      for (int e = 0; e < 2; ++e) {
        const int t = 8 * i + 2 * t2 + e;
        const float tr = sfma(nli, si, ar[t >> 2][t & 3]), ti = sfma(li, sr, ai[t >> 2][t & 3]);
        sr = sfma(lr, sr, tr); si = sfma(lr, si, ti);
        hr[e] = smul(sr, gv[t >> 2][t & 3]); hi[e] = smul(-si, gv[t >> 2][t & 3]);
      }
      vr[t2] = pack2(hr[0], hr[1]); vi[t2] = pack2(hi[0], hi[1]);
    }
    __builtin_amdgcn_raw_buffer_store_b128(vr, rW, ho ^ ((i & 1) * 16), i * 2048, SC1);
    __builtin_amdgcn_raw_buffer_store_b128(vi, rW, ho ^ ((i & 1) * 16), i * 2048 + 5 * 8192, SC1);
  }
  STAMP(19);
  WAITV(0);
  STAMP(20);
  if (lane == 0) set_flag(rW, OFF_FH + ((rc * 4 + cg) * 4 + jl) * 128 + u * 8, tlo, thi);
  __builtin_amdgcn_s_setprio(0);
}

__global__ __launch_bounds__(512, 2) void fused(Args a) {
  __shared__ __attribute__((aligned(16))) char smem[LDS_BYTES];
  const int tid = threadIdx.x, lane = tid & 63;
  const int wave = __builtin_amdgcn_readfirstlane(tid >> 6);
  const int b = blockIdx.x, grp = b & 7, idx = b >> 3;
  const int cg = grp & 3, jl = idx & 3;
  const int rc = 8 * (grp >> 2) + (idx >> 2), j = 4 * cg + jl;
  const int r16 = lane & 15, g = lane >> 4;
  char* ws = a.ws;
  const RSRC rW = __builtin_amdgcn_make_buffer_rsrc((void*)ws, 0, WS_BYTES, 0x00020000);
  volatile LAS unsigned* misc = (volatile LAS unsigned*)(smem + SP_MISC);
  const unsigned sbase = (unsigned)(size_t)(LAS char*)smem;
  STAMP_DECL
  STAMP(0);

  if (wave >= 4) {
    const int lw = wave - 4;
    const int rw = lane >> 3, c8 = lane & 7;
    unsigned ow[5];
    const char* wbase2 = (const char*)(a.w2 + (size_t)384 * cg * D_ + 160 * jl);
    auto kmlp = [&](const int n) { return 3 * (jl ^ (n / 3)) + n % 3; };
    auto woffs = [&](unsigned* o, int h) {
#pragma unroll
      for (int i5 = 0; i5 < 5; ++i5) {
        const unsigned i = 5 * h + i5;
        const unsigned v = 4 * i + lane, kq = (v * 205u) >> 12, k = 3 * i + kq, cc = v - 20 * kq - ((k >> 1) & 2);
        o[i5] = k * (D_ * 2) + 16 * min(cc, cc + 20u) - 1024 * (i5 & 3);
      }
    };
    auto issueW = [&](const int n, const unsigned* o, int h) {
      gld5(o, wbase2 + (size_t)kmlp(n) * (32 * D_ * 2), sbase + btile(n) + h * 5120);
    };
    {
      const unsigned vx = rw * (D_ * 2) + 16 * (c8 ^ (rw >> 1) ^ (4 * (lw & 1)));
      const unsigned vb = rw * (D_ * 2) + 16 * (c8 ^ ((rw >> 1) << 1));
      const char* px = (const char*)(a.x + (size_t)(128 * rc + 8 * lw) * D_);
      const char* pre = (const char*)(a.Bre + (size_t)(40 * j + 8 * lw) * D_);
      const char* pim = (const char*)(a.Bim + (size_t)(40 * j + 8 * lw) * D_);
      const char* pex = (const char*)((lw & 1 ? a.Bim : a.Bre) + (size_t)(40 * j + 32) * D_);
      const char* pw = (const char*)(a.w1 + (size_t)16 * lw * F_ + 96 * j);
      const unsigned dl = sbase + lw * 1024, vex = vb ^ ((lw & 1) * 16);
      unsigned vw[3];
      auto stage1 = [&](unsigned boff) {
        if (lw < 2) gldss(vex, pex, sbase + ST_BM + (4 + 5 * lw) * 1024 + boff);
#pragma unroll
        for (int i = 0; i < 4; ++i) gldss(vx + i * (32 * D_ * 2), px, dl + ST_X + i * 4096 + boff);
        gldss(vb, pre, dl + ST_BM + boff);
        gldss(vb ^ 16, pim, dl + ST_BM + 5120 + boff);
        px += 128; pre += 128; pim += 128; pex += 128;
      };
      auto stage2 = [&](unsigned boff) {
#pragma unroll
        for (int i = 0; i < 3; ++i) gldss(vw[i], pw, sbase + ST_W1 + (3 * lw + i) * 1024 + boff);
        pw += 64 * F_ * 2;
      };
      auto stage = [&](unsigned boff) { stage1(boff); stage2(boff); };
      stage1(0);
#pragma unroll
      for (int i = 0; i < 3; ++i) {
        const unsigned v = 64 * i + lane, k = (v * 171u) >> 11, cc = v - 12 * k - ((k >> 2) & 2);
        vw[i] = k * (F_ * 2) + 16 * min(cc, cc + 12u);
      }
      stage2(0); stage(ST_SZ);
      STAMP(1);
      WBAR(9);
      STAMP(2);
      stage(2 * ST_SZ);
      WBAR(9);
      stage(3 * ST_SZ); stage(0);
      woffs(ow, lw & 1);
#pragma unroll 1
      for (int s = 2; s <= 6; ++s) {
        WBAR(18);
        stage(((s + 3) & 3) * ST_SZ);
      }
      WBAR(18);
      if (lw < 2) issueW(0, ow, lw); else issueW(1, ow, lw - 2);
      WBAR(14);
      if (lw < 2) issueW(5, ow, lw);
      WBAR(5);
      if (lw < 2) issueW(2, ow, lw); else { issueW(3, ow, lw - 2); issueW(4, ow, lw - 2); }
    }
    STAMP(3);
    const unsigned tlo = misc[M_TAG], thi = misc[M_TAG + 1];
    const char* abase = ws + OFF_AF + (size_t)(rc * 4 + cg) * 22 * 8192;
    const unsigned aoff = lane * 16;
    const int co = cg & 1;
    auto kstep = [&](int n) { return n < 12 ? kmlp(n) : n < 16 ? n + co : n < 20 ? n + 1 + co : (n == 20 ? 16 : 21) - 4 * co; };
    auto issueA4 = [&](int n, int h) {
      gld4c(aoff, abase + (size_t)kstep(n) * 8192 + h * 4096, sbase + atile(n) + h * 4096);
    };
    if (lw != 1) {
      scan_task(rW, smem, misc, lw == 0 ? 2 : lw - 2, lane, rc, cg, jl, tlo, thi SCAN_PASS);
      STAMP(21);
    } else {
      unsigned o0[5];
      woffs(o0, 0);
      if (lane == 0) *(volatile unsigned*)(ws + OFF_MK + b * 128) = tlo;
      WAITV(0);
      lds_wait_ge(misc + M_SRD, 3u); lds_wait_ge(misc + M_P1D, 4u);
      lds_add(smem, M_SRD, lane);
      issueW(6, o0, 0); issueW(6, ow, 1); issueW(7, o0, 0); issueW(7, ow, 1);
      lds_wait_ge(misc + M_ACT, 4u);
      STAMP(4);
      poll_flags(rW, OFF_FA + (rc * 4 + cg) * 128, tlo, thi, lane);
      STAMP(5);
#pragma unroll
      for (int n = 3; n < 11; ++n) {
        if (n == 8) lds_wait_ge(misc + M_OWN, 4u);
        issueA4(n, 0); issueA4(n, 1);
        if (n <= K0 + 1) gld1c(0u, ws + OFF_MK + b * 128, sbase + SP_DF + (n - 3) * 256);
      }
    }
    STAMP(6);
    if (lw < 2) {
      const char* fbase = ws + OFF_FH + (rc * 4 + cg) * 512;
      const unsigned hfo = ((lane >> 3) & 3) * 128 + ((lane & 7) < 6 ? lane & 7 : lane & 1) * 4;
      const unsigned want = (lane & 1) ? thi : tlo;
      if (lw == 0) gld1c(hfo, fbase, sbase + SP_HF);
      WBAR(8 * (8 - K0));
      gld1c(hfo, fbase, sbase + SP_HF);
      issueA4(11, lw);
      int k = K0 + 1;
      bool hok = false;
#pragma unroll
      for (int kk = K0 + 1; kk <= 9; ++kk) {
        if (!hok) {
          const unsigned hv = *(volatile LAS unsigned*)(smem + SP_HF + lane * 4);
          WBAR((kk <= 8 ? 8 * (8 - kk) + 4 : 0) + kk - K0 - 1);
          k = kk + 1;
          hok = __all(hv == want);
          asm volatile("" ::: "memory");
          if (!hok) gld1c(hfo, fbase, sbase + SP_HF);
        }
      }
#pragma unroll 1
      for (int sp = 0; !hok && sp < SPIN_G; ++sp) {
        hok = __all(__builtin_amdgcn_raw_buffer_load_b32(rW, OFF_FH + (rc * 4 + cg) * 512 + hfo, 0, SC1) == want);
        asm volatile("" ::: "memory");
      }
#pragma unroll 1
      for (int n = 12; n < k + DA; ++n) issueA4(n, lw);
#pragma unroll 1
      for (; k <= 21 - DA; ++k) { WBAR(4 * (DA - 3)); issueA4(k + DA, lw); }
#pragma unroll
      for (int kk = 22 - DA; kk <= 19; ++kk) WBAR(4 * (19 - kk));
    } else {
      const int h = lw - 2;
      lds_wait_ge(misc + M_OWN, 4u);
      issueW(8, ow, h); issueW(9, ow, h); issueW(10, ow, h);
      unsigned oc[10];
#pragma unroll
      for (int p = 0; p < 10; ++p) oc[p] = (8 * p + rw) * (P_ * 2) + 16 * (c8 ^ (rw >> 1) ^ (4 * (p & 1))) - 1024 * ((p % 5) & 3);
      const char* cre = (const char*)(a.Cre + (size_t)(160 * jl + 80 * h) * P_ + 160 * cg);
      const char* cim = (const char*)(a.Cim + (size_t)(160 * jl + 80 * h) * P_ + 160 * cg);
      WBAR(5 * (8 - K0));
      issueW(11, ow, h);
#pragma unroll
      for (int n = 12; n < 20; ++n) {
        if (n > K0 + DB) WBAR(5 * (DB - 4));
        gld5(oc + 5 * (n & 1), (n < 16 ? cre : cim) + co * 64 + ((n >> 1) & 1) * 128, sbase + btile(n) + h * 10240 + (n & 1) * 5120);
      }
#pragma unroll
      for (int n = 20; n < 22; ++n) {
        WBAR(n == 20 ? 10 : 20);
#pragma unroll
        for (int i = 0; i < 5; ++i) {
          const int p = 5 * (n & 1) + i;
          const unsigned o = (oc[p] & ~64u) + 1024 * (i & 3), d = sbase + btile(n) + h * 10240 + p * 1024;
          if (oc[p] & 64u) gldss(o, cim + (1 - co) * 256, d); else gldss(o, cre + (1 - co) * 256, d);
        }
      }
#pragma unroll
      for (int kk = 22 - DB; kk <= 19; ++kk) WBAR(kk < 18 ? 20 + 5 * (19 - ((kk + 2) | 1)) : 0);
    }
    STAMP(8);
    const int tl = 16 * (lw & 1) + r16, n0 = 160 * jl + 80 * (lw >> 1) + 4 * g;
    const size_t orow = (size_t)(128 * rc + 32 * cg + tl) * D_ + n0;
    f32x4 sk[5];
    {
      const float gt = 0.5f * *(const float*)(smem + SP_G + (32 * cg + tl) * 4);
#pragma unroll
      for (int i = 0; i < 5; ++i) {
        u32x2 xv = *(const u32x2*)(a.x + orow + 16 * i);
        u32x2 dv = *(const u32x2*)(a.Dsk + n0 + 16 * i);
        sk[i][0] = gt * bflo(xv[0]) * bflo(dv[0]); sk[i][1] = gt * bfhi(xv[0]) * bfhi(dv[0]);
        sk[i][2] = gt * bflo(xv[1]) * bflo(dv[1]); sk[i][3] = gt * bfhi(xv[1]) * bfhi(dv[1]);
      }
    }
    STAMP(22);
    for (int sp = 0; misc[M_RED] < 4u && sp < SPIN_LDS; ++sp) __builtin_amdgcn_s_sleep(4);
    asm volatile("" ::: "memory");
    STAMP(23);
    poll_flags(rW, OFF_FR + (rc * 4 + jl) * 128, tlo, thi, lane);
    STAMP(24);
    if (rc == 15 && j == 0 && tid == 256) set_flag(rW, OFF_EPOCH, tlo, thi);
    u32x2 pv[3][5];
#pragma unroll
    for (int s = 0; s < 3; ++s) {
      const int sc = (cg + 1 + s) & 3;
#pragma unroll
      for (int i = 0; i < 5; ++i)
        pv[s][i] = __builtin_amdgcn_raw_buffer_load_b64(rW, OFF_PB + (((rc * 4 + jl) * 4 + cg) * 4 + sc) * 10240 + (lw >> 1) * 5120 + i * 1024 + lane * 16 + (lw & 1) * 8, 0, SC1);
    }
#pragma unroll
    for (int i = 0; i < 5; ++i) {
      f32x4 v = *(const f32x4*)(smem + atile(0) + (5 * lw + i) * 1024 + lane * 16) + sk[i];
#pragma unroll
      for (int s = 0; s < 3; ++s) v += (f32x4){bflo(pv[s][i][0]), bfhi(pv[s][i][0]), bflo(pv[s][i][1]), bfhi(pv[s][i][1])};
      *(f32x4*)(a.out + orow + 16 * i) = v;
    }
  } else {
    if (wave == 3) {
      unsigned long long pc; unsigned back, csz;
      asm volatile("s_getpc_b64 %0\n1:\n\ts_mov_b32 %1, 1b-_Z5fused4Args\n\ts_mov_b32 %2, _Z8code_endv-_Z5fused4Args" : "=s"(pc), "=s"(back), "=s"(csz));
      const unsigned co = (idx * 14 + (lane < 14 ? lane : 0)) * 128;
      touch((const char*)(pc - back) + (co + 4 <= csz && csz < (1u << 20) ? co : 0u), sbase + SP_EL);
      touch((const char*)a.w1 + (size_t)(b * 60 + (lane < 60 ? lane : 0)) * 128, sbase + SP_EL);
      touch((const char*)(lane < 32 ? a.Bre : a.Bim) + (size_t)(b * 25 + ((lane & 31) < 25 ? (lane & 31) : 0)) * 128, sbase + SP_EL);
    } else if (wave == 0) {
      touch((const char*)a.x + (size_t)(b * 80 + lane) * 128, sbase + SP_EL);
      touch((const char*)a.x + (size_t)(b * 80 + 64 + r16) * 128, sbase + SP_EL);
      touch((const char*)a.w2 + (size_t)(b * 60 + (lane < 60 ? lane : 0)) * 128, sbase + SP_EL);
      touch((const char*)(lane < 32 ? a.Cre : a.Cim) + (size_t)(b * 25 + ((lane & 31) < 25 ? (lane & 31) : 0)) * 128, sbase + SP_EL);
    }
    if (wave == 1) {
      const u64 tg = __hip_atomic_load((GAS u64*)(ws + OFF_EPOCH), __ATOMIC_RELAXED, __HIP_MEMORY_SCOPE_AGENT) + 1;
      const unsigned v = *(const unsigned*)(a.b1 + 96 * j + 2 * (lane < 48 ? lane : 0));
      const u32x4 g0 = *(const u32x4*)(a.gw + 8 * lane), g1 = *(const u32x4*)(a.gw + 8 * (64 + r16));
      if (lane < 48) *(f32x2*)(smem + SP_B1 + lane * 8) = (f32x2){bflo(v), bfhi(v)};
      *(u32x4*)(smem + SP_GW + lane * 16) = g0;
      if (lane < 16) *(u32x4*)(smem + SP_GW + 1024 + lane * 16) = g1;
      misc[lane] = lane == M_TAG ? (unsigned)tg : lane == M_TAG + 1 ? (unsigned)(tg >> 32) : 0u;
      const unsigned nt = ~(unsigned)tg;
      *(u32x4*)(smem + SP_DF + lane * 16) = (u32x4){nt, nt, nt, nt};
      *(u32x2*)(smem + SP_DF + 1024 + lane * 8) = (u32x2){nt, nt};
    } else if (wave == 2) {
      const int ch = 40 * j + (lane < 40 ? lane : 0);
      const float lre = bflo(a.Lre[ch]), lim = bflo(a.Lim[ch]), dt = expf(bflo(a.ldt[ch]));
      float e = expf(dt * lre), sn, cs;
      sincosf(dt * lim, &sn, &cs);
      f32x2 l1 = {e * cs, e * sn};
      const float nr = l1[0] - 1.f, ni = l1[1], den = lre * lre + lim * lim;
      f32x2 cf = {(nr * lre + ni * lim) / den, (ni * lre - nr * lim) / den};
      f32x2 l2 = cm2(l1, l1), l4 = cm2(l2, l2), l8 = cm2(l4, l4), l16 = cm2(l8, l8);
      f32x2 l32 = cm2(l16, l16), l64 = cm2(l32, l32), l128 = cm2(l64, l64), l96 = cm2(l64, l32);
      if (lane < 40) {
        float* pp = (float*)(smem + SP_PAR + lane * PARS);
        *(f32x4*)(pp) = (f32x4){l1[0], l1[1], l128[0], l128[1]};
        *(f32x4*)(pp + 4) = (f32x4){cf[0], cf[1], l32[0], l32[1]};
        *(f32x4*)(pp + 8) = (f32x4){l64[0], l64[1], l96[0], l96[1]};
      }
    }
    STAMP(1);
    const int th = wave & 1;
    const unsigned xo0 = ST_X + (64 * th + r16) * 128 + ((g ^ (r16 >> 1)) * 16);
    const unsigned xo1 = xo0 ^ 64;
    const int q4 = r16 >> 2, p4 = r16 & 3;

#define P1_PIPE(LOADF, STEP, MMA, INIT)                                             \
    CBAR;                                                                           \
    __builtin_amdgcn_s_setprio(3);                                                  \
    STAMP(2); CSTAMP(12);                                                           \
    INIT;                                                                           \
    LOADF(F0, 0u, 0);                                                               \
    STEP(F0, F1, 0u, 1);                                                            \
    _Pragma("unroll 1") for (int s = 1; s < 10; ++s) {                              \
      CBAR;                                                                         \
      STEP(F1, F0, (s & 3) * ST_SZ, 0);                                             \
      STEP(F0, F1, (s & 3) * ST_SZ, 1);                                             \
    }                                                                               \
    WAITL;                                                                          \
    lds_add(smem, M_P1D, lane);                                                     \
    MMA(F1); SCHED; __builtin_amdgcn_s_setprio(0);                                  \
    STAMP(3); CSTAMP(13);

    auto put_act = [&](int fp, int tile, u32x4 v) {
      st128(rW, OFF_AF + (((rc * 4 + cg) * 22 + 3 * jl + fp) * 8 + tile) * 1024 + lane * 16, v);
      *(u32x4*)(smem + atile(fp) + tile * 1024 + lane * 16) = v;
    };
    unsigned tlo, thi;
    if (wave < 2) {
      f32x4 acc[6][4];
      const unsigned wbase = ST_W1 + (8 * g + q4) * 192 + (p4 >> 1) * 16 + (p4 & 1) * 8;
      const unsigned wa = wbase + (g & 1) * 32;
      const unsigned wb = wbase + ((g & 1) ? 0 : 160);
      struct Frag { bf16x8 x[4], w[6]; };
      Frag F0, F1;
      auto rdw = [&](const char* sb, int ft, int kk) {
        unsigned ad = (ft < 5 ? wa + 32 * ft : wb) + kk * 32 * 192;
        s16x4 lo = __builtin_amdgcn_ds_read_tr16_b64_v4i16((LAS s16x4*)(sb + ad));
        s16x4 hi = __builtin_amdgcn_ds_read_tr16_b64_v4i16((LAS s16x4*)(sb + ad + 4 * 192));
        return __builtin_bit_cast(bf16x8, __builtin_shufflevector(lo, hi, 0, 1, 2, 3, 4, 5, 6, 7));
      };
      auto loadf = [&](Frag& F, unsigned boff, int kk) {
        const char* sb = smem + boff;
#pragma unroll
        for (int tt = 0; tt < 4; ++tt) F.x[tt] = *(const bf16x8*)(sb + (kk ? xo1 : xo0) + tt * 2048);
#pragma unroll
        for (int ft = 0; ft < 6; ++ft) F.w[ft] = rdw(sb, ft, kk);
      };
      auto mma = [&](Frag& F) {
#pragma unroll
        for (int m = 0; m < 24; ++m)
          acc[m >> 2][m & 3] = __builtin_amdgcn_mfma_f32_16x16x32_bf16(F.w[m >> 2], F.x[m & 3], acc[m >> 2][m & 3], 0, 0, 0);
      };
      auto step = [&](Frag& C, Frag& N, unsigned boff, const int kk) {
        const char* sb = smem + boff;
#pragma unroll
        for (int m = 0; m < 24; ++m) {
          SCHED;
          acc[m >> 2][m & 3] = __builtin_amdgcn_mfma_f32_16x16x32_bf16(C.w[m >> 2], C.x[m & 3], acc[m >> 2][m & 3], 0, 0, 0);
          SCHED;
          if (m < 4) N.x[m] = *(const bf16x8*)(sb + (kk ? xo1 : xo0) + m * 2048);
          if (m >= 4 && m < 10) N.w[m - 4] = rdw(sb, m - 4, kk);
        }
        SCHED;
      };
      auto init = [&]() {
#pragma unroll
        for (int ft = 0; ft < 6; ++ft) {
          f32x4 bv = *(const f32x4*)(smem + SP_B1 + (16 * ft + 4 * g) * 4);
#pragma unroll
          for (int tt = 0; tt < 4; ++tt) acc[ft][tt] = bv;
        }
      };
      P1_PIPE(loadf, step, mma, init());
      tlo = misc[M_TAG]; thi = misc[M_TAG + 1];
      lds_wait_ge(misc + M_P1D, 4u);
#pragma unroll
      for (int i = 0; i < 6; ++i) *(f32x4*)(smem + L_SCR + th * 6144 + i * 1024 + lane * 16) = acc[i][3];
      WAITL;
      misc[M_OFF + th] = 1u;
#pragma unroll
      for (int tt = 0; tt < 2; ++tt)
#pragma unroll
        for (int i = 0; i < 6; ++i) { acc[i][tt] = mish4(acc[i][tt]); asm volatile("" : "+v"(acc[i][tt])); }
      SCHED;
      lds_wait_ge(misc + M_GRDY, 2u);
#pragma unroll
      for (int tt = 0; tt < 3; ++tt) {
        const float og = 1.f - 0.5f * *(const float*)(smem + SP_G + (64 * th + 16 * tt + r16) * 4);
#pragma unroll
        for (int fp = 0; fp < 3; ++fp) {
          if (tt == 2) { acc[2 * fp][2] = mish4(acc[2 * fp][2]); acc[2 * fp + 1][2] = mish4(acc[2 * fp + 1][2]); }
          put_act(fp, 4 * th + tt, pack8(acc[2 * fp][tt], acc[2 * fp + 1][tt], og));
        }
      }
    } else {
      f32x4 acc[4][5], gac[4];
      const unsigned bo0 = ST_BM + (40 * (r16 & 1) + (r16 >> 1)) * 128 + ((g ^ ((((r16 >> 2) & 3) << 1) | (r16 & 1))) * 16);
      const unsigned bo1 = bo0 ^ 64;
      struct Frag { bf16x8 x[4], w[5], gf; };
      Frag F0, F1;
      int kc = 0;
      auto loadf = [&](Frag& F, unsigned boff, int kk) {
        const char* sb = smem + boff;
#pragma unroll
        for (int tt = 0; tt < 4; ++tt) F.x[tt] = *(const bf16x8*)(sb + (kk ? xo1 : xo0) + tt * 2048);
#pragma unroll
        for (int nt = 0; nt < 5; ++nt) F.w[nt] = *(const bf16x8*)(sb + (kk ? bo1 : bo0) + nt * 1024);
        F.gf = *(const bf16x8*)(smem + SP_GW + kc * 64 + g * 16);
        ++kc;
      };
      auto mma = [&](Frag& F) {
#pragma unroll
        for (int tt = 0; tt < 4; ++tt) gac[tt] = __builtin_amdgcn_mfma_f32_16x16x32_bf16(F.x[tt], F.gf, gac[tt], 0, 0, 0);
#pragma unroll
        for (int m = 0; m < 20; ++m)
          acc[m & 3][m >> 2] = __builtin_amdgcn_mfma_f32_16x16x32_bf16(F.x[m & 3], F.w[m >> 2], acc[m & 3][m >> 2], 0, 0, 0);
      };
      auto step = [&](Frag& C, Frag& N, unsigned boff, const int kk) {
        const char* sb = smem + boff;
#pragma unroll
        for (int m = 0; m < 24; ++m) {
          SCHED;
          if (m < 20) acc[m & 3][m >> 2] = __builtin_amdgcn_mfma_f32_16x16x32_bf16(C.x[m & 3], C.w[m >> 2], acc[m & 3][m >> 2], 0, 0, 0);
          else gac[m & 3] = __builtin_amdgcn_mfma_f32_16x16x32_bf16(C.x[m & 3], C.gf, gac[m & 3], 0, 0, 0);
          SCHED;
          if (m < 4) N.x[m] = *(const bf16x8*)(sb + (kk ? xo1 : xo0) + m * 2048);
          if (m >= 4 && m < 9) N.w[m - 4] = *(const bf16x8*)(sb + (kk ? bo1 : bo0) + (m - 4) * 1024);
          if (m == 9) N.gf = *(const bf16x8*)(smem + SP_GW + kc * 64 + g * 16);
        }
        ++kc;
        SCHED;
      };
      auto init = [&]() {
#pragma unroll
        for (int tt = 0; tt < 4; ++tt) {
          gac[tt] = (f32x4){0.f, 0.f, 0.f, 0.f};
#pragma unroll
          for (int nt = 0; nt < 5; ++nt) acc[tt][nt] = (f32x4){0.f, 0.f, 0.f, 0.f};
        }
      };
      P1_PIPE(loadf, step, mma, init());
      tlo = misc[M_TAG]; thi = misc[M_TAG + 1];
      {
        char* bw = smem + L_BU + r16 * BUS + th * 288 + g * 16;
#pragma unroll
        for (int tt = 0; tt < 4; ++tt)
#pragma unroll
          for (int nt = 0; nt < 5; ++nt) *(f32x4*)(bw + nt * 16 * BUS + (tt >> 1) * 144 + (tt & 1) * 64) = acc[tt][nt];
        WAITL;
        lds_add(smem, M_BUW, lane);
        f32x4 lg = r16 == 0 ? gac[0] : r16 == 1 ? gac[1] : r16 == 2 ? gac[2] : gac[3];
        f32x4 gg;
#pragma unroll
        for (int r = 0; r < 4; ++r) gg[r] = 2.f * __builtin_amdgcn_rcpf(1.f + __expf(-lg[r]));
        if (r16 < 4) *(f32x4*)(smem + SP_G + (64 * th + 16 * r16 + 4 * g) * 4) = gg;
        WAITL;
        lds_add(smem, M_GRDY, lane);
      }
      STAMP(4);
      lds_wait_ge(misc + M_OFF + th, 1u);
      const float og = 1.f - 0.5f * *(const float*)(smem + SP_G + (64 * th + 48 + r16) * 4);
#pragma unroll
      for (int fp = 0; fp < 3; ++fp) {
        const char* sp = smem + L_SCR + th * 6144 + fp * 2048 + lane * 16;
        put_act(fp, 4 * th + 3, pack8(mish4(*(const f32x4*)sp), mish4(*(const f32x4*)(sp + 1024)), og));
      }
    }
    STAMP(18);
    asm volatile("s_waitcnt vmcnt(0) lgkmcnt(0)" ::: "memory");
    if (lds_inc(smem, M_ACT, lane) == 3u && lane == 0) set_flag(rW, OFF_FA + (rc * 4 + cg) * 128 + jl * 8, tlo, thi);
    STAMP(5);

    const int wn = wave & 1, wt = wave >> 1;
    f32x4 acc[5][4];
#pragma unroll
    for (int nt = 0; nt < 5; ++nt)
#pragma unroll
      for (int tt = 0; tt < 4; ++tt) acc[nt][tt] = (f32x4){0.f, 0.f, 0.f, 0.f};
    struct Frag2 { bf16x8 x[4], w[5]; };
    Frag2 G0, G1;
    const unsigned ao = 4 * wt * 1024 + lane * 16;
    const unsigned twb = (4 * g + q4) * 320 + (p4 >> 1) * 16 + (p4 & 1) * 8;
    const unsigned twa = twb + (5 * wn + (g & 1)) * 32;
    const unsigned twl = (wn == 1 && (g & 1)) ? twb : twa + 128;
    const unsigned cwo = (80 * wn + r16) * 128 + ((g ^ (r16 >> 1)) * 16);
    const unsigned hxo = (p4 >> 1) * 2048 + (8 * g + q4) * 64 + (((2 * wt) ^ (((g & 1) << 1) | (p4 >> 1))) * 16) + (p4 & 1) * 8;
    constexpr int A0 = atile(0), A1 = atile(4), B1 = btile(0);
    unsigned ba[2] = {ao + A0, ao + A1}, bta[2] = {twa, twa + B1}, btl[2] = {twl, twl + B1};
    unsigned bc[4] = {cwo, cwo + B1, cwo ^ 64, (cwo ^ 64) + B1};
    unsigned bh[4] = {hxo + A0, hxo + A1, (hxo ^ 16) + A0, (hxo ^ 16) + A1};
    unsigned mb = SP_DF;
    asm volatile("" : "+v"(bh[0]), "+v"(bh[1]), "+v"(bh[2]), "+v"(bh[3]), "+v"(bc[0]), "+v"(bc[1]), "+v"(bc[2]), "+v"(bc[3]));
    asm volatile("" : "+v"(ba[0]), "+v"(ba[1]), "+v"(bta[0]), "+v"(bta[1]), "+v"(btl[0]), "+v"(btl[1]), "+v"(mb));
    auto half = [&](bf16x8& d, const int h, unsigned ad) {
      const s16x4 v = __builtin_amdgcn_ds_read_tr16_b64_v4i16((LAS s16x4*)(smem + ad));
      u32x4 t = __builtin_bit_cast(u32x4, d); const u32x2 vv = __builtin_bit_cast(u32x2, v);
      t[2 * h] = vv[0]; t[2 * h + 1] = vv[1];
      d = __builtin_bit_cast(bf16x8, t);
    };
    auto rd2 = [&](Frag2& N, const int n, const int r) {
      const int ha = (n >> 2) & 1, sa = atile(n) - (ha ? A1 : A0), hb = btile(n) >= B1, so = btile(n) - (hb ? B1 : 0);
      if (n >= 12) {
        if (r < 8) half(N.x[r >> 1], r & 1, bh[(r >> 2) * 2 + ha] + sa + ((r >> 1) & 1) * 4096 + (r & 1) * 256);
        else if (r < 13) N.w[r - 8] = *(const bf16x8*)(smem + bc[(n & 1) * 2 + hb] + so + (r - 8) * 2048);
      } else if (r < 4) N.x[r] = *(const bf16x8*)(smem + ba[ha] + sa + r * 1024);
      else if (r < 14) half(N.w[(r - 4) >> 1], r & 1, (r < 12 ? bta[hb] + 32 * ((r - 4) >> 1) : btl[hb]) + so + (r & 1) * 16 * 320);
    };
    auto wait_mark = [&](int st) {
      for (int sp = 0; *(volatile LAS unsigned*)(smem + mb + (st - 3) * 256) != tlo && sp < SPIN_LDS; ++sp) __builtin_amdgcn_s_sleep(1);
      asm volatile("" ::: "memory");
    };
    bool fok = false;
    auto step2 = [&](Frag2& C, Frag2& N, const int n) {
      if (n >= 2 && n <= K0 && __builtin_expect(!fok, 0)) wait_mark(n + 1);
      unsigned fpre = 0u;
#pragma unroll
      for (int m = 0; m < 20; ++m) {
        SCHED;
        acc[m >> 2][m & 3] = __builtin_amdgcn_mfma_f32_16x16x32_bf16(C.w[m >> 2], C.x[m & 3], acc[m >> 2][m & 3], 0, 0, 0);
        SCHED;
        if (n < 21) {
          rd2(N, n + 1, m);
          if (m == 14 && n >= 1 && n < K0) fpre = *(volatile LAS unsigned*)(smem + mb + (n - 1) * 256);
          if (m == 17) __builtin_amdgcn_s_waitcnt(0xC07F);
          if (m == 18 && n == 1) lds_add(smem, M_OWN, lane);
          if (m == 18 && n >= 1 && n < K0) fok = __builtin_amdgcn_ballot_w64(fpre != tlo) == 0ull;
          if (m == 18 && n >= K0 && n < 20) asm volatile("s_barrier" ::: "memory");
        }
      }
      SCHED;
    };
    lds_wait_ge(misc + ((lane & 1) ? M_ACT : M_SRD), 4u);
    __builtin_amdgcn_s_setprio(1);
    STAMP(6); CSTAMP(16);
#pragma unroll
    for (int r = 0; r < 14; ++r) rd2(G0, 0, r);
    __builtin_amdgcn_s_waitcnt(0xC07F);
#pragma unroll
    for (int n = 0; n < 22; n += 2) {
      if (n == 2) { STAMP(19); }
      if (n == 4) { STAMP(20); }
      if (n == 10) { STAMP(7); CSTAMP(17); }
      if (n == 12) { STAMP(8); CSTAMP(14); }
      step2(G0, G1, n); step2(G1, G0, n + 1);
    }
    __builtin_amdgcn_s_setprio(0);
    STAMP(9); CSTAMP(15);
#pragma unroll
    for (int t2 = 0; t2 < 2; ++t2) {
      const int owner = 2 * wt + t2;
      if (owner != cg) {
        const unsigned pb = OFF_PB + (((rc * 4 + jl) * 4 + owner) * 4 + cg) * 10240 + wn * 5120 + lane * 16;
#pragma unroll
        for (int nt = 0; nt < 5; ++nt) {
          const f32x4 v0 = acc[nt][2 * t2], v1 = acc[nt][2 * t2 + 1];
          u32x4 o; o[0] = pack2(v0[0], v0[1]); o[1] = pack2(v0[2], v0[3]); o[2] = pack2(v1[0], v1[1]); o[3] = pack2(v1[2], v1[3]);
          st128(rW, pb + nt * 1024, o);
        }
      }
    }
#pragma unroll
    for (int tt = 0; tt < 4; ++tt) {
      if (2 * wt + (tt >> 1) == cg) {
#pragma unroll
        for (int nt = 0; nt < 5; ++nt) *(f32x4*)(smem + atile(0) + (5 * (2 * wn + (tt & 1)) + nt) * 1024 + lane * 16) = acc[nt][tt];
      }
    }
    STAMP(10);
    asm volatile("s_waitcnt vmcnt(0) lgkmcnt(0)" ::: "memory");
    if (lds_inc(smem, M_RED2, lane) == 3u) {
      if (lane == 0) { set_flag(rW, OFF_FR + (rc * 4 + jl) * 128 + cg * 8, tlo, thi); misc[M_RED] = 4u; }
    }
    STAMP(11);
  }
  STAMP(25);
  STAMP(26);
  STAMP_OUT(0);
}

__global__ void code_end() {}

extern "C" void kernel_launch(void* const* d_in, const int* in_sizes, int n_in,
                              void* d_out, int out_size, void* d_ws, size_t ws_size,
                              hipStream_t stream) {
  Args a;
  a.x   = (const u16*)d_in[0];
  a.Lre = (const u16*)d_in[1];
  a.Lim = (const u16*)d_in[2];
  a.Bre = (const u16*)d_in[3];
  a.Bim = (const u16*)d_in[4];
  a.Cre = (const u16*)d_in[5];
  a.Cim = (const u16*)d_in[6];
  a.ldt = (const u16*)d_in[7];
  a.Dsk = (const u16*)d_in[8];
  a.w1  = (const u16*)d_in[10];
  a.b1  = (const u16*)d_in[11];
  a.w2  = (const u16*)d_in[12];
  a.gw  = (const u16*)d_in[13];
  a.ws  = (char*)d_ws;
  a.out = (float*)d_out;
  fused<<<256, 512, 0, stream>>>(a);
}
```

```cpp
#include <hip/hip_runtime.h>
#include <hip/hip_bf16.h>
#include <stdint.h>
#pragma clang diagnostic ignored "-Winline-asm"

typedef __attribute__((ext_vector_type(8))) __bf16 bf16x8;
typedef __attribute__((ext_vector_type(2))) __bf16 bf16x2;
typedef __attribute__((ext_vector_type(4))) float f32x4;
typedef __attribute__((ext_vector_type(2))) float f32x2;
typedef __attribute__((ext_vector_type(4))) unsigned u32x4;
typedef __attribute__((ext_vector_type(2))) unsigned u32x2;
typedef __attribute__((ext_vector_type(4))) short s16x4;
typedef unsigned short u16;
typedef unsigned long long u64;
#define LAS __attribute__((address_space(3)))
#define GAS __attribute__((address_space(1)))
#define DI __device__ __forceinline__

constexpr int D_ = 640, P_ = 640, F_ = 1536;

constexpr unsigned OFF_AF = 0;
constexpr unsigned OFF_PB = OFF_AF + 64u * 22 * 8192;
constexpr unsigned OFF_E  = OFF_PB + 64u * 16 * 10240;
constexpr unsigned OFF_FA = OFF_E + 256u * 640;
constexpr unsigned OFF_FH = OFF_FA + 256 * 128;
constexpr unsigned OFF_MK = OFF_FH + 256 * 128;
constexpr unsigned OFF_FR = OFF_MK + 256 * 128;
constexpr unsigned OFF_EPOCH = OFF_FR + 64 * 128;
constexpr unsigned WS_BYTES = OFF_EPOCH + 256;

#define STAMP(i)
#define CSTAMP(i)
#define STAMP_DECL
#define SCAN_ARG
#define SCAN_PASS
#define STAMP_OUT(kid)

DI float bflo(unsigned v) { return __uint_as_float(v << 16); }
DI float bfhi(unsigned v) { return __uint_as_float(v & 0xffff0000u); }
DI unsigned pack2(float a, float b) {
  bf16x2 r; r[0] = (__bf16)a; r[1] = (__bf16)b; return __builtin_bit_cast(unsigned, r);
}
DI f32x2 cm2(f32x2 a, f32x2 b) { return (f32x2){a[0] * b[0] - a[1] * b[1], a[0] * b[1] + a[1] * b[0]}; }
DI float sfma(float a, float b, float c) { float d; asm("v_fma_f32 %0, %1, %2, %3" : "=v"(d) : "v"(a), "v"(b), "v"(c)); return d; }
DI float smul(float a, float b) { float d; asm("v_mul_f32 %0, %1, %2" : "=v"(d) : "v"(a), "v"(b)); return d; }
template <int I> DI float qb(float v) {
  return __int_as_float(__builtin_amdgcn_mov_dpp(__float_as_int(v), I * 0x55, 0xf, 0xf, true));
}

DI f32x2 mish2(f32x2 h) {
  const f32x2 z = h * 1.44269504088896341f;
  f32x2 w; w[0] = __builtin_amdgcn_exp2f(z[0]); w[1] = __builtin_amdgcn_exp2f(z[1]);
  const f32x2 t = __builtin_elementwise_fma(w, (f32x2){-0.5f, -0.5f}, (f32x2){-1.f, -1.f});
  const f32x2 d = __builtin_elementwise_fma(w, t, (f32x2){-1.f, -1.f});
  f32x2 r; r[0] = __builtin_amdgcn_rcpf(d[0]); r[1] = __builtin_amdgcn_rcpf(d[1]);
  return __builtin_elementwise_fma(h, r, h);
}
DI f32x4 mish4(f32x4 a) {
  const f32x2 m0 = mish2((f32x2){a[0], a[1]}), m1 = mish2((f32x2){a[2], a[3]});
  return (f32x4){m0[0], m0[1], m1[0], m1[1]};
}
DI u32x4 pack8(f32x4 a0, f32x4 a1, float og) {
  a0 *= og; a1 *= og;
  return (u32x4){pack2(a0[0], a0[1]), pack2(a0[2], a0[3]), pack2(a1[0], a1[1]), pack2(a1[2], a1[3])};
}

#define SC1 16
#define SCHED __builtin_amdgcn_sched_barrier(0)
#define RSRC __amdgpu_buffer_rsrc_t
DI void st128(RSRC r, unsigned off, u32x4 v) { __builtin_amdgcn_raw_buffer_store_b128(v, r, off, 0, SC1); }
DI void set_flag(RSRC r, unsigned off, unsigned tlo, unsigned thi) { __builtin_amdgcn_raw_buffer_store_b64((u32x2){tlo, thi}, r, off, 0, SC1); }

DI void touch(const void* gsrc, unsigned lds_dst) {
  asm volatile("s_mov_b32 m0, %1\n\ts_nop 0\n\tglobal_load_lds_dword %0, off" :: "v"(gsrc), "s"(lds_dst) : "memory", "m0");
}
DI void gldss(unsigned voff, const void* sbase_, unsigned lds_dst) {
  asm volatile("s_mov_b32 m0, %2\n\ts_nop 0\n\tglobal_load_lds_dwordx4 %0, %1" :: "v"(voff), "s"(sbase_), "s"(lds_dst) : "memory", "m0");
}
DI void gld4c(unsigned voff, const void* sbase_, unsigned lds_dst) {
  asm volatile("s_mov_b32 m0, %2\n\ts_nop 0\n\tglobal_load_lds_dwordx4 %0, %1 sc1\n\tglobal_load_lds_dwordx4 %0, %1 offset:1024 sc1\n\t"
               "global_load_lds_dwordx4 %0, %1 offset:2048 sc1\n\tglobal_load_lds_dwordx4 %0, %1 offset:3072 sc1"
               :: "v"(voff), "s"(sbase_), "s"(lds_dst) : "memory", "m0");
}
DI void gld5(const unsigned* v, const void* sbase_, unsigned lds_dst) {
  asm volatile("s_mov_b32 m0, %5\n\ts_nop 0\n\tglobal_load_lds_dwordx4 %0, %4\n\tglobal_load_lds_dwordx4 %1, %4 offset:1024\n\t"
               "global_load_lds_dwordx4 %2, %4 offset:2048\n\tglobal_load_lds_dwordx4 %3, %4 offset:3072"
               :: "v"(v[0]), "v"(v[1]), "v"(v[2]), "v"(v[3]), "s"(sbase_), "s"(lds_dst) : "memory", "m0");
  gldss(v[4], sbase_, lds_dst + 4096);
}
DI void gld1c(unsigned voff, const void* sbase_, unsigned lds_dst) {
  asm volatile("s_mov_b32 m0, %2\n\ts_nop 0\n\tglobal_load_lds_dword %0, %1 sc1" :: "v"(voff), "s"(sbase_), "s"(lds_dst) : "memory", "m0");
}

constexpr int ST_X = 0, ST_BM = 16384, ST_W1 = 26624, ST_SZ = 38912;
constexpr int SP = 4 * ST_SZ;
constexpr int PARS = 48;
constexpr int SP_PAR = SP, SP_HF = SP + 1920, SP_DF = SP + 2176, SP_B1 = SP + 4224, SP_EL = SP + 4608,
              SP_GW = SP + 4928, SP_G = SP + 6208, SP_MISC = SP + 6720, LDS_BYTES = SP + 6976;
constexpr int BUS = 560, L_BU = SP - 80 * BUS;
constexpr int L_SCR = 65536;
DI constexpr int atile(int n) { return 40960 + 8192 * (n & 7) + ((n & 4) ? 45056 : 0); }
DI constexpr int btile(int n) {
  return n >= 12 ? (n < 14 ? 20480 : n < 16 ? 98304 : n < 18 ? 77824 : n < 20 ? 0 : 20480)
       : (n & 7) < 2 ? 77824 + 10240 * (n & 7) : (n & 7) < 6 ? 10240 * ((n & 7) - 2) : 98304 + 10240 * (n - 6);
}
enum { M_GRDY = 0, M_P1D, M_ACT, M_SRD, M_RED, M_RED2, M_OFF, M_OFF2, M_OWN, M_BUW, M_R2, M_TAG = 16 };
constexpr int K0 = 8;
constexpr int DA = 5, DB = 6;
constexpr int SPIN_LDS = 1 << 22, SPIN_G = 1 << 19;

struct Args {
  const u16 *x, *Lre, *Lim, *Bre, *Bim, *Cre, *Cim, *ldt, *Dsk, *w1, *b1, *w2, *gw;
  char* ws;
  float* out;
};

#define CBAR do { SCHED; asm volatile("s_waitcnt lgkmcnt(0)\n\ts_barrier" ::: "memory"); SCHED; } while (0)
#define WBAR(n) asm volatile("s_waitcnt vmcnt(%0)\n\ts_barrier" :: "n"(n) : "memory")
#define WAITV(n) asm volatile("s_waitcnt vmcnt(%0)" :: "n"(n) : "memory")
#define WAITL asm volatile("s_waitcnt lgkmcnt(0)" ::: "memory")

DI void lds_wait_ge(volatile LAS unsigned* w, unsigned v) {
  for (int sp = 0; !__all(*w >= v) && sp < SPIN_LDS; ++sp) __builtin_amdgcn_s_sleep(1);
  asm volatile("" ::: "memory");
}
DI unsigned lds_inc(char* smem, int word, int lane) {
  unsigned old = 0;
  asm volatile("; wave converged" ::: "memory");
  if (lane == 0) old = __hip_atomic_fetch_add((LAS unsigned*)(smem + SP_MISC) + word, 1u, __ATOMIC_RELAXED, __HIP_MEMORY_SCOPE_WORKGROUP);
  asm volatile("; wave converged" : "+v"(old) :: "memory");
  return __builtin_amdgcn_readfirstlane(old);
}
DI void lds_add(char* smem, int word, int lane) {
  asm volatile("; wave converged" ::: "memory");
  if (lane == 0) __hip_atomic_fetch_add((LAS unsigned*)(smem + SP_MISC) + word, 1u, __ATOMIC_RELAXED, __HIP_MEMORY_SCOPE_WORKGROUP);
  asm volatile("; wave converged" ::: "memory");
}
DI void poll_flags(RSRC rW, unsigned off, unsigned tlo, unsigned thi, int lane) {
  const unsigned o = off + (lane & 3) * 8;
#pragma unroll 1
  for (int sp = 0; sp < SPIN_G; ++sp) {
    const u32x2 f = __builtin_amdgcn_raw_buffer_load_b64(rW, o, 0, SC1);
    if (__all(f[0] == tlo && f[1] == thi)) break;
    __builtin_amdgcn_s_sleep(1);
    asm volatile("" ::: "memory");
  }
  asm volatile("" ::: "memory");
}

typedef __attribute__((ext_vector_type(8))) unsigned u32x8;
DI u32x8 sld8(const void* p) { u32x8 v; asm volatile("s_load_dwordx8 %0, %1, 0x0 glc" : "=s"(v) : "s"(p) : "memory"); return v; }
template <int N> DI bool tags(const u32x8& v, unsigned tlo, unsigned thi) {
  bool ok = true;
#pragma unroll
  for (int i = 0; i < N; ++i) ok = ok && v[2 * i] == tlo && v[2 * i + 1] == thi;
  return ok;
}
#define SWAIT(...) asm volatile("s_waitcnt lgkmcnt(0)" : __VA_ARGS__ :: "memory")

DI void scan_task(RSRC rW, char* smem, volatile LAS unsigned* misc, int u, int lane, int rc, int cg, int jl, unsigned tlo, unsigned thi SCAN_ARG) {
  const int j = 4 * cg + jl, seg = lane & 3;
  const bool act = 16 * u + (lane >> 2) < 40;
  const int chl = act ? 16 * u + (lane >> 2) : 39;
  const unsigned OOB = 0x7fffffffu;
  const float* pp = (const float*)(smem + SP_PAR + chl * PARS);
  const f32x4 la = *(const f32x4*)pp;
  const f32x4 cl = *(const f32x4*)(pp + 4);
  f32x2 pw = seg == 1 ? (f32x2){cl[2], cl[3]} : *(const f32x2*)(pp + 8 + (seg == 3 ? 2 : 0));
  if (seg == 0) pw = (f32x2){1.f, 0.f};
  const float lr = la[0], li = la[1], nli = -la[1];
  const f32x2 l256 = cm2((f32x2){la[2], la[3]}, (f32x2){la[2], la[3]}), l512 = cm2(l256, l256);
  const int r0 = rc - 16 + 4 * seg;
  unsigned eo[4];
#pragma unroll
  for (int i = 0; i < 4; ++i) eo[i] = r0 + i >= 0 ? OFF_E + ((j * 16 + r0 + i) * 40 + chl) * 16 : OOB;
  const int c = 40 * jl + chl, k = c & 31;
  const unsigned ho = act ? OFF_AF + ((rc * 4 + cg) * 22 + 12 + (c >> 5)) * 8192 + k * 64 + ((seg ^ ((k >> 2) & 2)) * 16) : OOB - 65536;
  STAMP(9);
  lds_wait_ge(misc + M_BUW, 2u);
  STAMP(10);
  __builtin_amdgcn_s_setprio(3);
  f32x4 ar[8], ai[8];
  {
    const char* bp = smem + L_BU + 2 * chl * BUS + seg * 144;
#pragma unroll
    for (int i = 0; i < 8; ++i) { ar[i] = *(const f32x4*)(bp + i * 16); ai[i] = *(const f32x4*)(bp + BUS + i * 16); }
  }
  WAITV(0);
  float sr = 0.f, si = 0.f;
#pragma unroll
  for (int t = 0; t < 32; ++t) {
    const float tr = fmaf(nli, si, ar[t >> 2][t & 3]), ti = fmaf(li, sr, ai[t >> 2][t & 3]);
    sr = fmaf(lr, sr, tr); si = fmaf(lr, si, ti);
  }
  STAMP(11);
  float qr = qb<0>(sr), qi = qb<0>(si);
  qr = seg > 0 ? qr : 0.f; qi = seg > 0 ? qi : 0.f;
  {
    float nr = fmaf(cl[2], qr, fmaf(-cl[3], qi, qb<1>(sr))), ni = fmaf(cl[2], qi, fmaf(cl[3], qr, qb<1>(si)));
    qr = seg > 1 ? nr : qr; qi = seg > 1 ? ni : qi;
    nr = fmaf(cl[2], qr, fmaf(-cl[3], qi, qb<2>(sr))); ni = fmaf(cl[2], qi, fmaf(cl[3], qr, qb<2>(si)));
    qr = seg > 2 ? nr : qr; qi = seg > 2 ? ni : qi;
    const float er = fmaf(cl[2], qr, fmaf(-cl[3], qi, sr)), ei = fmaf(cl[2], qi, fmaf(cl[3], qr, si));
    st128(rW, act && seg == 3 ? OFF_E + ((j * 16 + rc) * 40 + chl) * 16 : OOB, (u32x4){__float_as_uint(er), __float_as_uint(ei), tlo, thi});
  }
  STAMP(13);
  __builtin_amdgcn_s_setprio(0);
  lds_add(smem, M_SRD, lane);
#pragma unroll
  for (int i = 0; i < 8; ++i)
#pragma unroll
    for (int r = 0; r < 4; ++r) {
      const float xr = fmaf(cl[0], ar[i][r], -cl[1] * ai[i][r]), xi = fmaf(cl[0], ai[i][r], cl[1] * ar[i][r]);
      ar[i][r] = xr; ai[i][r] = xi;
    }
  lds_wait_ge(misc + M_GRDY, 2u);
  f32x4 gv[8];
#pragma unroll
  for (int i = 0; i < 8; ++i) gv[i] = *(const f32x4*)(smem + SP_G + seg * 128 + i * 16);
  STAMP(14);
  {
    u32x4 ev[4];
#pragma unroll 1
    for (int sp = 0; sp < SPIN_G; ++sp) {
      bool ok = true;
#pragma unroll
      for (int i = 0; i < 4; ++i) ev[i] = __builtin_amdgcn_raw_buffer_load_b128(rW, eo[i], 0, SC1);
#pragma unroll
      for (int i = 0; i < 4; ++i) ok = ok && (r0 + i < 0 || (ev[i][2] == tlo && ev[i][3] == thi));
      if (__all(ok)) break;
      __builtin_amdgcn_s_sleep(1);
      asm volatile("" ::: "memory");
    }
    STAMP(15);
    __builtin_amdgcn_s_setprio(3);
    float cr = __uint_as_float(ev[0][0]), ci = __uint_as_float(ev[0][1]);
#pragma unroll
    for (int i = 1; i < 4; ++i) {
      const float tr = sfma(-la[3], ci, __uint_as_float(ev[i][0])), ti = sfma(la[3], cr, __uint_as_float(ev[i][1]));
      cr = sfma(la[2], cr, tr); ci = sfma(la[2], ci, ti);
    }
    float dr = qb<0>(cr), di = qb<0>(ci), tr, ti;
    tr = sfma(-l512[1], di, qb<1>(cr)); ti = sfma(l512[1], dr, qb<1>(ci)); dr = sfma(l512[0], dr, tr); di = sfma(l512[0], di, ti);
    tr = sfma(-l512[1], di, qb<2>(cr)); ti = sfma(l512[1], dr, qb<2>(ci)); dr = sfma(l512[0], dr, tr); di = sfma(l512[0], di, ti);
    tr = sfma(-l512[1], di, qb<3>(cr)); ti = sfma(l512[1], dr, qb<3>(ci)); dr = sfma(l512[0], dr, tr); di = sfma(l512[0], di, ti);
    tr = sfma(-pw[1], di, qr); ti = sfma(pw[1], dr, qi);
    cr = sfma(pw[0], dr, tr); ci = sfma(pw[0], di, ti);
    sr = sfma(cl[0], cr, smul(-cl[1], ci)); si = sfma(cl[0], ci, smul(cl[1], cr));
  }
  STAMP(16);
#pragma unroll
  for (int i = 0; i < 4; ++i) {
    u32x4 vr, vi;
#pragma unroll
    for (int t2 = 0; t2 < 4; ++t2) {
      float hr[2], hi[2];
#pragma unroll
      for (int e = 0; e < 2; ++e) {
        const int t = 8 * i + 2 * t2 + e;
        const float tr = sfma(nli, si, ar[t >> 2][t & 3]), ti = sfma(li, sr, ai[t >> 2][t & 3]);
        sr = sfma(lr, sr, tr); si = sfma(lr, si, ti);
        hr[e] = smul(sr, gv[t >> 2][t & 3]); hi[e] = smul(-si, gv[t >> 2][t & 3]);
      }
      vr[t2] = pack2(hr[0], hr[1]); vi[t2] = pack2(hi[0], hi[1]);
    }
    __builtin_amdgcn_raw_buffer_store_b128(vr, rW, ho ^ ((i & 1) * 16), i * 2048, SC1);
    __builtin_amdgcn_raw_buffer_store_b128(vi, rW, ho ^ ((i & 1) * 16), i * 2048 + 5 * 8192, SC1);
  }
  STAMP(19);
  WAITV(0);
  STAMP(20);
  if (lane == 0) set_flag(rW, OFF_FH + ((rc * 4 + cg) * 4 + jl) * 128 + u * 8, tlo, thi);
  __builtin_amdgcn_s_setprio(0);
}

__global__ __launch_bounds__(512, 2) void fused(Args a) {
  __shared__ __attribute__((aligned(16))) char smem[LDS_BYTES];
  const int tid = threadIdx.x, lane = tid & 63;
  const int wave = __builtin_amdgcn_readfirstlane(tid >> 6);
  const int b = blockIdx.x, grp = b & 7, idx = b >> 3;
  const int cg = grp & 3, jl = idx & 3;
  const int rc = 8 * (grp >> 2) + (idx >> 2), j = 4 * cg + jl;
  const int r16 = lane & 15, g = lane >> 4;
  char* ws = a.ws;
  const RSRC rW = __builtin_amdgcn_make_buffer_rsrc((void*)ws, 0, WS_BYTES, 0x00020000);
  volatile LAS unsigned* misc = (volatile LAS unsigned*)(smem + SP_MISC);
  const unsigned sbase = (unsigned)(size_t)(LAS char*)smem;
  STAMP_DECL
  STAMP(0);
  const char* wbase2 = (const char*)(a.w2 + (size_t)384 * cg * D_ + 160 * jl);
  auto kmlp = [&](const int n) { return 3 * (jl ^ (n / 3)) + n % 3; };
  auto woffs = [&](unsigned* o, int h) {
#pragma unroll
    for (int i5 = 0; i5 < 5; ++i5) {
      const unsigned i = 5 * h + i5;
      const unsigned v = 4 * i + lane, kq = (v * 205u) >> 12, k = 3 * i + kq, cc = v - 20 * kq - ((k >> 1) & 2);
      o[i5] = k * (D_ * 2) + 16 * min(cc, cc + 20u) - 1024 * (i5 & 3);
    }
  };
  auto issueW = [&](const int n, const unsigned* o, int h) {
    gld5(o, wbase2 + (size_t)kmlp(n) * (32 * D_ * 2), sbase + btile(n) + h * 5120);
  };

  if (wave >= 4) {
    const int lw = wave - 4;
    const int rw = lane >> 3, c8 = lane & 7;
    unsigned ow[5];
    {
      const unsigned vx = rw * (D_ * 2) + 16 * (c8 ^ (rw >> 1) ^ (4 * (lw & 1)));
      const unsigned vb = rw * (D_ * 2) + 16 * (c8 ^ ((rw >> 1) << 1));
      const char* px = (const char*)(a.x + (size_t)(128 * rc + 8 * lw) * D_);
      const char* pre = (const char*)(a.Bre + (size_t)(40 * j + 8 * lw) * D_);
      const char* pim = (const char*)(a.Bim + (size_t)(40 * j + 8 * lw) * D_);
      const char* pex = (const char*)((lw & 1 ? a.Bim : a.Bre) + (size_t)(40 * j + 32) * D_);
      const char* pw = (const char*)(a.w1 + (size_t)16 * lw * F_ + 96 * j);
      const unsigned dl = sbase + lw * 1024, vex = vb ^ ((lw & 1) * 16);
      unsigned vw[3];
      auto stage1 = [&](unsigned boff) {
        if (lw < 2) gldss(vex, pex, sbase + ST_BM + (4 + 5 * lw) * 1024 + boff);
#pragma unroll
        for (int i = 0; i < 4; ++i) gldss(vx + i * (32 * D_ * 2), px, dl + ST_X + i * 4096 + boff);
        gldss(vb, pre, dl + ST_BM + boff);
        gldss(vb ^ 16, pim, dl + ST_BM + 5120 + boff);
        px += 128; pre += 128; pim += 128; pex += 128;
      };
      auto stage2 = [&](unsigned boff) {
#pragma unroll
        for (int i = 0; i < 3; ++i) gldss(vw[i], pw, sbase + ST_W1 + (3 * lw + i) * 1024 + boff);
        pw += 64 * F_ * 2;
      };
      auto stage = [&](unsigned boff) { stage1(boff); stage2(boff); };
      stage1(0);
#pragma unroll
      for (int i = 0; i < 3; ++i) {
        const unsigned v = 64 * i + lane, k = (v * 171u) >> 11, cc = v - 12 * k - ((k >> 2) & 2);
        vw[i] = k * (F_ * 2) + 16 * min(cc, cc + 12u);
      }
      stage2(0); stage(ST_SZ);
      STAMP(1);
      WBAR(9);
      STAMP(2);
      stage(2 * ST_SZ);
      woffs(ow, lw & 1);
#pragma unroll 1
      for (int s = 1; s <= 8; ++s) {
        WBAR(9);
        if (s <= 7) stage(((s + 2) & 3) * ST_SZ);
      }
      if (lw < 2) issueW(0, ow, lw); else issueW(1, ow, lw - 2);
      WBAR(5);
      if (lw < 2) { issueW(2, ow, lw); issueW(6, ow, lw); } else { issueW(3, ow, lw - 2); issueW(4, ow, lw - 2); }
    }
    STAMP(3);
    const unsigned tlo = __builtin_amdgcn_readfirstlane(misc[M_TAG]), thi = __builtin_amdgcn_readfirstlane(misc[M_TAG + 1]);
    const char* abase = ws + OFF_AF + (size_t)(rc * 4 + cg) * 22 * 8192;
    const unsigned aoff = lane * 16;
    const int co = cg & 1;
    auto kstep = [&](int n) { return n < 12 ? kmlp(n) : n < 16 ? n + co : n < 20 ? n + 1 + co : (n == 20 ? 16 : 21) - 4 * co; };
    auto issueA4 = [&](int n, int h) {
      gld4c(aoff, abase + (size_t)kstep(n) * 8192 + h * 4096, sbase + atile(n) + h * 4096);
    };
    if (lw != 1) {
      scan_task(rW, smem, misc, lw == 0 ? 2 : lw - 2, lane, rc, cg, jl, tlo, thi SCAN_PASS);
      STAMP(21);
    } else {
      {
        const int l60 = lane < 60 ? lane : 0, r3 = (l60 * 43) >> 7;
        const unsigned wo = (idx * 120 + l60) * 128, co_ = (20 * idx + r3) * (P_ * 2) + (l60 - 3 * r3) * 128;
        unsigned d0, d1, d2, d3;
        asm volatile("global_load_dword %0, %4, %6\n\tglobal_load_dword %1, %4, %7\n\tglobal_load_dword %2, %5, %8\n\tglobal_load_dword %3, %5, %9\n\ts_waitcnt vmcnt(0)"
                     : "=&v"(d0), "=&v"(d1), "=&v"(d2), "=&v"(d3)
                     : "v"(co_), "v"(wo), "s"(a.Cre + 160 * cg - 32 * co), "s"(a.Cim + 160 * cg - 32 * co), "s"(a.w2 + (size_t)384 * cg * D_), "s"(a.w2 + (size_t)384 * cg * D_ + 60 * 64) : "memory");
      }
      unsigned o0[5];
      woffs(o0, 0);
      __builtin_amdgcn_raw_buffer_store_b32(tlo, rW, OFF_MK + b * 128 + (lane & 31) * 4, 0, 0);
      WAITV(0);
      gld1c(0u, ws + OFF_MK + b * 128, sbase + SP_EL);
      lds_wait_ge(misc + M_SRD, 3u); lds_wait_ge(misc + M_P1D, 4u);
      lds_add(smem, M_SRD, lane);
      issueW(5, o0, 0); issueW(5, ow, 1); issueW(7, o0, 0); issueW(7, ow, 1);
      lds_wait_ge(misc + M_ACT, 4u);
      STAMP(4);
      {
        const char* fp = ws + OFF_FA + (rc * 4 + cg) * 512;
        bool ok = false;
#pragma unroll 1
        for (int sp = 0; sp < 64 && !ok; ++sp) {
          u32x8 f1 = sld8(fp + (jl ^ 1) * 128), f2 = sld8(fp + (jl ^ 2) * 128), f3 = sld8(fp + (jl ^ 3) * 128);
          SWAIT("+s"(f1), "+s"(f2), "+s"(f3));
          ok = tags<4>(f1, tlo, thi) && tags<4>(f2, tlo, thi) && tags<4>(f3, tlo, thi);
        }
        if (!ok) poll_flags(rW, OFF_FA + ((rc * 4 + cg) * 4 + (jl ^ (1 + (g == 3 ? 0 : g)))) * 128, tlo, thi, lane);
      }
      STAMP(5);
#pragma unroll
      for (int n = 3; n < 12; ++n) {
        if (n == 8) lds_wait_ge(misc + M_OWN, 4u);
        if (n == 11) lds_wait_ge(misc + M_R2, 4u);
        if (n >= 8) { issueW(n, o0, 0); issueW(n, ow, 1); }
        issueA4(n, 0); issueA4(n, 1);
        if (n <= K0 + 1) gld1c(0u, ws + OFF_MK + b * 128, sbase + SP_DF + (n - 3) * 256);
      }
    }
    STAMP(6);
    if (lw < 2) {
      const char* fbase = ws + OFF_FH + (rc * 4 + cg) * 512;
      const unsigned hfo = ((lane >> 3) & 3) * 128 + ((lane & 7) < 6 ? lane & 7 : lane & 1) * 4;
      const unsigned want = (lane & 1) ? thi : tlo;
      int k = K0;
      bool hok = false;
      auto look_h = [&]() {
        u32x8 f0 = sld8(fbase), f1 = sld8(fbase + 128), f2 = sld8(fbase + 256), f3 = sld8(fbase + 384);
        SWAIT("+s"(f0), "+s"(f1), "+s"(f2), "+s"(f3));
        return tags<3>(f0, tlo, thi) && tags<3>(f1, tlo, thi) && tags<3>(f2, tlo, thi) && tags<3>(f3, tlo, thi);
      };
#pragma unroll
      for (int kk = K0; kk <= 9; ++kk) {
        if (!hok) {
          u32x8 f0 = sld8(fbase), f1 = sld8(fbase + 128), f2 = sld8(fbase + 256), f3 = sld8(fbase + 384);
          WBAR(kk == 8 ? 18 : 0);
          SWAIT("+s"(f0), "+s"(f1), "+s"(f2), "+s"(f3));
          k = kk + 1;
          hok = tags<3>(f0, tlo, thi) && tags<3>(f1, tlo, thi) && tags<3>(f2, tlo, thi) && tags<3>(f3, tlo, thi);
          asm volatile("" ::: "memory");
        }
      }
#pragma unroll 1
      for (int sp = 0; !hok && sp < 64; ++sp) hok = look_h();
#pragma unroll 1
      for (int sp = 0; !hok && sp < SPIN_G; ++sp) {
        hok = __all(__builtin_amdgcn_raw_buffer_load_b32(rW, OFF_FH + (rc * 4 + cg) * 512 + hfo, 0, SC1) == want);
        asm volatile("" ::: "memory");
      }
#pragma unroll 1
      for (int n = 12; n < k + DA; ++n) issueA4(n, lw);
#pragma unroll 1
      for (; k <= 21 - DA; ++k) { WBAR(4 * (DA - 3)); issueA4(k + DA, lw); }
#pragma unroll
      for (int kk = 22 - DA; kk <= 19; ++kk) WBAR(4 * (19 - kk));
    } else {
      const int h = lw - 2;
      unsigned oc[10];
#pragma unroll
      for (int p = 0; p < 10; ++p) oc[p] = (8 * p + rw) * (P_ * 2) + 16 * (c8 ^ (rw >> 1) ^ (4 * (p & 1))) - 1024 * ((p % 5) & 3);
      const char* cre = (const char*)(a.Cre + (size_t)(160 * jl + 80 * h) * P_ + 160 * cg);
      const char* cim = (const char*)(a.Cim + (size_t)(160 * jl + 80 * h) * P_ + 160 * cg);
      const char* sp[10];
#pragma unroll
      for (int p = 0; p < 10; ++p) {
        sp[p] = ((oc[p] & 64u) ? cim : cre) + (1 - co) * 256 + ((oc[p] & ~64u) + 1024 * ((p % 5) & 3));
        asm volatile("" : "+v"(sp[p]));
      }
      lds_wait_ge(misc + M_R2, 4u);
#pragma unroll
      for (int n = 12; n < 20; ++n) {
        if (n >= K0 + DB) WBAR(5 * (DB - 4));
        gld5(oc + 5 * (n & 1), (n < 16 ? cre : cim) + co * 64 + ((n >> 1) & 1) * 128, sbase + btile(n) + h * 10240 + (n & 1) * 5120);
      }
#pragma unroll
      for (int n = 20; n < 22; ++n) {
        WBAR(5 * (DB - 4));
#pragma unroll
        for (int i = 0; i < 5; ++i)
          asm volatile("s_mov_b32 m0, %1\n\ts_nop 0\n\tglobal_load_lds_dwordx4 %0, off"
                       :: "v"(sp[5 * (n & 1) + i]), "s"(sbase + btile(n) + h * 10240 + (5 * (n & 1) + i) * 1024) : "memory", "m0");
      }
#pragma unroll
      for (int kk = 22 - DB; kk <= 19; ++kk) WBAR(kk < 18 ? 10 : 0);
    }
    STAMP(8);
    const int n0 = 160 * jl + 16 * lw + 4 * g;
    const size_t orow = (size_t)(128 * rc + 32 * cg + r16) * D_ + n0;
    f32x4 sk[3][2];
#pragma unroll
    for (int i = 0; i < 3; ++i) {
      const int c = (i < 2 || lw < 2) ? 64 * i : 0;
      const u32x2 dv = *(const u32x2*)(a.Dsk + n0 + c);
#pragma unroll
      for (int t = 0; t < 2; ++t) {
        const float gt = 0.5f * *(const float*)(smem + SP_G + (32 * cg + 16 * t + r16) * 4);
        const u32x2 xv = *(const u32x2*)(a.x + orow + 16 * t * D_ + c);
        sk[i][t] = (f32x4){gt * bflo(xv[0]) * bflo(dv[0]), gt * bfhi(xv[0]) * bfhi(dv[0]), gt * bflo(xv[1]) * bflo(dv[1]), gt * bfhi(xv[1]) * bfhi(dv[1])};
      }
    }
    STAMP(22);
    for (int sp = 0; misc[M_RED] < 4u && sp < SPIN_LDS; ++sp) __builtin_amdgcn_s_sleep(4);
    asm volatile("" ::: "memory");
    STAMP(23);
    {
      bool ok = false;
#pragma unroll 1
      for (int sp = 0; sp < 64 && !ok; ++sp) {
        u32x8 f1 = sld8(ws + OFF_FR + (rc * 4 + jl) * 128);
        SWAIT("+s"(f1));
        ok = tags<4>(f1, tlo, thi);
      }
      if (!ok) poll_flags(rW, OFF_FR + (rc * 4 + jl) * 128, tlo, thi, lane);
    }
    STAMP(24);
    if (rc == 15 && j == 0 && tid == 256) set_flag(rW, OFF_EPOCH, tlo, thi);
    u32x4 pv[3][3];
#pragma unroll
    for (int i = 0; i < 3; ++i)
#pragma unroll
      for (int s = 0; s < 3; ++s)
        pv[i][s] = __builtin_amdgcn_raw_buffer_load_b128(rW, (i < 2 || lw < 2) ? OFF_PB + (((rc * 4 + jl) * 4 + cg) * 4 + ((cg + 1 + s) & 3)) * 10240 + (lw + 4 * i) * 1024 + lane * 16 : 0x7fffffffu, 0, SC1);
#pragma unroll
    for (int i = 0; i < 3; ++i) {
      if (i < 2 || lw < 2) {
#pragma unroll
        for (int t = 0; t < 2; ++t) {
          f32x4 v = *(const f32x4*)(smem + atile(0) + ((lw + 4 * i) * 2 + t) * 1024 + lane * 16) + sk[i][t];
#pragma unroll
          for (int s = 0; s < 3; ++s) v += (f32x4){bflo(pv[i][s][2 * t]), bfhi(pv[i][s][2 * t]), bflo(pv[i][s][2 * t + 1]), bfhi(pv[i][s][2 * t + 1])};
          *(f32x4*)(a.out + orow + 16 * t * D_ + 64 * i) = v;
        }
      }
    }
  } else {
    if (wave == 3) {
      unsigned long long pc; unsigned back, csz;
      asm volatile("s_getpc_b64 %0\n1:\n\ts_mov_b32 %1, 1b-_Z5fused4Args\n\ts_mov_b32 %2, _Z8code_endv-_Z5fused4Args" : "=s"(pc), "=s"(back), "=s"(csz));
      const unsigned co = (idx * 14 + (lane < 14 ? lane : 0)) * 128;
      touch((const char*)(pc - back) + (co + 4 <= csz && csz < (1u << 20) ? co : 0u), sbase + SP_EL);
      touch((const char*)a.w1 + (size_t)(b * 60 + (lane < 60 ? lane : 0)) * 128, sbase + SP_EL);
      touch((const char*)(lane < 32 ? a.Bre : a.Bim) + (size_t)(b * 25 + ((lane & 31) < 25 ? (lane & 31) : 0)) * 128, sbase + SP_EL);
    } else if (wave == 0) {
      touch((const char*)a.x + (size_t)(b * 80 + lane) * 128, sbase + SP_EL);
      touch((const char*)a.x + (size_t)(b * 80 + 64 + r16) * 128, sbase + SP_EL);
    }
    if (wave == 1) {
      const u64 tg = __hip_atomic_load((GAS u64*)(ws + OFF_EPOCH), __ATOMIC_RELAXED, __HIP_MEMORY_SCOPE_AGENT) + 1;
      const unsigned v = *(const unsigned*)(a.b1 + 96 * j + 2 * (lane < 48 ? lane : 0));
      const u32x4 g0 = *(const u32x4*)(a.gw + 8 * lane), g1 = *(const u32x4*)(a.gw + 8 * (64 + r16));
      if (lane < 48) *(f32x2*)(smem + SP_B1 + lane * 8) = (f32x2){bflo(v), bfhi(v)};
      *(u32x4*)(smem + SP_GW + lane * 16) = g0;
      if (lane < 16) *(u32x4*)(smem + SP_GW + 1024 + lane * 16) = g1;
      misc[lane] = lane == M_TAG ? (unsigned)tg : lane == M_TAG + 1 ? (unsigned)(tg >> 32) : 0u;
      const unsigned nt = ~(unsigned)tg;
      *(u32x4*)(smem + SP_DF + lane * 16) = (u32x4){nt, nt, nt, nt};
      *(u32x4*)(smem + SP_DF + 1024 + lane * 16) = (u32x4){nt, nt, nt, nt};
      *(unsigned*)(smem + SP_HF + lane * 4) = nt;
    } else if (wave == 2) {
      const int ch = 40 * j + (lane < 40 ? lane : 0);
      const float lre = bflo(a.Lre[ch]), lim = bflo(a.Lim[ch]), dt = expf(bflo(a.ldt[ch]));
      float e = expf(dt * lre), sn, cs;
      sincosf(dt * lim, &sn, &cs);
      f32x2 l1 = {e * cs, e * sn};
      const float nr = l1[0] - 1.f, ni = l1[1], den = lre * lre + lim * lim;
      f32x2 cf = {(nr * lre + ni * lim) / den, (ni * lre - nr * lim) / den};
      f32x2 l2 = cm2(l1, l1), l4 = cm2(l2, l2), l8 = cm2(l4, l4), l16 = cm2(l8, l8);
      f32x2 l32 = cm2(l16, l16), l64 = cm2(l32, l32), l128 = cm2(l64, l64), l96 = cm2(l64, l32);
      if (lane < 40) {
        float* pp = (float*)(smem + SP_PAR + lane * PARS);
        *(f32x4*)(pp) = (f32x4){l1[0], l1[1], l128[0], l128[1]};
        *(f32x4*)(pp + 4) = (f32x4){cf[0], cf[1], l32[0], l32[1]};
        *(f32x4*)(pp + 8) = (f32x4){l64[0], l64[1], l96[0], l96[1]};
      }
    }
    STAMP(1);
    const int th = wave & 1;
    const unsigned xo0 = ST_X + (64 * th + r16) * 128 + ((g ^ (r16 >> 1)) * 16);
    const unsigned xo1 = xo0 ^ 64;
    const int q4 = r16 >> 2, p4 = r16 & 3;

#define P1_PIPE(LOADF, STEP, MMA, INIT)                                             \
    CBAR;                                                                           \
    __builtin_amdgcn_s_setprio(3);                                                  \
    STAMP(2); CSTAMP(12);                                                           \
    INIT;                                                                           \
    LOADF(F0, 0u, 0);                                                               \
    STEP(F0, F1, 0u, 1);                                                            \
    _Pragma("unroll 1") for (int s = 1; s < 10; ++s) {                              \
      CBAR;                                                                         \
      STEP(F1, F0, (s & 3) * ST_SZ, 0);                                             \
      STEP(F0, F1, (s & 3) * ST_SZ, 1);                                             \
    }                                                                               \
    WAITL;                                                                          \
    lds_add(smem, M_P1D, lane);                                                     \
    MMA(F1); SCHED; __builtin_amdgcn_s_setprio(0);                                  \
    STAMP(3); CSTAMP(13);

    auto put_act = [&](int fp, int tile, u32x4 v) {
      st128(rW, OFF_AF + (((rc * 4 + cg) * 22 + 3 * jl + fp) * 8 + tile) * 1024 + lane * 16, v);
      *(u32x4*)(smem + atile(fp) + tile * 1024 + lane * 16) = v;
    };
    unsigned tlo, thi;
    if (wave < 2) {
      f32x4 acc[6][4];
      const unsigned wbase = ST_W1 + (8 * g + q4) * 192 + (p4 >> 1) * 16 + (p4 & 1) * 8;
      const unsigned wa = wbase + (g & 1) * 32;
      const unsigned wb = wbase + ((g & 1) ? 0 : 160);
      struct Frag { bf16x8 x[4], w[6]; };
      Frag F0, F1;
      auto rdw = [&](const char* sb, int ft, int kk) {
        unsigned ad = (ft < 5 ? wa + 32 * ft : wb) + kk * 32 * 192;
        s16x4 lo = __builtin_amdgcn_ds_read_tr16_b64_v4i16((LAS s16x4*)(sb + ad));
        s16x4 hi = __builtin_amdgcn_ds_read_tr16_b64_v4i16((LAS s16x4*)(sb + ad + 4 * 192));
        return __builtin_bit_cast(bf16x8, __builtin_shufflevector(lo, hi, 0, 1, 2, 3, 4, 5, 6, 7));
      };
      auto loadf = [&](Frag& F, unsigned boff, int kk) {
        const char* sb = smem + boff;
#pragma unroll
        for (int tt = 0; tt < 4; ++tt) F.x[tt] = *(const bf16x8*)(sb + (kk ? xo1 : xo0) + tt * 2048);
#pragma unroll
        for (int ft = 0; ft < 6; ++ft) F.w[ft] = rdw(sb, ft, kk);
      };
      auto mma = [&](Frag& F) {
#pragma unroll
        for (int m = 0; m < 24; ++m)
          acc[m >> 2][m & 3] = __builtin_amdgcn_mfma_f32_16x16x32_bf16(F.w[m >> 2], F.x[m & 3], acc[m >> 2][m & 3], 0, 0, 0);
      };
      auto step = [&](Frag& C, Frag& N, unsigned boff, const int kk) {
        const char* sb = smem + boff;
#pragma unroll
        for (int m = 0; m < 24; ++m) {
          SCHED;
          acc[m >> 2][m & 3] = __builtin_amdgcn_mfma_f32_16x16x32_bf16(C.w[m >> 2], C.x[m & 3], acc[m >> 2][m & 3], 0, 0, 0);
          SCHED;
          if (m < 4) N.x[m] = *(const bf16x8*)(sb + (kk ? xo1 : xo0) + m * 2048);
          if (m >= 4 && m < 10) N.w[m - 4] = rdw(sb, m - 4, kk);
        }
        SCHED;
      };
      auto init = [&]() {
#pragma unroll
        for (int ft = 0; ft < 6; ++ft) {
          f32x4 bv = *(const f32x4*)(smem + SP_B1 + (16 * ft + 4 * g) * 4);
#pragma unroll
          for (int tt = 0; tt < 4; ++tt) acc[ft][tt] = bv;
        }
      };
      P1_PIPE(loadf, step, mma, init());
      tlo = misc[M_TAG]; thi = misc[M_TAG + 1];
      lds_wait_ge(misc + M_P1D, 4u);
#pragma unroll
      for (int i = 0; i < 6; ++i) *(f32x4*)(smem + L_SCR + th * 6144 + i * 1024 + lane * 16) = acc[i][3];
      WAITL;
      misc[M_OFF + th] = 1u;
#pragma unroll
      for (int tt = 0; tt < 2; ++tt)
#pragma unroll
        for (int i = 0; i < 6; ++i) { acc[i][tt] = mish4(acc[i][tt]); asm volatile("" : "+v"(acc[i][tt])); }
      SCHED;
      lds_wait_ge(misc + M_GRDY, 2u);
#pragma unroll
      for (int tt = 0; tt < 3; ++tt) {
        const float og = 1.f - 0.5f * *(const float*)(smem + SP_G + (64 * th + 16 * tt + r16) * 4);
#pragma unroll
        for (int fp = 0; fp < 3; ++fp) {
          if (tt == 2) { acc[2 * fp][2] = mish4(acc[2 * fp][2]); acc[2 * fp + 1][2] = mish4(acc[2 * fp + 1][2]); }
          put_act(fp, 4 * th + tt, pack8(acc[2 * fp][tt], acc[2 * fp + 1][tt], og));
        }
      }
    } else {
      f32x4 acc[4][5], gac[4];
      const unsigned bo0 = ST_BM + (40 * (r16 & 1) + (r16 >> 1)) * 128 + ((g ^ ((((r16 >> 2) & 3) << 1) | (r16 & 1))) * 16);
      const unsigned bo1 = bo0 ^ 64;
      struct Frag { bf16x8 x[4], w[5], gf; };
      Frag F0, F1;
      int kc = 0;
      auto loadf = [&](Frag& F, unsigned boff, int kk) {
        const char* sb = smem + boff;
#pragma unroll
        for (int tt = 0; tt < 4; ++tt) F.x[tt] = *(const bf16x8*)(sb + (kk ? xo1 : xo0) + tt * 2048);
#pragma unroll
        for (int nt = 0; nt < 5; ++nt) F.w[nt] = *(const bf16x8*)(sb + (kk ? bo1 : bo0) + nt * 1024);
        F.gf = *(const bf16x8*)(smem + SP_GW + kc * 64 + g * 16);
        ++kc;
      };
      auto mma = [&](Frag& F) {
#pragma unroll
        for (int tt = 0; tt < 4; ++tt) gac[tt] = __builtin_amdgcn_mfma_f32_16x16x32_bf16(F.x[tt], F.gf, gac[tt], 0, 0, 0);
#pragma unroll
        for (int m = 0; m < 20; ++m)
          acc[m & 3][m >> 2] = __builtin_amdgcn_mfma_f32_16x16x32_bf16(F.x[m & 3], F.w[m >> 2], acc[m & 3][m >> 2], 0, 0, 0);
      };
      auto step = [&](Frag& C, Frag& N, unsigned boff, const int kk) {
        const char* sb = smem + boff;
#pragma unroll
        for (int m = 0; m < 24; ++m) {
          SCHED;
          if (m < 20) acc[m & 3][m >> 2] = __builtin_amdgcn_mfma_f32_16x16x32_bf16(C.x[m & 3], C.w[m >> 2], acc[m & 3][m >> 2], 0, 0, 0);
          else gac[m & 3] = __builtin_amdgcn_mfma_f32_16x16x32_bf16(C.x[m & 3], C.gf, gac[m & 3], 0, 0, 0);
          SCHED;
          if (m < 4) N.x[m] = *(const bf16x8*)(sb + (kk ? xo1 : xo0) + m * 2048);
          if (m >= 4 && m < 9) N.w[m - 4] = *(const bf16x8*)(sb + (kk ? bo1 : bo0) + (m - 4) * 1024);
          if (m == 9) N.gf = *(const bf16x8*)(smem + SP_GW + kc * 64 + g * 16);
        }
        ++kc;
        SCHED;
      };
      auto init = [&]() {
#pragma unroll
        for (int tt = 0; tt < 4; ++tt) {
          gac[tt] = (f32x4){0.f, 0.f, 0.f, 0.f};
#pragma unroll
          for (int nt = 0; nt < 5; ++nt) acc[tt][nt] = (f32x4){0.f, 0.f, 0.f, 0.f};
        }
      };
      P1_PIPE(loadf, step, mma, init());
      tlo = misc[M_TAG]; thi = misc[M_TAG + 1];
      {
        char* bw = smem + L_BU + r16 * BUS + th * 288 + g * 16;
#pragma unroll
        for (int tt = 0; tt < 4; ++tt)
#pragma unroll
          for (int nt = 0; nt < 5; ++nt) *(f32x4*)(bw + nt * 16 * BUS + (tt >> 1) * 144 + (tt & 1) * 64) = acc[tt][nt];
        WAITL;
        lds_add(smem, M_BUW, lane);
        f32x4 lg = r16 == 0 ? gac[0] : r16 == 1 ? gac[1] : r16 == 2 ? gac[2] : gac[3];
        f32x4 gg;
#pragma unroll
        for (int r = 0; r < 4; ++r) gg[r] = 2.f * __builtin_amdgcn_rcpf(1.f + __expf(-lg[r]));
        if (r16 < 4) *(f32x4*)(smem + SP_G + (64 * th + 16 * r16 + 4 * g) * 4) = gg;
        WAITL;
        lds_add(smem, M_GRDY, lane);
      }
      STAMP(4);
      lds_wait_ge(misc + M_OFF + th, 1u);
      const float og = 1.f - 0.5f * *(const float*)(smem + SP_G + (64 * th + 48 + r16) * 4);
#pragma unroll
      for (int fp = 0; fp < 3; ++fp) {
        const char* sp = smem + L_SCR + th * 6144 + fp * 2048 + lane * 16;
        put_act(fp, 4 * th + 3, pack8(mish4(*(const f32x4*)sp), mish4(*(const f32x4*)(sp + 1024)), og));
      }
    }
    STAMP(18);
    WAITL;
    lds_add(smem, M_ACT, lane);
    auto publish = [&]() { WAITV(0); if (lane == 0) set_flag(rW, OFF_FA + ((rc * 4 + cg) * 4 + jl) * 128 + wave * 8, tlo, thi); };
    if (wave >= 2) publish();
    STAMP(5);

    const int wn = wave & 1, wt = wave >> 1;
    f32x4 acc[5][4];
#pragma unroll
    for (int nt = 0; nt < 5; ++nt)
#pragma unroll
      for (int tt = 0; tt < 4; ++tt) acc[nt][tt] = (f32x4){0.f, 0.f, 0.f, 0.f};
    struct Frag2 { bf16x8 x[4], w[5]; };
    Frag2 G0, G1;
    const unsigned ao = 4 * wt * 1024 + lane * 16;
    const unsigned twb = (4 * g + q4) * 320 + (p4 >> 1) * 16 + (p4 & 1) * 8;
    const unsigned twa = twb + (5 * wn + (g & 1)) * 32;
    const unsigned twl = (wn == 1 && (g & 1)) ? twb : twa + 128;
    const unsigned cwo = (80 * wn + r16) * 128 + ((g ^ (r16 >> 1)) * 16);
    const unsigned hxo = (p4 >> 1) * 2048 + (8 * g + q4) * 64 + (((2 * wt) ^ (((g & 1) << 1) | (p4 >> 1))) * 16) + (p4 & 1) * 8;
    constexpr int A0 = atile(0), A1 = atile(4), B1 = btile(0);
    unsigned ba[2] = {ao + A0, ao + A1}, bta[2] = {twa, twa + B1}, btl[2] = {twl, twl + B1};
    unsigned bc[4] = {cwo, cwo + B1, cwo ^ 64, (cwo ^ 64) + B1};
    unsigned bh[4] = {hxo + A0, hxo + A1, (hxo ^ 16) + A0, (hxo ^ 16) + A1};
    unsigned mb = SP_DF;
    asm volatile("" : "+v"(bh[0]), "+v"(bh[1]), "+v"(bh[2]), "+v"(bh[3]), "+v"(bc[0]), "+v"(bc[1]), "+v"(bc[2]), "+v"(bc[3]));
    asm volatile("" : "+v"(ba[0]), "+v"(ba[1]), "+v"(bta[0]), "+v"(bta[1]), "+v"(btl[0]), "+v"(btl[1]), "+v"(mb));
    auto half = [&](bf16x8& d, const int h, unsigned ad) {
      const s16x4 v = __builtin_amdgcn_ds_read_tr16_b64_v4i16((LAS s16x4*)(smem + ad));
      u32x4 t = __builtin_bit_cast(u32x4, d); const u32x2 vv = __builtin_bit_cast(u32x2, v);
      t[2 * h] = vv[0]; t[2 * h + 1] = vv[1];
      d = __builtin_bit_cast(bf16x8, t);
    };
    auto rd2 = [&](Frag2& N, const int n, const int r) {
      const int ha = (n >> 2) & 1, sa = atile(n) - (ha ? A1 : A0), hb = btile(n) >= B1, so = btile(n) - (hb ? B1 : 0);
      if (n >= 12) {
        if (r < 8) half(N.x[r >> 1], r & 1, bh[(r >> 2) * 2 + ha] + sa + ((r >> 1) & 1) * 4096 + (r & 1) * 256);
        else if (r < 13) N.w[r - 8] = *(const bf16x8*)(smem + bc[(n & 1) * 2 + hb] + so + (r - 8) * 2048);
      } else if (r < 4) N.x[r] = *(const bf16x8*)(smem + ba[ha] + sa + r * 1024);
      else if (r < 14) half(N.w[(r - 4) >> 1], r & 1, (r < 12 ? bta[hb] + 32 * ((r - 4) >> 1) : btl[hb]) + so + (r & 1) * 16 * 320);
    };
    auto wait_mark = [&](int st) {
      for (int sp = 0; *(volatile LAS unsigned*)(smem + mb + (st - 3) * 256) != tlo && sp < SPIN_LDS; ++sp) __builtin_amdgcn_s_sleep(1);
      asm volatile("" ::: "memory");
    };
    unsigned pb[2];
#pragma unroll
    for (int t2 = 0; t2 < 2; ++t2)
      pb[t2] = 2 * wt + t2 != cg ? OFF_PB + (((rc * 4 + jl) * 4 + 2 * wt + t2) * 4 + cg) * 10240 + wn * 5120 + lane * 16 : 0x7fff0000u;
    auto put_part = [&](const int nt) {
#pragma unroll
      for (int t2 = 0; t2 < 2; ++t2) {
        const f32x4 v0 = acc[nt][2 * t2], v1 = acc[nt][2 * t2 + 1];
        st128(rW, pb[t2] + nt * 1024, (u32x4){pack2(v0[0], v0[1]), pack2(v0[2], v0[3]), pack2(v1[0], v1[1]), pack2(v1[2], v1[3])});
      }
    };
    bool fok = false;
    auto step2 = [&](Frag2& C, Frag2& N, const int n) {
      if (n == 1 && wave < 2) publish();
      if (n >= 2 && n <= K0 && __builtin_expect(!fok, 0)) wait_mark(n + 1);
      unsigned fpre = 0u;
#pragma unroll
      for (int m = 0; m < 20; ++m) {
        SCHED;
        acc[m >> 2][m & 3] = __builtin_amdgcn_mfma_f32_16x16x32_bf16(C.w[m >> 2], C.x[m & 3], acc[m >> 2][m & 3], 0, 0, 0);
        SCHED;
        if (n < 21) {
          rd2(N, n + 1, m);
          if (m == 14 && n >= 1 && n < K0) fpre = *(volatile LAS unsigned*)(smem + mb + (n - 1) * 256);
          if (m == 17) __builtin_amdgcn_s_waitcnt(0xC07F);
          if (m == 18 && n == 1) lds_add(smem, M_OWN, lane);
          if (m == 18 && n == 4) lds_add(smem, M_R2, lane);
          if (m == 18 && n >= 1 && n < K0) fok = __builtin_amdgcn_ballot_w64(fpre != tlo) == 0ull;
          if (m == 18 && n >= K0 && n < 20) asm volatile("s_barrier" ::: "memory");
        } else if (m >= 7 && m < 19 && (m & 3) == 3) put_part((m >> 2) - 1);
      }
      SCHED;
    };
    lds_wait_ge(misc + ((lane & 1) ? M_ACT : M_SRD), 4u);
    __builtin_amdgcn_s_setprio(1);
    STAMP(6); CSTAMP(16);
#pragma unroll
    for (int r = 0; r < 14; ++r) rd2(G0, 0, r);
    __builtin_amdgcn_s_waitcnt(0xC07F);
#pragma unroll
    for (int n = 0; n < 22; n += 2) {
      if (n == 2) { STAMP(19); }
      if (n == 4) { STAMP(20); }
      if (n == 10) { STAMP(7); CSTAMP(17); }
      if (n == 12) { STAMP(8); CSTAMP(14); }
      step2(G0, G1, n); step2(G1, G0, n + 1);
    }
    __builtin_amdgcn_s_setprio(0);
    STAMP(9); CSTAMP(15);
    put_part(3); put_part(4);
#pragma unroll
    for (int tt = 0; tt < 4; ++tt) {
      if (2 * wt + (tt >> 1) == cg) {
#pragma unroll
        for (int nt = 0; nt < 5; ++nt) *(f32x4*)(smem + atile(0) + ((5 * wn + nt) * 2 + (tt & 1)) * 1024 + lane * 16) = acc[nt][tt];
      }
    }
    STAMP(10);
    asm volatile("s_waitcnt vmcnt(0) lgkmcnt(0)" ::: "memory");
    if (lds_inc(smem, M_RED2, lane) == 3u) {
      if (lane == 0) { set_flag(rW, OFF_FR + (rc * 4 + jl) * 128 + cg * 8, tlo, thi); misc[M_RED] = 4u; }
    }
    STAMP(11);
  }
  STAMP(25);
  STAMP(26);
  STAMP_OUT(0);
}

__global__ void code_end() {}

extern "C" void kernel_launch(void* const* d_in, const int* in_sizes, int n_in,
                              void* d_out, int out_size, void* d_ws, size_t ws_size,
                              hipStream_t stream) {
  Args a;
  a.x   = (const u16*)d_in[0];
  a.Lre = (const u16*)d_in[1];
  a.Lim = (const u16*)d_in[2];
  a.Bre = (const u16*)d_in[3];
  a.Bim = (const u16*)d_in[4];
  a.Cre = (const u16*)d_in[5];
  a.Cim = (const u16*)d_in[6];
  a.ldt = (const u16*)d_in[7];
  a.Dsk = (const u16*)d_in[8];
  a.w1  = (const u16*)d_in[10];
  a.b1  = (const u16*)d_in[11];
  a.w2  = (const u16*)d_in[12];
  a.gw  = (const u16*)d_in[13];
  a.ws  = (char*)d_ws;
  a.out = (float*)d_out;
  fused<<<256, 512, 0, stream>>>(a);
}
```

```cpp
#include <hip/hip_runtime.h>
#include <hip/hip_bf16.h>
#include <stdint.h>
#pragma clang diagnostic ignored "-Winline-asm"

typedef __attribute__((ext_vector_type(8))) __bf16 bf16x8;
typedef __attribute__((ext_vector_type(2))) __bf16 bf16x2;
typedef __attribute__((ext_vector_type(4))) float f32x4;
typedef __attribute__((ext_vector_type(2))) float f32x2;
typedef __attribute__((ext_vector_type(4))) unsigned u32x4;
typedef __attribute__((ext_vector_type(2))) unsigned u32x2;
typedef __attribute__((ext_vector_type(4))) short s16x4;
typedef unsigned short u16;
typedef unsigned long long u64;
#define LAS __attribute__((address_space(3)))
#define GAS __attribute__((address_space(1)))
#define DI __device__ __forceinline__

constexpr int D_ = 640, P_ = 640, F_ = 1536;

constexpr unsigned OFF_AF = 0;
constexpr unsigned OFF_PB = OFF_AF + 64u * 22 * 8192;
constexpr unsigned OFF_E  = OFF_PB + 64u * 16 * 10240;
constexpr unsigned OFF_FA = OFF_E + 256u * 640;
constexpr unsigned OFF_FH = OFF_FA + 256 * 128;
constexpr unsigned OFF_MK = OFF_FH + 256 * 128;
constexpr unsigned OFF_FR = OFF_MK + 256 * 128;
constexpr unsigned OFF_EPOCH = OFF_FR + 256 * 128;
constexpr unsigned WS_BYTES = OFF_EPOCH + 256;

DI float bflo(unsigned v) { return __uint_as_float(v << 16); }
DI float bfhi(unsigned v) { return __uint_as_float(v & 0xffff0000u); }
DI unsigned pack2(float a, float b) {
  bf16x2 r; r[0] = (__bf16)a; r[1] = (__bf16)b; return __builtin_bit_cast(unsigned, r);
}
DI f32x2 cm2(f32x2 a, f32x2 b) { return (f32x2){a[0] * b[0] - a[1] * b[1], a[0] * b[1] + a[1] * b[0]}; }
DI float sfma(float a, float b, float c) { float d; asm("v_fma_f32 %0, %1, %2, %3" : "=v"(d) : "v"(a), "v"(b), "v"(c)); return d; }
DI float smul(float a, float b) { float d; asm("v_mul_f32 %0, %1, %2" : "=v"(d) : "v"(a), "v"(b)); return d; }
template <int I> DI float qb(float v) {
  return __int_as_float(__builtin_amdgcn_mov_dpp(__float_as_int(v), I * 0x55, 0xf, 0xf, true));
}

DI f32x2 mish2(f32x2 h) {
  const f32x2 z = h * 1.44269504088896341f;
  f32x2 w; w[0] = __builtin_amdgcn_exp2f(z[0]); w[1] = __builtin_amdgcn_exp2f(z[1]);
  const f32x2 t = __builtin_elementwise_fma(w, (f32x2){-0.5f, -0.5f}, (f32x2){-1.f, -1.f});
  const f32x2 d = __builtin_elementwise_fma(w, t, (f32x2){-1.f, -1.f});
  f32x2 r; r[0] = __builtin_amdgcn_rcpf(d[0]); r[1] = __builtin_amdgcn_rcpf(d[1]);
  return __builtin_elementwise_fma(h, r, h);
}
DI f32x4 mish4(f32x4 a) {
  const f32x2 m0 = mish2((f32x2){a[0], a[1]}), m1 = mish2((f32x2){a[2], a[3]});
  return (f32x4){m0[0], m0[1], m1[0], m1[1]};
}
DI u32x4 pack8(f32x4 a0, f32x4 a1, float og) {
  a0 *= og; a1 *= og;
  return (u32x4){pack2(a0[0], a0[1]), pack2(a0[2], a0[3]), pack2(a1[0], a1[1]), pack2(a1[2], a1[3])};
}

#define SC1 16
#define SCHED __builtin_amdgcn_sched_barrier(0)
#define RSRC __amdgpu_buffer_rsrc_t
DI void st128(RSRC r, unsigned off, u32x4 v) { __builtin_amdgcn_raw_buffer_store_b128(v, r, off, 0, SC1); }
DI void set_flag(RSRC r, unsigned off, unsigned tlo, unsigned thi) { __builtin_amdgcn_raw_buffer_store_b64((u32x2){tlo, thi}, r, off, 0, SC1); }

DI void touch(const void* gsrc, unsigned lds_dst) {
  asm volatile("s_mov_b32 m0, %1\n\ts_nop 0\n\tglobal_load_lds_dword %0, off" :: "v"(gsrc), "s"(lds_dst) : "memory", "m0");
}
DI void gldss(unsigned voff, const void* sbase_, unsigned lds_dst) {
  asm volatile("s_mov_b32 m0, %2\n\ts_nop 0\n\tglobal_load_lds_dwordx4 %0, %1" :: "v"(voff), "s"(sbase_), "s"(lds_dst) : "memory", "m0");
}
DI void gld4c(unsigned voff, const void* sbase_, unsigned lds_dst) {
  asm volatile("s_mov_b32 m0, %2\n\ts_nop 0\n\tglobal_load_lds_dwordx4 %0, %1 sc1\n\tglobal_load_lds_dwordx4 %0, %1 offset:1024 sc1\n\t"
               "global_load_lds_dwordx4 %0, %1 offset:2048 sc1\n\tglobal_load_lds_dwordx4 %0, %1 offset:3072 sc1"
               :: "v"(voff), "s"(sbase_), "s"(lds_dst) : "memory", "m0");
}
DI void gld5(const unsigned* v, const void* sbase_, unsigned lds_dst) {
  asm volatile("s_mov_b32 m0, %5\n\ts_nop 0\n\tglobal_load_lds_dwordx4 %0, %4\n\tglobal_load_lds_dwordx4 %1, %4 offset:1024\n\t"
               "global_load_lds_dwordx4 %2, %4 offset:2048\n\tglobal_load_lds_dwordx4 %3, %4 offset:3072"
               :: "v"(v[0]), "v"(v[1]), "v"(v[2]), "v"(v[3]), "s"(sbase_), "s"(lds_dst) : "memory", "m0");
  gldss(v[4], sbase_, lds_dst + 4096);
}
DI void gld1c(unsigned voff, const void* sbase_, unsigned lds_dst) {
  asm volatile("s_mov_b32 m0, %2\n\ts_nop 0\n\tglobal_load_lds_dword %0, %1 sc1" :: "v"(voff), "s"(sbase_), "s"(lds_dst) : "memory", "m0");
}

constexpr int ST_X = 0, ST_BM = 16384, ST_W1 = 26624, ST_SZ = 38912;
constexpr int SP = 4 * ST_SZ;
constexpr int PARS = 48;
constexpr int SP_PAR = SP, SP_DF = SP + 1920, SP_B1 = SP + 4224, SP_EL = SP + 4608,
              SP_GW = SP + 4864, SP_G = SP + 6144, SP_MISC = SP + 6656, SP_Z = SP + 6912, LDS_BYTES = SP + 8192;
constexpr int BUS = 560, L_BU = SP - 80 * BUS;
constexpr int L_SCR = 65536;
DI constexpr int atile(int n) { return 40960 + 8192 * (n & 7) + ((n & 4) ? 45056 : 0); }
DI constexpr int btile(int n) {
  return n >= 12 ? (n < 14 ? 20480 : n < 16 ? 98304 : n < 18 ? 77824 : n < 20 ? 0 : 20480)
       : (n & 7) < 2 ? 77824 + 10240 * (n & 7) : (n & 7) < 6 ? 10240 * ((n & 7) - 2) : 98304 + 10240 * (n - 6);
}
enum { M_GRDY = 0, M_P1D, M_ACT, M_SRD, M_RED, M_RED2, M_OFF, M_OFF2, M_OWN, M_BUW, M_R2, M_R3, M_TAG = 16 };
constexpr int K0 = 10;
constexpr int DA = 5, DB = 6;
constexpr int SPIN_LDS = 1 << 22, SPIN_G = 1 << 19;

struct Args {
  const u16 *x, *Lre, *Lim, *Bre, *Bim, *Cre, *Cim, *ldt, *Dsk, *w1, *b1, *w2, *gw;
  char* ws;
  float* out;
};

#define CBAR do { SCHED; asm volatile("s_waitcnt lgkmcnt(0)\n\ts_barrier" ::: "memory"); SCHED; } while (0)
#define WBAR(n) asm volatile("s_waitcnt vmcnt(%0)\n\ts_barrier" :: "n"(n) : "memory")
#define WAITV(n) asm volatile("s_waitcnt vmcnt(%0)" :: "n"(n) : "memory")
#define WAITL asm volatile("s_waitcnt lgkmcnt(0)" ::: "memory")

DI void lds_wait_ge(volatile LAS unsigned* w, unsigned v) {
  for (int sp = 0; !__all(*w >= v) && sp < SPIN_LDS; ++sp) __builtin_amdgcn_s_sleep(1);
  asm volatile("" ::: "memory");
}
DI void lds_add(char* smem, int word, int lane) {
  asm volatile("; wave converged" ::: "memory");
  if (lane == 0) __hip_atomic_fetch_add((LAS unsigned*)(smem + SP_MISC) + word, 1u, __ATOMIC_RELAXED, __HIP_MEMORY_SCOPE_WORKGROUP);
  asm volatile("; wave converged" ::: "memory");
}
DI void poll_flags(RSRC rW, unsigned off, unsigned tlo, unsigned thi, int lane) {
  const unsigned o = off + (lane & 3) * 8;
#pragma unroll 1
  for (int sp = 0; sp < SPIN_G; ++sp) {
    const u32x2 f = __builtin_amdgcn_raw_buffer_load_b64(rW, o, 0, SC1);
    if (__all(f[0] == tlo && f[1] == thi)) break;
    __builtin_amdgcn_s_sleep(1);
    asm volatile("" ::: "memory");
  }
  asm volatile("" ::: "memory");
}

typedef __attribute__((ext_vector_type(8))) unsigned u32x8;
DI u32x8 sld8(const void* p) {
  u32x8 v; asm volatile("s_load_dwordx8 %0, %1, 0x0 glc\n\ts_waitcnt lgkmcnt(0)" : "=&s"(v) : "s"(p) : "memory"); return v;
}
#define SLD8X4(f0, f1, f2, f3, p, MID, n)                                                                            \
  asm volatile("s_load_dwordx8 %0, %4, 0x0 glc\n\ts_load_dwordx8 %1, %4, 0x80 glc\n\ts_load_dwordx8 %2, %4, 0x100 glc\n\t" \
               "s_load_dwordx8 %3, %4, 0x180 glc\n\t" MID "s_waitcnt lgkmcnt(0)"                                      \
               : "=&s"(f0), "=&s"(f1), "=&s"(f2), "=&s"(f3) : "s"(p), "n"(n) : "memory")
template <int N> DI bool tags(const u32x8& v, unsigned tlo, unsigned thi) {
  bool ok = true;
#pragma unroll
  for (int i = 0; i < N; ++i) ok = ok && v[2 * i] == tlo && v[2 * i + 1] == thi;
  return ok;
}
DI void wait4(RSRC rW, const char* ws, unsigned off, unsigned tlo, unsigned thi, int lane) {
  bool ok = false;
#pragma unroll 1
  for (int sp = 0; sp < 64 && !ok; ++sp) ok = tags<4>(sld8(ws + off), tlo, thi);
  if (!ok) poll_flags(rW, off, tlo, thi, lane);
}

DI void scan_task(RSRC rW, char* smem, volatile LAS unsigned* misc, int u, int lane, int rc, int cg, int jl, unsigned tlo, unsigned thi) {
  const int j = 4 * cg + jl, seg = lane & 3;
  const bool act = 16 * u + (lane >> 2) < 40;
  const int chl = act ? 16 * u + (lane >> 2) : 39;
  const unsigned OOB = 0x7fffffffu;
  const float* pp = (const float*)(smem + SP_PAR + chl * PARS);
  const f32x4 la = *(const f32x4*)pp;
  const f32x4 cl = *(const f32x4*)(pp + 4);
  f32x2 pw = seg == 1 ? (f32x2){cl[2], cl[3]} : *(const f32x2*)(pp + 8 + (seg == 3 ? 2 : 0));
  if (seg == 0) pw = (f32x2){1.f, 0.f};
  const float lr = la[0], li = la[1], nli = -la[1];
  const f32x2 l256 = cm2((f32x2){la[2], la[3]}, (f32x2){la[2], la[3]});
  f32x2 l512 = cm2(l256, l256);
  const int r0 = rc - 16 + 4 * seg;
  unsigned eo[4];
#pragma unroll
  for (int i = 0; i < 4; ++i) eo[i] = r0 + i >= 0 ? OFF_E + ((j * 16 + r0 + i) * 40 + chl) * 16 : OOB;
  const int c = 40 * jl + chl, k = c & 31;
  unsigned ho = act ? OFF_AF + ((rc * 4 + cg) * 22 + 12 + (c >> 5)) * 8192 + k * 64 + ((seg ^ ((k >> 2) & 2)) * 16) : OOB - 65536;
  asm volatile("" : "+v"(l512), "+v"(ho), "+v"(pw), "+v"(eo[0]), "+v"(eo[1]), "+v"(eo[2]), "+v"(eo[3]));
  lds_wait_ge(misc + M_BUW, 2u);
  __builtin_amdgcn_s_setprio(3);
  f32x4 ar[8], ai[8];
  {
    const char* bp = smem + L_BU + 2 * chl * BUS + seg * 144;
#pragma unroll
    for (int i = 0; i < 8; ++i) { ar[i] = *(const f32x4*)(bp + i * 16); ai[i] = *(const f32x4*)(bp + BUS + i * 16); }
  }
  WAITV(0);
  float sr = 0.f, si = 0.f;
#pragma unroll
  for (int t = 0; t < 32; ++t) {
    const float tr = fmaf(nli, si, ar[t >> 2][t & 3]), ti = fmaf(li, sr, ai[t >> 2][t & 3]);
    sr = fmaf(lr, sr, tr); si = fmaf(lr, si, ti);
  }
  float qr = qb<0>(sr), qi = qb<0>(si);
  qr = seg > 0 ? qr : 0.f; qi = seg > 0 ? qi : 0.f;
  {
    float nr = fmaf(cl[2], qr, fmaf(-cl[3], qi, qb<1>(sr))), ni = fmaf(cl[2], qi, fmaf(cl[3], qr, qb<1>(si)));
    qr = seg > 1 ? nr : qr; qi = seg > 1 ? ni : qi;
    nr = fmaf(cl[2], qr, fmaf(-cl[3], qi, qb<2>(sr))); ni = fmaf(cl[2], qi, fmaf(cl[3], qr, qb<2>(si)));
    qr = seg > 2 ? nr : qr; qi = seg > 2 ? ni : qi;
    const float er = fmaf(cl[2], qr, fmaf(-cl[3], qi, sr)), ei = fmaf(cl[2], qi, fmaf(cl[3], qr, si));
    st128(rW, act && seg == 3 ? OFF_E + ((j * 16 + rc) * 40 + chl) * 16 : OOB, (u32x4){__float_as_uint(er), __float_as_uint(ei), tlo, thi});
  }
  __builtin_amdgcn_s_setprio(0);
  lds_add(smem, M_SRD, lane);
#pragma unroll
  for (int i = 0; i < 8; ++i) {
#pragma unroll
    for (int r = 0; r < 4; ++r) {
      const float xr = fmaf(cl[0], ar[i][r], -cl[1] * ai[i][r]), xi = fmaf(cl[0], ai[i][r], cl[1] * ar[i][r]);
      ar[i][r] = xr; ai[i][r] = xi;
    }
    asm volatile("" : "+v"(ar[i]), "+v"(ai[i]));
  }
  lds_wait_ge(misc + M_GRDY, 2u);
  f32x4 gv[8];
#pragma unroll
  for (int i = 0; i < 8; ++i) gv[i] = *(const f32x4*)(smem + SP_G + seg * 128 + i * 16);
  {
    u32x4 ev[4];
#pragma unroll 1
    for (int sp = 0; sp < SPIN_G; ++sp) {
      unsigned bad = 0u;
#pragma unroll
      for (int i = 0; i < 4; ++i) ev[i] = __builtin_amdgcn_raw_buffer_load_b128(rW, eo[i], 0, SC1);
#pragma unroll
      for (int i = 0; i < 4; ++i) bad |= ((ev[i][2] ^ tlo) | (ev[i][3] ^ thi)) & (eo[i] == OOB ? 0u : ~0u);
      if (__all(bad == 0u)) break;
      __builtin_amdgcn_s_sleep(1);
      asm volatile("" ::: "memory");
    }
    __builtin_amdgcn_s_setprio(3);
    float cr = __uint_as_float(ev[0][0]), ci = __uint_as_float(ev[0][1]);
#pragma unroll
    for (int i = 1; i < 4; ++i) {
      const float tr = sfma(-la[3], ci, __uint_as_float(ev[i][0])), ti = sfma(la[3], cr, __uint_as_float(ev[i][1]));
      cr = sfma(la[2], cr, tr); ci = sfma(la[2], ci, ti);
    }
    float dr = qb<0>(cr), di = qb<0>(ci), tr, ti;
    tr = sfma(-l512[1], di, qb<1>(cr)); ti = sfma(l512[1], dr, qb<1>(ci)); dr = sfma(l512[0], dr, tr); di = sfma(l512[0], di, ti);
    tr = sfma(-l512[1], di, qb<2>(cr)); ti = sfma(l512[1], dr, qb<2>(ci)); dr = sfma(l512[0], dr, tr); di = sfma(l512[0], di, ti);
    tr = sfma(-l512[1], di, qb<3>(cr)); ti = sfma(l512[1], dr, qb<3>(ci)); dr = sfma(l512[0], dr, tr); di = sfma(l512[0], di, ti);
    tr = sfma(-pw[1], di, qr); ti = sfma(pw[1], dr, qi);
    cr = sfma(pw[0], dr, tr); ci = sfma(pw[0], di, ti);
    sr = sfma(cl[0], cr, smul(-cl[1], ci)); si = sfma(cl[0], ci, smul(cl[1], cr));
  }
#pragma unroll
  for (int i = 0; i < 4; ++i) {
    u32x4 vr, vi;
#pragma unroll
    for (int t2 = 0; t2 < 4; ++t2) {
      float hr[2], hi[2];
#pragma unroll
      for (int e = 0; e < 2; ++e) {
        const int t = 8 * i + 2 * t2 + e;
        const float tr = sfma(nli, si, ar[t >> 2][t & 3]), ti = sfma(li, sr, ai[t >> 2][t & 3]);
        sr = sfma(lr, sr, tr); si = sfma(lr, si, ti);
        hr[e] = smul(sr, gv[t >> 2][t & 3]); hi[e] = smul(-si, gv[t >> 2][t & 3]);
      }
      vr[t2] = pack2(hr[0], hr[1]); vi[t2] = pack2(hi[0], hi[1]);
    }
    __builtin_amdgcn_raw_buffer_store_b128(vr, rW, ho ^ ((i & 1) * 16), i * 2048, SC1);
    __builtin_amdgcn_raw_buffer_store_b128(vi, rW, ho ^ ((i & 1) * 16), i * 2048 + 5 * 8192, SC1);
  }
  WAITV(0);
  if (lane == 0) set_flag(rW, OFF_FH + ((rc * 4 + cg) * 4 + jl) * 128 + u * 8, tlo, thi);
  __builtin_amdgcn_s_setprio(0);
}

__global__ __launch_bounds__(512, 2) void fused(Args a) {
  __shared__ __attribute__((aligned(16))) char smem[LDS_BYTES];
  const int tid = threadIdx.x, lane = tid & 63;
  const int wave = __builtin_amdgcn_readfirstlane(tid >> 6);
  const int b = blockIdx.x, grp = b & 7, idx = b >> 3;
  const int cg = grp & 3, jl = idx & 3;
  const int rc = 8 * (grp >> 2) + (idx >> 2), j = 4 * cg + jl;
  const int r16 = lane & 15, g = lane >> 4;
  char* ws = a.ws;
  const RSRC rW = __builtin_amdgcn_make_buffer_rsrc((void*)ws, 0, WS_BYTES, 0x00020000);
  volatile LAS unsigned* misc = (volatile LAS unsigned*)(smem + SP_MISC);
  const unsigned sbase = (unsigned)(size_t)(LAS char*)smem;
  const char* wbase2 = (const char*)(a.w2 + (size_t)384 * cg * D_ + 160 * jl);
  auto kmlp = [&](const int n) { return 3 * (jl ^ (n / 3)) + n % 3; };
  auto woffs = [&](unsigned* o, int h) {
#pragma unroll
    for (int i5 = 0; i5 < 5; ++i5) {
      const unsigned i = 5 * h + i5;
      const unsigned v = 4 * i + lane, kq = (v * 205u) >> 12, k = 3 * i + kq, cc = v - 20 * kq - ((k >> 1) & 2);
      o[i5] = k * (D_ * 2) + 16 * min(cc, cc + 20u) - 1024 * (i5 & 3);
    }
  };
  auto issueW = [&](const int n, const unsigned* o, int h) {
    gld5(o, wbase2 + (size_t)kmlp(n) * (32 * D_ * 2), sbase + btile(n) + h * 5120);
  };

  if (wave >= 4) {
    const int lw = wave - 4;
    const int rw = lane >> 3, c8 = lane & 7;
    unsigned ow[5];
    {
      const unsigned vx = rw * (D_ * 2) + 16 * (c8 ^ (rw >> 1) ^ (4 * (lw & 1)));
      const unsigned vb = rw * (D_ * 2) + 16 * (c8 ^ ((rw >> 1) << 1));
      const char* px = (const char*)(a.x + (size_t)(128 * rc + 8 * lw) * D_);
      const char* pre = (const char*)(a.Bre + (size_t)(40 * j + 8 * lw) * D_);
      const char* pim = (const char*)(a.Bim + (size_t)(40 * j + 8 * lw) * D_);
      const char* pex = (const char*)((lw & 1 ? a.Bim : a.Bre) + (size_t)(40 * j + 32) * D_);
      const char* pw = (const char*)(a.w1 + (size_t)16 * lw * F_ + 96 * j);
      const unsigned dl = sbase + lw * 1024, vex = vb ^ ((lw & 1) * 16);
      unsigned vw[3];
      auto stage1 = [&](unsigned boff) {
        if (lw < 2) gldss(vex, pex, sbase + ST_BM + (4 + 5 * lw) * 1024 + boff);
#pragma unroll
        for (int i = 0; i < 4; ++i) gldss(vx + i * (32 * D_ * 2), px, dl + ST_X + i * 4096 + boff);
        gldss(vb, pre, dl + ST_BM + boff);
        gldss(vb ^ 16, pim, dl + ST_BM + 5120 + boff);
        px += 128; pre += 128; pim += 128; pex += 128;
      };
      auto stage2 = [&](unsigned boff) {
#pragma unroll
        for (int i = 0; i < 3; ++i) gldss(vw[i], pw, sbase + ST_W1 + (3 * lw + i) * 1024 + boff);
        pw += 64 * F_ * 2;
      };
      auto stage = [&](unsigned boff) { stage1(boff); stage2(boff); };
      stage1(0);
#pragma unroll
      for (int i = 0; i < 3; ++i) {
        const unsigned v = 64 * i + lane, k = (v * 171u) >> 11, cc = v - 12 * k - ((k >> 2) & 2);
        vw[i] = k * (F_ * 2) + 16 * min(cc, cc + 12u);
      }
      stage2(0); stage(ST_SZ);
      WBAR(9);
      stage(2 * ST_SZ);
      woffs(ow, lw & 1);
      WBAR(9);
      stage(3 * ST_SZ);
      WBAR(9);
      stage(0); stage1(ST_SZ);
#pragma unroll 1
      for (int s = 3; s <= 6; ++s) {
        WBAR(15);
        stage2(((s + 2) & 3) * ST_SZ); stage1(((s + 3) & 3) * ST_SZ);
      }
      WBAR(15);
      stage2(ST_SZ);
      if (lw < 2) issueW(0, ow, lw); else issueW(1, ow, lw - 2);
      WBAR(14);
      if (lw < 2) issueW(6, ow, lw);
      WBAR(5);
      if (lw < 2) issueW(2, ow, lw); else { issueW(3, ow, lw - 2); issueW(4, ow, lw - 2); }
    }
    const unsigned tlo = __builtin_amdgcn_readfirstlane(misc[M_TAG]), thi = __builtin_amdgcn_readfirstlane(misc[M_TAG + 1]);
    const char* abase = ws + OFF_AF + (size_t)(rc * 4 + cg) * 22 * 8192;
    const unsigned aoff = lane * 16;
    const int co = cg & 1;
    auto kstep = [&](int n) { return n < 12 ? kmlp(n) : n < 16 ? n + co : n < 20 ? n + 1 + co : (n == 20 ? 16 : 21) - 4 * co; };
    auto issueA4 = [&](int n, int h) {
      gld4c(aoff, abase + (size_t)kstep(n) * 8192 + h * 4096, sbase + atile(n) + h * 4096);
    };
    if (lw != 1) {
      scan_task(rW, smem, misc, lw == 0 ? 2 : lw - 2, lane, rc, cg, jl, tlo, thi);
    } else {
      {
        const int l60 = lane < 60 ? lane : 0, r3 = (l60 * 43) >> 7;
        const unsigned wo = (idx * 120 + l60) * 128, co_ = (20 * idx + r3) * (P_ * 2) + (l60 - 3 * r3) * 128;
        unsigned d0, d1, d2, d3;
        asm volatile("global_load_dword %0, %4, %6\n\tglobal_load_dword %1, %4, %7\n\tglobal_load_dword %2, %5, %8\n\tglobal_load_dword %3, %5, %9\n\ts_waitcnt vmcnt(0)"
                     : "=&v"(d0), "=&v"(d1), "=&v"(d2), "=&v"(d3)
                     : "v"(co_), "v"(wo), "s"(a.Cre + 160 * cg - 32 * co), "s"(a.Cim + 160 * cg - 32 * co), "s"(a.w2 + (size_t)384 * cg * D_), "s"(a.w2 + (size_t)384 * cg * D_ + 60 * 64) : "memory");
      }
      unsigned o0[5];
      woffs(o0, 0);
      __builtin_amdgcn_raw_buffer_store_b32(tlo, rW, OFF_MK + b * 128 + (lane & 31) * 4, 0, 0);
      WAITV(0);
      gld1c(0u, ws + OFF_MK + b * 128, sbase + SP_EL);
      lds_wait_ge(misc + M_SRD, 3u); lds_wait_ge(misc + M_P1D, 4u);
      lds_add(smem, M_SRD, lane);
      issueW(5, o0, 0); issueW(5, ow, 1); issueW(7, o0, 0); issueW(7, ow, 1);
      lds_wait_ge(misc + M_ACT, 4u);
#pragma unroll
      for (int n = 3; n < 12; ++n) {
        if (n % 3 == 0) wait4(rW, ws, OFF_FA + ((rc * 4 + cg) * 4 + (jl ^ (n / 3))) * 128, tlo, thi, lane);
        if (n == 8) lds_wait_ge(misc + M_OWN, 4u);
        if (n == 11) lds_wait_ge(misc + M_R2, 4u);
        if (n >= 8) { issueW(n, o0, 0); issueW(n, ow, 1); }
        issueA4(n, 0); issueA4(n, 1);
        if (n <= K0 + 1) gld1c(0u, ws + OFF_MK + b * 128, sbase + SP_DF + (n - 3) * 256);
        WAITV(45);
      }
    }
    if (lw < 2) {
      if (lw == 0) {
        const char* fbase = ws + OFF_FH + (rc * 4 + cg) * 512;
        bool hok = false;
#pragma unroll 1
        for (int sp = 0; !hok && sp < 64; ++sp) {
          u32x8 f0, f1, f2, f3;
          SLD8X4(f0, f1, f2, f3, fbase, "", 0);
          hok = tags<3>(f0, tlo, thi) && tags<3>(f1, tlo, thi) && tags<3>(f2, tlo, thi) && tags<3>(f3, tlo, thi);
        }
        const unsigned hfo = ((lane >> 3) & 3) * 128 + ((lane & 7) < 6 ? lane & 7 : lane & 1) * 4;
#pragma unroll 1
        for (int sp = 0; !hok && sp < SPIN_G; ++sp) {
          hok = __all(__builtin_amdgcn_raw_buffer_load_b32(rW, OFF_FH + (rc * 4 + cg) * 512 + hfo, 0, SC1) == ((lane & 1) ? thi : tlo));
          asm volatile("" ::: "memory");
        }
        lds_wait_ge(misc + M_R2, 4u);
        issueA4(12, 0); issueA4(12, 1); issueA4(13, 0); issueA4(13, 1);
        lds_wait_ge(misc + M_R3, 4u);
        issueA4(14, 0); issueA4(14, 1);
      }
      WBAR(16); issueA4(15, lw);
      WBAR(12); issueA4(16, lw);
#pragma unroll 1
      for (int k = 12; k <= 21 - DA; ++k) { WBAR(4 * (DA - 3)); issueA4(k + DA, lw); }
#pragma unroll
      for (int kk = 22 - DA; kk <= 19; ++kk) WBAR(4 * (19 - kk));
    } else {
      const int h = lw - 2;
      unsigned oc[10];
#pragma unroll
      for (int p = 0; p < 10; ++p) oc[p] = (8 * p + rw) * (P_ * 2) + 16 * (c8 ^ (rw >> 1) ^ (4 * (p & 1))) - 1024 * ((p % 5) & 3);
      const char* cre = (const char*)(a.Cre + (size_t)(160 * jl + 80 * h) * P_ + 160 * cg);
      const char* cim = (const char*)(a.Cim + (size_t)(160 * jl + 80 * h) * P_ + 160 * cg);
      const char* sp[10];
#pragma unroll
      for (int p = 0; p < 10; ++p) {
        sp[p] = ((oc[p] & 64u) ? cim : cre) + (1 - co) * 256 + ((oc[p] & ~64u) + 1024 * ((p % 5) & 3));
        asm volatile("" : "+v"(sp[p]));
      }
      lds_wait_ge(misc + M_R2, 4u);
#pragma unroll
      for (int n = 12; n < 20; ++n) {
        if (n == 14) lds_wait_ge(misc + M_R3, 4u);
        if (n >= K0 + DB) WBAR(5 * (DB - 4));
        gld5(oc + 5 * (n & 1), (n < 16 ? cre : cim) + co * 64 + ((n >> 1) & 1) * 128, sbase + btile(n) + h * 10240 + (n & 1) * 5120);
      }
#pragma unroll
      for (int n = 20; n < 22; ++n) {
        WBAR(5 * (DB - 4));
#pragma unroll
        for (int i = 0; i < 5; ++i)
          asm volatile("s_mov_b32 m0, %1\n\ts_nop 0\n\tglobal_load_lds_dwordx4 %0, off"
                       :: "v"(sp[5 * (n & 1) + i]), "s"(sbase + btile(n) + h * 10240 + (5 * (n & 1) + i) * 1024) : "memory", "m0");
      }
#pragma unroll
      for (int kk = 22 - DB; kk <= 19; ++kk) WBAR(kk < 18 ? 10 : 0);
    }
    const int n0 = 160 * jl + 16 * lw + 4 * g;
    const size_t orow = (size_t)(128 * rc + 32 * cg + r16) * D_ + n0;
    f32x4 sk[3][2];
#pragma unroll
    for (int i = 0; i < 3; ++i) {
      const int c = (i < 2 || lw < 2) ? 64 * i : 0;
      const u32x2 dv = *(const u32x2*)(a.Dsk + n0 + c);
#pragma unroll
      for (int t = 0; t < 2; ++t) {
        const float gt = 0.5f * *(const float*)(smem + SP_G + (32 * cg + 16 * t + r16) * 4);
        const u32x2 xv = *(const u32x2*)(a.x + orow + 16 * t * D_ + c);
        sk[i][t] = (f32x4){gt * bflo(xv[0]) * bflo(dv[0]), gt * bfhi(xv[0]) * bfhi(dv[0]), gt * bflo(xv[1]) * bflo(dv[1]), gt * bfhi(xv[1]) * bfhi(dv[1])};
      }
    }
    {
      const unsigned fr = OFF_FR + ((rc * 4 + jl) * 2 + (cg >> 1)) * 256;
      bool ok = false;
#pragma unroll 1
      for (int sp = 0; sp < 64 && !ok; ++sp) {
        u32x8 f0, f1;
        asm volatile("s_load_dwordx8 %0, %2, 0x0 glc\n\ts_load_dwordx8 %1, %2, 0x80 glc\n\ts_waitcnt lgkmcnt(0)" : "=&s"(f0), "=&s"(f1) : "s"(ws + fr) : "memory");
        ok = tags<4>(f0, tlo, thi) && tags<4>(f1, tlo, thi);
        if (!ok) __builtin_amdgcn_s_sleep(1);
      }
      if (!ok) poll_flags(rW, fr + (lane & 4) * 32, tlo, thi, lane);
    }
    if (rc == 15 && j == 0 && tid == 256) set_flag(rW, OFF_EPOCH, tlo, thi);
    u32x4 pv[3][3];
#pragma unroll
    for (int i = 0; i < 3; ++i)
#pragma unroll
      for (int s = 0; s < 3; ++s)
        pv[i][s] = __builtin_amdgcn_raw_buffer_load_b128(rW, (i < 2 || lw < 2) ? OFF_PB + (((rc * 4 + jl) * 4 + cg) * 4 + ((cg + 1 + s) & 3)) * 10240 + (lw + 4 * i) * 1024 + lane * 16 : 0x7fffffffu, 0, SC1);
#pragma unroll
    for (int i = 0; i < 3; ++i) {
      if (i < 2 || lw < 2) {
#pragma unroll
        for (int t = 0; t < 2; ++t) {
          f32x4 v = *(const f32x4*)(smem + atile(0) + ((lw + 4 * i) * 2 + t) * 1024 + lane * 16) + sk[i][t];
#pragma unroll
          for (int s = 0; s < 3; ++s) v += (f32x4){bflo(pv[i][s][2 * t]), bfhi(pv[i][s][2 * t]), bflo(pv[i][s][2 * t + 1]), bfhi(pv[i][s][2 * t + 1])};
          *(f32x4*)(a.out + orow + 16 * t * D_ + 64 * i) = v;
        }
      }
    }
  } else {
    if (wave == 3) {
      unsigned long long pc; unsigned back, csz;
      asm volatile("s_getpc_b64 %0\n1:\n\ts_mov_b32 %1, 1b-_Z5fused4Args\n\ts_mov_b32 %2, _Z8code_endv-_Z5fused4Args" : "=s"(pc), "=s"(back), "=s"(csz));
      const unsigned co = (idx * 14 + (lane < 14 ? lane : 0)) * 128;
      touch((const char*)(pc - back) + (co + 4 <= csz && csz < (1u << 20) ? co : 0u), sbase + SP_EL);
      touch((const char*)a.w1 + (size_t)(3072 + b * 48 + (lane < 48 ? lane : 0)) * 128, sbase + SP_EL);
      { const int r = (b & 127) * 5 + ((lane >> 3) < 5 ? (lane >> 3) : 0);
        touch((const char*)(b < 128 ? a.Bre : a.Bim) + (size_t)r * 1280 + (2 + (lane & 7)) * 128, sbase + SP_EL); }
    } else if (wave == 0) {
      touch((const char*)a.x + (size_t)(b * 8 + (lane >> 3)) * 1280 + (2 + (lane & 7)) * 128, sbase + SP_EL);
    }
    if (wave == 1) {
      const u64 tg = __hip_atomic_load((GAS u64*)(ws + OFF_EPOCH), __ATOMIC_RELAXED, __HIP_MEMORY_SCOPE_AGENT) + 1;
      const unsigned v = *(const unsigned*)(a.b1 + 96 * j + 2 * (lane < 48 ? lane : 0));
      const u32x4 g0 = *(const u32x4*)(a.gw + 8 * lane), g1 = *(const u32x4*)(a.gw + 8 * (64 + r16));
      if (lane < 48) *(f32x2*)(smem + SP_B1 + lane * 8) = (f32x2){bflo(v), bfhi(v)};
      *(u32x4*)(smem + SP_GW + lane * 16) = g0;
      if (lane < 16) *(u32x4*)(smem + SP_GW + 1024 + lane * 16) = g1;
      misc[lane] = lane == M_TAG ? (unsigned)tg : lane == M_TAG + 1 ? (unsigned)(tg >> 32) : 0u;
      const unsigned nt = ~(unsigned)tg;
      *(u32x4*)(smem + SP_DF + lane * 16) = (u32x4){nt, nt, nt, nt};
      *(u32x4*)(smem + SP_DF + 1024 + lane * 16) = (u32x4){nt, nt, nt, nt};
      *(unsigned*)(smem + SP_DF + 2048 + lane * 4) = nt;
      *(u32x4*)(smem + SP_Z + lane * 16) = (u32x4){0u, 0u, 0u, 0u};
      if (lane < 16) *(u32x4*)(smem + SP_Z + 1024 + lane * 16) = (u32x4){0u, 0u, 0u, 0u};
    } else if (wave == 2) {
      const int ch = 40 * j + (lane < 40 ? lane : 0);
      const float lre = bflo(a.Lre[ch]), lim = bflo(a.Lim[ch]), dt = expf(bflo(a.ldt[ch]));
      float e = expf(dt * lre), sn, cs;
      sincosf(dt * lim, &sn, &cs);
      f32x2 l1 = {e * cs, e * sn};
      const float nr = l1[0] - 1.f, ni = l1[1], den = lre * lre + lim * lim;
      f32x2 cf = {(nr * lre + ni * lim) / den, (ni * lre - nr * lim) / den};
      f32x2 l2 = cm2(l1, l1), l4 = cm2(l2, l2), l8 = cm2(l4, l4), l16 = cm2(l8, l8);
      f32x2 l32 = cm2(l16, l16), l64 = cm2(l32, l32), l128 = cm2(l64, l64), l96 = cm2(l64, l32);
      if (lane < 40) {
        float* pp = (float*)(smem + SP_PAR + lane * PARS);
        *(f32x4*)(pp) = (f32x4){l1[0], l1[1], l128[0], l128[1]};
        *(f32x4*)(pp + 4) = (f32x4){cf[0], cf[1], l32[0], l32[1]};
        *(f32x4*)(pp + 8) = (f32x4){l64[0], l64[1], l96[0], l96[1]};
      }
    }
    const int th = wave & 1;
    const unsigned xo0 = ST_X + (64 * th + r16) * 128 + ((g ^ (r16 >> 1)) * 16);
    const unsigned xo1 = xo0 ^ 64;
    const int q4 = r16 >> 2, p4 = r16 & 3;

#define P1_PIPE(LOADF, STEP, MMA, INIT)                                             \
    CBAR;                                                                           \
    __builtin_amdgcn_s_setprio(3);                                                  \
    INIT;                                                                           \
    LOADF(F0, 0u, 0);                                                               \
    STEP(F0, F1, 0u, 1);                                                            \
    _Pragma("unroll 1") for (int s = 1; s < 10; ++s) {                              \
      CBAR;                                                                         \
      STEP(F1, F0, (s & 3) * ST_SZ, 0);                                             \
      STEP(F0, F1, (s & 3) * ST_SZ, 1);                                             \
    }                                                                               \
    WAITL;                                                                          \
    lds_add(smem, M_P1D, lane);                                                     \
    MMA(F1); SCHED; __builtin_amdgcn_s_setprio(0);                                  \

    auto put_act = [&](int fp, int tile, u32x4 v) {
      st128(rW, OFF_AF + (((rc * 4 + cg) * 22 + 3 * jl + fp) * 8 + tile) * 1024 + lane * 16, v);
      *(u32x4*)(smem + atile(fp) + tile * 1024 + lane * 16) = v;
    };
    unsigned tlo, thi;
    if (wave < 2) {
      f32x4 acc[6][4];
      const unsigned wbase = ST_W1 + (8 * g + q4) * 192 + (p4 >> 1) * 16 + (p4 & 1) * 8;
      const unsigned wa = wbase + (g & 1) * 32;
      const unsigned wb = wbase + ((g & 1) ? 0 : 160);
      struct Frag { bf16x8 x[4], w[6]; };
      Frag F0, F1;
      auto rdw = [&](const char* sb, int ft, int kk) {
        unsigned ad = (ft < 5 ? wa + 32 * ft : wb) + kk * 32 * 192;
        s16x4 lo = __builtin_amdgcn_ds_read_tr16_b64_v4i16((LAS s16x4*)(sb + ad));
        s16x4 hi = __builtin_amdgcn_ds_read_tr16_b64_v4i16((LAS s16x4*)(sb + ad + 4 * 192));
        return __builtin_bit_cast(bf16x8, __builtin_shufflevector(lo, hi, 0, 1, 2, 3, 4, 5, 6, 7));
      };
      auto loadf = [&](Frag& F, unsigned boff, int kk) {
        const char* sb = smem + boff;
#pragma unroll
        for (int tt = 0; tt < 4; ++tt) F.x[tt] = *(const bf16x8*)(sb + (kk ? xo1 : xo0) + tt * 2048);
#pragma unroll
        for (int ft = 0; ft < 6; ++ft) F.w[ft] = rdw(sb, ft, kk);
      };
      auto mma = [&](Frag& F) {
#pragma unroll
        for (int m = 0; m < 24; ++m)
          acc[m >> 2][m & 3] = __builtin_amdgcn_mfma_f32_16x16x32_bf16(F.w[m >> 2], F.x[m & 3], acc[m >> 2][m & 3], 0, 0, 0);
      };
      auto step = [&](Frag& C, Frag& N, unsigned boff, const int kk) {
        const char* sb = smem + boff;
#pragma unroll
        for (int m = 0; m < 24; ++m) {
          SCHED;
          acc[m >> 2][m & 3] = __builtin_amdgcn_mfma_f32_16x16x32_bf16(C.w[m >> 2], C.x[m & 3], acc[m >> 2][m & 3], 0, 0, 0);
          SCHED;
          if (m < 4) N.x[m] = *(const bf16x8*)(sb + (kk ? xo1 : xo0) + m * 2048);
          if (m >= 4 && m < 10) N.w[m - 4] = rdw(sb, m - 4, kk);
        }
        SCHED;
      };
      auto init = [&]() {
#pragma unroll
        for (int ft = 0; ft < 6; ++ft) {
          f32x4 bv = *(const f32x4*)(smem + SP_B1 + (16 * ft + 4 * g) * 4);
#pragma unroll
          for (int tt = 0; tt < 4; ++tt) acc[ft][tt] = bv;
        }
      };
      P1_PIPE(loadf, step, mma, init());
      tlo = misc[M_TAG]; thi = misc[M_TAG + 1];
#pragma unroll
      for (int i = 0; i < 3; ++i) { acc[i][0] = mish4(acc[i][0]); asm volatile("" : "+v"(acc[i][0])); }
      lds_wait_ge(misc + M_P1D, 4u);
#pragma unroll
      for (int i = 0; i < 6; ++i) *(f32x4*)(smem + L_SCR + th * 6144 + i * 1024 + lane * 16) = acc[i][3];
#pragma unroll
      for (int i = 3; i < 6; ++i) { acc[i][0] = mish4(acc[i][0]); asm volatile("" : "+v"(acc[i][0])); }
      WAITL;
      misc[M_OFF + th] = 1u;
#pragma unroll
      for (int i = 0; i < 6; ++i) { acc[i][1] = mish4(acc[i][1]); asm volatile("" : "+v"(acc[i][1])); }
      SCHED;
      lds_wait_ge(misc + M_GRDY, 2u);
#pragma unroll
      for (int tt = 0; tt < 3; ++tt) {
        const float og = 1.f - 0.5f * *(const float*)(smem + SP_G + (64 * th + 16 * tt + r16) * 4);
#pragma unroll
        for (int fp = 0; fp < 3; ++fp) {
          if (tt == 2) { acc[2 * fp][2] = mish4(acc[2 * fp][2]); acc[2 * fp + 1][2] = mish4(acc[2 * fp + 1][2]); }
          put_act(fp, 4 * th + tt, pack8(acc[2 * fp][tt], acc[2 * fp + 1][tt], og));
        }
      }
    } else {
      f32x4 acc[4][5], gac[4];
      const unsigned bo0 = ST_BM + (40 * (r16 & 1) + (r16 >> 1)) * 128 + ((g ^ ((((r16 >> 2) & 3) << 1) | (r16 & 1))) * 16);
      const unsigned bo1 = bo0 ^ 64;
      struct Frag { bf16x8 x[4], w[5], gf; };
      Frag F0, F1;
      int kc = 0;
      auto loadf = [&](Frag& F, unsigned boff, int kk) {
        const char* sb = smem + boff;
#pragma unroll
        for (int tt = 0; tt < 4; ++tt) F.x[tt] = *(const bf16x8*)(sb + (kk ? xo1 : xo0) + tt * 2048);
#pragma unroll
        for (int nt = 0; nt < 5; ++nt) F.w[nt] = *(const bf16x8*)(sb + (kk ? bo1 : bo0) + nt * 1024);
        F.gf = *(const bf16x8*)(smem + (r16 < 4 ? SP_GW : SP_Z) + kc * 64 + g * 16);
        ++kc;
      };
      char* bw = smem + L_BU + r16 * BUS + th * 288 + g * 16;
      auto dump = [&](const int m) {
        *(f32x4*)(bw + (m >> 2) * 16 * BUS + ((m >> 1) & 1) * 144 + (m & 1) * 64) = acc[m & 3][m >> 2];
      };
      auto mma = [&](Frag& F) {
#pragma unroll
        for (int tt = 0; tt < 4; ++tt) gac[tt] = __builtin_amdgcn_mfma_f32_16x16x32_bf16(F.x[tt], F.gf, gac[tt], 0, 0, 0);
#pragma unroll
        for (int m = 0; m < 20; ++m) {
          SCHED;
          acc[m & 3][m >> 2] = __builtin_amdgcn_mfma_f32_16x16x32_bf16(F.x[m & 3], F.w[m >> 2], acc[m & 3][m >> 2], 0, 0, 0);
          SCHED;
          if (m >= 3) dump(m - 3);
        }
        dump(17); dump(18); dump(19);
      };
      auto step = [&](Frag& C, Frag& N, unsigned boff, const int kk) {
        const char* sb = smem + boff;
#pragma unroll
        for (int m = 0; m < 24; ++m) {
          SCHED;
          if (m < 20) acc[m & 3][m >> 2] = __builtin_amdgcn_mfma_f32_16x16x32_bf16(C.x[m & 3], C.w[m >> 2], acc[m & 3][m >> 2], 0, 0, 0);
          else gac[m & 3] = __builtin_amdgcn_mfma_f32_16x16x32_bf16(C.x[m & 3], C.gf, gac[m & 3], 0, 0, 0);
          SCHED;
          if (m < 4) N.x[m] = *(const bf16x8*)(sb + (kk ? xo1 : xo0) + m * 2048);
          if (m >= 4 && m < 9) N.w[m - 4] = *(const bf16x8*)(sb + (kk ? bo1 : bo0) + (m - 4) * 1024);
          if (m == 9) N.gf = *(const bf16x8*)(smem + (r16 < 4 ? SP_GW : SP_Z) + kc * 64 + g * 16);
        }
        ++kc;
        SCHED;
      };
      auto init = [&]() {
#pragma unroll
        for (int tt = 0; tt < 4; ++tt) {
          gac[tt] = (f32x4){0.f, 0.f, 0.f, 0.f};
#pragma unroll
          for (int nt = 0; nt < 5; ++nt) acc[tt][nt] = (f32x4){0.f, 0.f, 0.f, 0.f};
        }
      };
      P1_PIPE(loadf, step, mma, init());
      tlo = misc[M_TAG]; thi = misc[M_TAG + 1];
      {
        WAITL;
        lds_add(smem, M_BUW, lane);
        f32x4 lg = r16 == 0 ? gac[0] : r16 == 1 ? gac[1] : r16 == 2 ? gac[2] : gac[3];
        f32x4 gg;
#pragma unroll
        for (int r = 0; r < 4; ++r) gg[r] = 2.f * __builtin_amdgcn_rcpf(1.f + __expf(-lg[r]));
        if (r16 < 4) *(f32x4*)(smem + SP_G + (64 * th + 16 * r16 + 4 * g) * 4) = gg;
        WAITL;
        lds_add(smem, M_GRDY, lane);
      }
      lds_wait_ge(misc + M_OFF + th, 1u);
      const float og = 1.f - 0.5f * *(const float*)(smem + SP_G + (64 * th + 48 + r16) * 4);
#pragma unroll
      for (int fp = 0; fp < 3; ++fp) {
        const char* sp = smem + L_SCR + th * 6144 + fp * 2048 + lane * 16;
        put_act(fp, 4 * th + 3, pack8(mish4(*(const f32x4*)sp), mish4(*(const f32x4*)(sp + 1024)), og));
      }
    }
    WAITL;
    lds_add(smem, M_ACT, lane);
    auto publish = [&]() { WAITV(0); if (lane == 0) set_flag(rW, OFF_FA + ((rc * 4 + cg) * 4 + jl) * 128 + wave * 8, tlo, thi); };
    if (wave >= 2) publish();

    const int wn = wave & 1, wt = wave >> 1;
    f32x4 acc[5][4];
#pragma unroll
    for (int nt = 0; nt < 5; ++nt)
#pragma unroll
      for (int tt = 0; tt < 4; ++tt) acc[nt][tt] = (f32x4){0.f, 0.f, 0.f, 0.f};
    struct Frag2 { bf16x8 x[4], w[5]; };
    Frag2 G0, G1;
    const unsigned ao = 4 * wt * 1024 + lane * 16;
    const unsigned twb = (4 * g + q4) * 320 + (p4 >> 1) * 16 + (p4 & 1) * 8;
    const unsigned twa = twb + (5 * wn + (g & 1)) * 32;
    const unsigned twl = (wn == 1 && (g & 1)) ? twb : twa + 128;
    const unsigned cwo = (80 * wn + r16) * 128 + ((g ^ (r16 >> 1)) * 16);
    const unsigned hxo = (p4 >> 1) * 2048 + (8 * g + q4) * 64 + (((2 * wt) ^ (((g & 1) << 1) | (p4 >> 1))) * 16) + (p4 & 1) * 8;
    constexpr int A0 = atile(0), A1 = atile(4), B1 = btile(0);
    unsigned ba[2] = {ao + A0, ao + A1}, bta[2] = {twa, twa + B1}, btl[2] = {twl, twl + B1};
    unsigned bc[4] = {cwo, cwo + B1, cwo ^ 64, (cwo ^ 64) + B1};
    unsigned bh[4] = {hxo + A0, hxo + A1, (hxo ^ 16) + A0, (hxo ^ 16) + A1};
    unsigned mb = SP_DF;
    asm volatile("" : "+v"(bh[0]), "+v"(bh[1]), "+v"(bh[2]), "+v"(bh[3]), "+v"(bc[0]), "+v"(bc[1]), "+v"(bc[2]), "+v"(bc[3]));
    asm volatile("" : "+v"(ba[0]), "+v"(ba[1]), "+v"(bta[0]), "+v"(bta[1]), "+v"(btl[0]), "+v"(btl[1]), "+v"(mb));
    auto half = [&](bf16x8& d, const int h, unsigned ad) {
      const s16x4 v = __builtin_amdgcn_ds_read_tr16_b64_v4i16((LAS s16x4*)(smem + ad));
      u32x4 t = __builtin_bit_cast(u32x4, d); const u32x2 vv = __builtin_bit_cast(u32x2, v);
      t[2 * h] = vv[0]; t[2 * h + 1] = vv[1];
      d = __builtin_bit_cast(bf16x8, t);
    };
    auto rd2 = [&](Frag2& N, const int n, const int r) {
      const int ha = (n >> 2) & 1, sa = atile(n) - (ha ? A1 : A0), hb = btile(n) >= B1, so = btile(n) - (hb ? B1 : 0);
      if (n >= 12) {
        if (r < 8) half(N.x[r >> 1], r & 1, bh[(r >> 2) * 2 + ha] + sa + ((r >> 1) & 1) * 4096 + (r & 1) * 256);
        else if (r < 13) N.w[r - 8] = *(const bf16x8*)(smem + bc[(n & 1) * 2 + hb] + so + (r - 8) * 2048);
      } else if (r < 4) N.x[r] = *(const bf16x8*)(smem + ba[ha] + sa + r * 1024);
      else if (r < 14) half(N.w[(r - 4) >> 1], r & 1, (r < 12 ? bta[hb] + 32 * ((r - 4) >> 1) : btl[hb]) + so + (r & 1) * 16 * 320);
    };
    auto wait_mark = [&](int st) {
      for (int sp = 0; *(volatile LAS unsigned*)(smem + mb + (st - 3) * 256) != tlo && sp < SPIN_LDS; ++sp) __builtin_amdgcn_s_sleep(1);
      asm volatile("" ::: "memory");
    };
    unsigned pb[2];
#pragma unroll
    for (int t2 = 0; t2 < 2; ++t2)
      pb[t2] = 2 * wt + t2 != cg ? OFF_PB + (((rc * 4 + jl) * 4 + 2 * wt + t2) * 4 + cg) * 10240 + wn * 5120 + lane * 16 : 0x7fff0000u;
    auto put_part = [&](const int nt) {
#pragma unroll
      for (int t2 = 0; t2 < 2; ++t2) {
        const f32x4 v0 = acc[nt][2 * t2], v1 = acc[nt][2 * t2 + 1];
        st128(rW, pb[t2] + nt * 1024, (u32x4){pack2(v0[0], v0[1]), pack2(v0[2], v0[3]), pack2(v1[0], v1[1]), pack2(v1[2], v1[3])});
      }
    };
    bool fok = false;
    auto step2 = [&](Frag2& C, Frag2& N, const int n) {
      if (n == 1 && wave < 2) publish();
      if (n >= 2 && n <= K0 && __builtin_expect(!fok, 0)) wait_mark(n + 1);
      unsigned fpre = 0u;
#pragma unroll
      for (int m = 0; m < 20; ++m) {
        SCHED;
        acc[m >> 2][m & 3] = __builtin_amdgcn_mfma_f32_16x16x32_bf16(C.w[m >> 2], C.x[m & 3], acc[m >> 2][m & 3], 0, 0, 0);
        SCHED;
        if (n < 21) {
          rd2(N, n + 1, m);
          if (m == 14 && n >= 1 && n < K0) fpre = *(volatile LAS unsigned*)(smem + mb + (n - 1) * 256);
          if (m == 17) __builtin_amdgcn_s_waitcnt(0xC07F);
          if (m == 18 && n == 1) lds_add(smem, M_OWN, lane);
          if (m == 18 && n == 4) lds_add(smem, M_R2, lane);
          if (m == 18 && n == 6) lds_add(smem, M_R3, lane);
          if (m == 18 && n >= 1 && n < K0) fok = __builtin_amdgcn_ballot_w64(fpre != tlo) == 0ull;
          if (m == 18 && n >= K0 && n < 20) asm volatile("s_barrier" ::: "memory");
        } else if (m >= 7 && m < 19 && (m & 3) == 3) put_part((m >> 2) - 1);
      }
      SCHED;
    };
    lds_wait_ge(misc + ((lane & 1) ? M_ACT : M_SRD), 4u);
    __builtin_amdgcn_s_setprio(1);
#pragma unroll
    for (int r = 0; r < 14; ++r) rd2(G0, 0, r);
    __builtin_amdgcn_s_waitcnt(0xC07F);
#pragma unroll
    for (int n = 0; n < 22; n += 2) {
      step2(G0, G1, n); step2(G1, G0, n + 1);
    }
    __builtin_amdgcn_s_setprio(0);
    put_part(3); put_part(4);
#pragma unroll
    for (int tt = 0; tt < 4; ++tt) {
      if (2 * wt + (tt >> 1) == cg) {
#pragma unroll
        for (int nt = 0; nt < 5; ++nt) *(f32x4*)(smem + atile(0) + ((5 * wn + nt) * 2 + (tt & 1)) * 1024 + lane * 16) = acc[nt][tt];
      }
    }
    asm volatile("s_waitcnt vmcnt(0) lgkmcnt(0)" ::: "memory");
    if (lane == 0) set_flag(rW, OFF_FR + (((rc * 4 + jl) * 2 + wt) * 2 + wn) * 128 + cg * 8, tlo, thi);
  }
}

__global__ void code_end() {}

extern "C" void kernel_launch(void* const* d_in, const int* in_sizes, int n_in,
                              void* d_out, int out_size, void* d_ws, size_t ws_size,
                              hipStream_t stream) {
  Args a;
  a.x   = (const u16*)d_in[0];
  a.Lre = (const u16*)d_in[1];
  a.Lim = (const u16*)d_in[2];
  a.Bre = (const u16*)d_in[3];
  a.Bim = (const u16*)d_in[4];
  a.Cre = (const u16*)d_in[5];
  a.Cim = (const u16*)d_in[6];
  a.ldt = (const u16*)d_in[7];
  a.Dsk = (const u16*)d_in[8];
  a.w1  = (const u16*)d_in[10];
  a.b1  = (const u16*)d_in[11];
  a.w2  = (const u16*)d_in[12];
  a.gw  = (const u16*)d_in[13];
  a.ws  = (char*)d_ws + ((ws_size - WS_BYTES) & ~(size_t)((2u << 20) - 1));
  a.out = (float*)d_out;
  fused<<<256, 512, 0, stream>>>(a);
}
```
